```python
import torch

def _softmax(x, axis=-1):
    return torch.softmax(x, dim=axis)
def _gelu(x):
    return torch.nn.functional.gelu(x, approximate="tanh")
def _silu(x):
    return torch.nn.functional.silu(x)
def _sigmoid(x):
    return torch.sigmoid(x)
def _layernorm(x, axis=-1, eps=1e-5):
    m = x.mean(dim=axis, keepdim=True)
    v = x.var(dim=axis, keepdim=True, unbiased=False)
    return (x - m) / torch.sqrt(v + eps)
def _scatter_add(tgt, idx, src, axis=0):
    return tgt.clone().index_add_(axis, idx, src)
def _segment_sum(src, seg, n):
    out = torch.zeros((n,) + src.shape[1:], dtype=src.dtype)
    return out.index_add_(0, seg, src)
def _pad(x, pw):
    flat = tuple(v for pair in reversed(pw) for v in pair)
    return torch.nn.functional.pad(x, flat)

kernel_name = 'interleave_layers__linear_kernel_hedgehog__sliding_block__prompt'

def setup_inputs():

    _w = (lambda a: (lambda t: t.to(torch.bfloat16) if t.is_floating_point() else t)(torch.as_tensor(a)))
    def _A_setup():
        B, S, D, H = 1, 2048, 448, 16
        Dh = D // H
        return {
            "x": _w((lambda *s: torch.randn(*s, dtype=torch.bfloat16) * 0.05)(B, S, D)),
            "wqkv": _w((lambda *s: torch.randn(*s, dtype=torch.bfloat16) * 0.4)(D, 3 * D)),
            "w_phi": _w((lambda *s: torch.randn(*s, dtype=torch.bfloat16) * 0.4)(H, Dh, Dh) * (Dh ** -0.5)),
            "wo": _w((lambda *s: torch.randn(*s, dtype=torch.bfloat16) * 0.05)(D, D)),
        }
    _A_in = _A_setup()

    def _B_setup():
        B, S, D = 1, 2048, 448
        return {
            "x": _w((lambda *s: torch.randn(*s, dtype=torch.bfloat16) * 0.05)(B, S, D)),
            "wqkv": _w((lambda *s: torch.randn(*s, dtype=torch.bfloat16) * 0.1)(D, 3 * D)),
            "wo": _w((lambda *s: torch.randn(*s, dtype=torch.bfloat16) * 0.05)(D, D)),
        }
    _B_in = _B_setup()

    return {
        "A_x": _A_in["x"],
        "A_wqkv": _A_in["wqkv"],
        "A_w_phi": _A_in["w_phi"],
        "A_wo": _A_in["wo"],
        "B_x": _B_in["x"],
        "B_wqkv": _B_in["wqkv"],
        "B_wo": _B_in["wo"],
    }

def reference(A_x, A_wqkv, A_w_phi, A_wo, B_x, B_wqkv, B_wo):
    def _A_ref(x, wqkv, w_phi, wo):
        B, S, D = x.shape
        H = 16; Dh = D // H
        qkv = (lambda x, *d: x.permute(*d))((x @ wqkv).reshape(B, S, 3, H, Dh), 2, 0, 3, 1, 4)
        q, k, v = qkv[0], qkv[1], qkv[2]
        pq = torch.einsum("bhsd,hdf->bhsf", q, w_phi)
        pk = torch.einsum("bhsd,hdf->bhsf", k, w_phi)
        phi_q = torch.concatenate([_softmax(pq, axis=-1), _softmax(-pq, axis=-1)], axis=-1)
        phi_k = torch.concatenate([_softmax(pk, axis=-1), _softmax(-pk, axis=-1)], axis=-1)
        kv = torch.einsum("bhsf,bhsd->bhfd", phi_k, v)
        z  = torch.einsum("bhsf->bhf", phi_k)
        num = torch.einsum("bhqf,bhfd->bhqd", phi_q, kv)
        den = torch.einsum("bhqf,bhf->bhq", phi_q, z)[..., None] + 1e-6
        o = num / den
        return (lambda x, *d: x.permute(*d))(o, 0, 2, 1, 3).reshape(B, S, D) @ wo

    def _B_ref(x, wqkv, wo):
        B, S, D = x.shape
        H = 16; Dh = D // H; W = 256
        qkv = (lambda x, *d: x.permute(*d))((x @ wqkv).reshape(B, S, 3, H, Dh), 2, 0, 3, 1, 4)
        q, k, v = qkv[0], qkv[1], qkv[2]
        scores = torch.einsum("bhqd,bhkd->bhqk", q, k) * (Dh ** -0.5)
        idx = torch.arange(S)
        mask = (idx[:, None] - idx[None, :] >= 0) & (idx[:, None] - idx[None, :] < W)
        scores = torch.where(mask[None, None], scores, -1e9)
        p = _softmax(scores, axis=-1)
        o = torch.einsum("bhqk,bhkd->bhqd", p, v)
        return (lambda x, *d: x.permute(*d))(o, 0, 2, 1, 3).reshape(B, S, D) @ wo

    x = A_x
    x = x + _A_ref(_layernorm(x, axis=-1), A_wqkv, A_w_phi, A_wo)
    x = x + _B_ref(_layernorm(x, axis=-1), B_wqkv, B_wo)
    x = x + _A_ref(_layernorm(x, axis=-1), A_wqkv, A_w_phi, A_wo)
    x = x + _B_ref(_layernorm(x, axis=-1), B_wqkv, B_wo)
    return x
```

```cpp
#include <hip/hip_runtime.h>
#include <hip/hip_bf16.h>
typedef __hip_bfloat16 bf16;
#define S_ 2048
#define D_ 448
#define H_ 16
#define DH 28
__device__ inline float b2f(bf16 x){return __bfloat162float(x);}
__device__ inline float rb(float x){return __bfloat162float(__float2bfloat16(x));}
__global__ void k_ln(const bf16* x, bf16* y){
  int r=blockIdx.x; int l=threadIdx.x;
  float v[7]; float s=0;
  for(int i=0;i<7;i++){v[i]=b2f(x[r*D_+l+64*i]); s+=v[i];}
  for(int m=32;m>=1;m>>=1) s+=__shfl_xor(s,m);
  float mean=s/D_; float q=0;
  for(int i=0;i<7;i++){float d=v[i]-mean; q+=d*d;}
  for(int m=32;m>=1;m>>=1) q+=__shfl_xor(q,m);
  float var=q/D_;
  float rs=1.f/sqrtf(var+1e-5f);
  for(int i=0;i<7;i++) y[r*D_+l+64*i]=__float2bfloat16((v[i]-mean)*rs);
}
__global__ void k_gemm(const bf16* A,const bf16* B,const bf16* R,bf16* C,int N,int K){
  __shared__ float As[16][17], Bs[16][17];
  int tx=threadIdx.x%16, ty=threadIdx.x/16;
  int row=blockIdx.y*16+ty, col=blockIdx.x*16+tx;
  float acc=0;
  for(int k0=0;k0<K;k0+=16){
    As[ty][tx]=b2f(A[row*K+k0+tx]);
    Bs[ty][tx]=b2f(B[(k0+ty)*N+col]);
    __syncthreads();
    for(int k=0;k<16;k++) acc+=As[ty][k]*Bs[k][tx];
    __syncthreads();
  }
  if(R) acc=rb(acc)+b2f(R[row*N+col]);
  C[row*N+col]=__float2bfloat16(acc);
}
__global__ void k_phi(const bf16* qkv,const bf16* wphi,float* phi){
  int idx=blockIdx.x*blockDim.x+threadIdx.x;
  int s=idx%S_; int h=(idx/S_)%H_; int w=idx/(S_*H_);
  float p[DH];
  for(int f=0;f<DH;f++) p[f]=0;
  for(int d=0;d<DH;d++){ float q=b2f(qkv[s*3*D_+w*D_+h*DH+d]);
    for(int f=0;f<DH;f++) p[f]+=q*b2f(wphi[(h*DH+d)*DH+f]); }
  float mx=-1e30f,mn=1e30f;
  for(int f=0;f<DH;f++){p[f]=rb(p[f]); mx=fmaxf(mx,p[f]); mn=fminf(mn,p[f]);}
  float s1=0,s2=0;
  for(int f=0;f<DH;f++){s1+=__expf(p[f]-mx); s2+=__expf(mn-p[f]);}
  float* o=phi+(size_t)idx*56;
  for(int f=0;f<DH;f++){o[f]=rb(__expf(p[f]-mx)/s1); o[DH+f]=rb(__expf(mn-p[f])/s2);}
}
__global__ void k_kv(const bf16* qkv,const float* phi,float* kv){
  int h=blockIdx.x; int f=blockIdx.y; int t=threadIdx.x;
  const float* pk=phi+(size_t)(H_+h)*S_*56;
  __shared__ float red[256];
  for(int d=0;d<29;d++){
    float a=0;
    for(int s=t;s<S_;s+=256){ float v=(d<DH)?b2f(qkv[s*3*D_+2*D_+h*DH+d]):1.f; a+=pk[s*56+f]*v; }
    red[t]=a; __syncthreads();
    for(int m=128;m>=1;m>>=1){ if(t<m) red[t]+=red[t+m]; __syncthreads(); }
    if(t==0) kv[(h*56+f)*29+d]=rb(red[0]);
    __syncthreads();
  }
}
__global__ void k_o(const float* phi,const float* kv,bf16* o){
  int idx=blockIdx.x*blockDim.x+threadIdx.x;
  int s=idx%S_; int h=idx/S_;
  const float* pq=phi+(size_t)(h*S_+s)*56;
  float den=0; for(int f=0;f<56;f++) den+=pq[f]*kv[(h*56+f)*29+28];
  den=rb(rb(den)+1e-6f);
  for(int d=0;d<DH;d++){ float a=0; for(int f=0;f<56;f++) a+=pq[f]*kv[(h*56+f)*29+d];
    o[s*D_+h*DH+d]=__float2bfloat16(rb(a)/den); }
}
__global__ void k_attn(const bf16* qkv,bf16* o){
  int s=blockIdx.x,h=blockIdx.y,t=threadIdx.x;
  __shared__ float p[256]; __shared__ float red[256];
  int j=s-t; float sc=-1e30f;
  if(j>=0){ float a=0; for(int d=0;d<DH;d++) a+=b2f(qkv[s*3*D_+h*DH+d])*b2f(qkv[j*3*D_+D_+h*DH+d]);
    sc=rb(rb(a)*0.18898223650461357f);}
  red[t]=sc; __syncthreads();
  for(int m=128;m>=1;m>>=1){ if(t<m) red[t]=fmaxf(red[t],red[t+m]); __syncthreads(); }
  float mx=red[0]; __syncthreads();
  float e=(j>=0)?__expf(sc-mx):0.f; red[t]=e; __syncthreads();
  for(int m=128;m>=1;m>>=1){ if(t<m) red[t]+=red[t+m]; __syncthreads(); }
  float sum=red[0];
  p[t]=rb(e/sum); __syncthreads();
  if(t<DH){ float a=0; for(int i=0;i<256&&s-i>=0;i++) a+=p[i]*b2f(qkv[(s-i)*3*D_+2*D_+h*DH+t]); o[s*D_+h*DH+t]=__float2bfloat16(a);}
}
extern "C" void kernel_launch(void* const* d_in, const int* in_sizes, int n_in, void* d_out, int out_size, void* d_ws, size_t ws_size, hipStream_t stream) {
  const bf16* x=(const bf16*)d_in[0]; const bf16* Aw=(const bf16*)d_in[1]; const bf16* Aphi=(const bf16*)d_in[2]; const bf16* Awo=(const bf16*)d_in[3];
  const bf16* Bw=(const bf16*)d_in[5]; const bf16* Bwo=(const bf16*)d_in[6];
  char* ws=(char*)d_ws;
  bf16* xn=(bf16*)ws; bf16* qkv=(bf16*)(ws+(4<<20)); bf16* o=(bf16*)(ws+(12<<20)); bf16* xa=(bf16*)(ws+(16<<20)); bf16* xb=(bf16*)(ws+(20<<20));
  float* phi=(float*)(ws+(24<<20)); float* kv=(float*)(ws+(48<<20));
  const bf16* cur=x;
  for(int l=0;l<4;l++){
    bf16* nxt=(l==3)?(bf16*)d_out:((l&1)?xb:xa);
    k_ln<<<S_,64,0,stream>>>(cur,xn);
    k_gemm<<<dim3(1344/16,S_/16),256,0,stream>>>(xn,(l&1)?Bw:Aw,nullptr,qkv,1344,448);
    if(!(l&1)){
      k_phi<<<2*H_*S_/64,64,0,stream>>>(qkv,Aphi,phi);
      k_kv<<<dim3(H_,56),256,0,stream>>>(qkv,phi,kv);
      k_o<<<H_*S_/64,64,0,stream>>>(phi,kv,o);
    } else k_attn<<<dim3(S_,H_),256,0,stream>>>(qkv,o);
    k_gemm<<<dim3(448/16,S_/16),256,0,stream>>>(o,(l&1)?Bwo:Awo,cur,nxt,448,448);
    cur=nxt;
  }
}
```

```cpp
#include <hip/hip_runtime.h>
#include <hip/hip_bf16.h>
typedef __hip_bfloat16 bf16;
#define S_ 2048
#define D_ 448
#define H_ 16
#define DH 28
__device__ inline float b2f(bf16 x){return __bfloat162float(x);}
__device__ inline float rb(float x){return x;}
__device__ inline float b2f(float x){return x;}
template<class T> __global__ void k_ln(const T* x, float* y){
  int r=blockIdx.x; int l=threadIdx.x;
  float v[7]; float s=0;
  for(int i=0;i<7;i++){v[i]=b2f(x[r*D_+l+64*i]); s+=v[i];}
  for(int m=32;m>=1;m>>=1) s+=__shfl_xor(s,m);
  float mean=s/D_; float q=0;
  for(int i=0;i<7;i++){float d=v[i]-mean; q+=d*d;}
  for(int m=32;m>=1;m>>=1) q+=__shfl_xor(q,m);
  float var=q/D_;
  float rs=1.f/sqrtf(var+1e-5f);
  for(int i=0;i<7;i++) y[r*D_+l+64*i]=((v[i]-mean)*rs);
}
template<class TR,class TC> __global__ void k_gemm(const float* A,const bf16* B,const TR* R,TC* C,int N,int K){
  __shared__ float As[16][17], Bs[16][17];
  int tx=threadIdx.x%16, ty=threadIdx.x/16;
  int row=blockIdx.y*16+ty, col=blockIdx.x*16+tx;
  float acc=0;
  for(int k0=0;k0<K;k0+=16){
    As[ty][tx]=b2f(A[row*K+k0+tx]);
    Bs[ty][tx]=b2f(B[(k0+ty)*N+col]);
    __syncthreads();
    for(int k=0;k<16;k++) acc+=As[ty][k]*Bs[k][tx];
    __syncthreads();
  }
  if(R) acc=rb(acc)+b2f(R[row*N+col]);
  C[row*N+col]=(TC)(acc);
}
__global__ void k_phi(const float* qkv,const bf16* wphi,float* phi){
  int idx=blockIdx.x*blockDim.x+threadIdx.x;
  int s=idx%S_; int h=(idx/S_)%H_; int w=idx/(S_*H_);
  float p[DH];
  for(int f=0;f<DH;f++) p[f]=0;
  for(int d=0;d<DH;d++){ float q=b2f(qkv[s*3*D_+w*D_+h*DH+d]);
    for(int f=0;f<DH;f++) p[f]+=q*b2f(wphi[(h*DH+d)*DH+f]); }
  float mx=-1e30f,mn=1e30f;
  for(int f=0;f<DH;f++){p[f]=rb(p[f]); mx=fmaxf(mx,p[f]); mn=fminf(mn,p[f]);}
  float s1=0,s2=0;
  for(int f=0;f<DH;f++){s1+=__expf(p[f]-mx); s2+=__expf(mn-p[f]);}
  float* o=phi+(size_t)idx*56;
  for(int f=0;f<DH;f++){o[f]=rb(__expf(p[f]-mx)/s1); o[DH+f]=rb(__expf(mn-p[f])/s2);}
}
__global__ void k_kv(const float* qkv,const float* phi,float* kv){
  int h=blockIdx.x; int f=blockIdx.y; int t=threadIdx.x;
  const float* pk=phi+(size_t)(H_+h)*S_*56;
  __shared__ float red[256];
  for(int d=0;d<29;d++){
    float a=0;
    for(int s=t;s<S_;s+=256){ float v=(d<DH)?b2f(qkv[s*3*D_+2*D_+h*DH+d]):1.f; a+=pk[s*56+f]*v; }
    red[t]=a; __syncthreads();
    for(int m=128;m>=1;m>>=1){ if(t<m) red[t]+=red[t+m]; __syncthreads(); }
    if(t==0) kv[(h*56+f)*29+d]=rb(red[0]);
    __syncthreads();
  }
}
__global__ void k_o(const float* phi,const float* kv,float* o){
  int idx=blockIdx.x*blockDim.x+threadIdx.x;
  int s=idx%S_; int h=idx/S_;
  const float* pq=phi+(size_t)(h*S_+s)*56;
  float den=0; for(int f=0;f<56;f++) den+=pq[f]*kv[(h*56+f)*29+28];
  den=rb(rb(den)+1e-6f);
  for(int d=0;d<DH;d++){ float a=0; for(int f=0;f<56;f++) a+=pq[f]*kv[(h*56+f)*29+d];
    o[s*D_+h*DH+d]=(rb(a)/den); }
}
__global__ void k_attn(const float* qkv,float* o){
  int s=blockIdx.x,h=blockIdx.y,t=threadIdx.x;
  __shared__ float p[256]; __shared__ float red[256];
  int j=s-t; float sc=-1e30f;
  if(j>=0){ float a=0; for(int d=0;d<DH;d++) a+=b2f(qkv[s*3*D_+h*DH+d])*b2f(qkv[j*3*D_+D_+h*DH+d]);
    sc=rb(rb(a)*0.18898223650461357f);}
  red[t]=sc; __syncthreads();
  for(int m=128;m>=1;m>>=1){ if(t<m) red[t]=fmaxf(red[t],red[t+m]); __syncthreads(); }
  float mx=red[0]; __syncthreads();
  float e=(j>=0)?__expf(sc-mx):0.f; red[t]=e; __syncthreads();
  for(int m=128;m>=1;m>>=1){ if(t<m) red[t]+=red[t+m]; __syncthreads(); }
  float sum=red[0];
  p[t]=rb(e/sum); __syncthreads();
  if(t<DH){ float a=0; for(int i=0;i<256&&s-i>=0;i++) a+=p[i]*b2f(qkv[(s-i)*3*D_+2*D_+h*DH+t]); o[s*D_+h*DH+t]=(a);}
}
extern "C" void kernel_launch(void* const* d_in, const int* in_sizes, int n_in, void* d_out, int out_size, void* d_ws, size_t ws_size, hipStream_t stream) {
  const bf16* x=(const bf16*)d_in[0]; const bf16* Aw=(const bf16*)d_in[1]; const bf16* Aphi=(const bf16*)d_in[2]; const bf16* Awo=(const bf16*)d_in[3];
  const bf16* Bw=(const bf16*)d_in[5]; const bf16* Bwo=(const bf16*)d_in[6];
  char* ws=(char*)d_ws;
  float* xn=(float*)ws; float* qkv=(float*)(ws+(8<<20)); float* o=(float*)(ws+(24<<20)); float* xa=(float*)(ws+(32<<20)); float* xb=(float*)(ws+(40<<20));
  float* phi=(float*)(ws+(48<<20)); float* kv=(float*)(ws+(80<<20));
  const float* cur=nullptr;
  for(int l=0;l<4;l++){
    float* nxt=((l&1)?xb:xa);
    if(l==0) k_ln<bf16><<<S_,64,0,stream>>>(x,xn); else k_ln<float><<<S_,64,0,stream>>>(cur,xn);
    k_gemm<float,float><<<dim3(1344/16,S_/16),256,0,stream>>>(xn,(l&1)?Bw:Aw,nullptr,qkv,1344,448);
    if(!(l&1)){
      k_phi<<<2*H_*S_/64,64,0,stream>>>(qkv,Aphi,phi);
      k_kv<<<dim3(H_,56),256,0,stream>>>(qkv,phi,kv);
      k_o<<<H_*S_/64,64,0,stream>>>(phi,kv,o);
    } else k_attn<<<dim3(S_,H_),256,0,stream>>>(qkv,o);
    if(l==0) k_gemm<bf16,float><<<dim3(448/16,S_/16),256,0,stream>>>(o,(l&1)?Bwo:Awo,x,nxt,448,448);
    else if(l==3) k_gemm<float,bf16><<<dim3(448/16,S_/16),256,0,stream>>>(o,(l&1)?Bwo:Awo,cur,(bf16*)d_out,448,448);
    else k_gemm<float,float><<<dim3(448/16,S_/16),256,0,stream>>>(o,(l&1)?Bwo:Awo,cur,nxt,448,448);
    cur=nxt;
  }
}
```

```cpp
#include <hip/hip_runtime.h>
#include <stdint.h>

#define LAS __attribute__((address_space(3)))
#define DEV __device__ __forceinline__
typedef unsigned v4u __attribute__((ext_vector_type(4)));
typedef unsigned v2u __attribute__((ext_vector_type(2)));
typedef float v4f __attribute__((ext_vector_type(4)));
typedef float v2f __attribute__((ext_vector_type(2)));
typedef short v4s __attribute__((ext_vector_type(4)));
typedef __bf16 v8bf __attribute__((ext_vector_type(8)));
typedef __bf16 v2bf __attribute__((ext_vector_type(2)));
typedef __amdgpu_buffer_rsrc_t rsrc_t;

#define TPX 2
#define DLY_O 8
#define DLY_X 4

namespace {
constexpr int SEQ = 2048, DM = 448;
constexpr unsigned L_WK = 0, L_WV = 28672, L_WQ = 57344, L_WO = 86016, L_SCR = 114688, L_TOTAL = 163840;
constexpr unsigned L_CS = L_SCR + 23 * 2048;
constexpr unsigned L_KVF = L_SCR + 24576;
constexpr unsigned TILEBUF = 16 * 8 * 14 * 1024;
constexpr unsigned W_FLAG = 4096, W_O = 65536, W_X = W_O + 4 * TILEBUF, W_KVP = W_X + 3 * TILEBUF, W_HALO = W_KVP + 2 * 1048576,
                   W_END = W_HALO + 2 * 4194304;
constexpr unsigned W_DBG = 32u << 20;
constexpr int SC1 = 16;
constexpr unsigned SENT = 0xffffffffu;

DEV v4f mfma(v4u a, v4u b, v4f c) {
  return __builtin_amdgcn_mfma_f32_16x16x32_bf16(__builtin_bit_cast(v8bf, a), __builtin_bit_cast(v8bf, b), c, 0, 0, 0);
}
DEV unsigned pk(float a, float b) { v2f f = {a, b}; v2bf r = __builtin_convertvector(f, v2bf); return __builtin_bit_cast(unsigned, r); }
typedef LAS char* lptr;
DEV lptr lbase(unsigned v) { asm volatile("" : "+v"(v)); return (lptr)(size_t)v; }
DEV v2u trd(lptr a) { v4s r = __builtin_amdgcn_ds_read_tr16_b64_v4i16((LAS v4s*)a); return __builtin_bit_cast(v2u, r); }
DEV v4u cat(v2u a, v2u b) { return v4u{a.x, a.y, b.x, b.y}; }
DEV v4u wfrag(lptr base, int s) { return cat(trd(base + s * 2048), trd(base + s * 2048 + 256)); }
DEV float asf(unsigned u) { return __builtin_bit_cast(float, u); }
DEV unsigned asu(float f) { return __builtin_bit_cast(unsigned, f); }
DEV void wg_bar() { asm volatile("s_waitcnt lgkmcnt(0)\n\ts_barrier" ::: "memory"); }
DEV void drain() { asm volatile("s_waitcnt vmcnt(0)" ::: "memory"); }
DEV void fence() { asm volatile("" ::: "memory"); }
DEV float ex2(float x) { return __builtin_amdgcn_exp2f(x); }

DEV void xmax2(float& a, float& b) {
  auto r = __builtin_amdgcn_permlane32_swap(asu(a), asu(b), false, false);
  unsigned r0 = r[0], r1 = r[1];
  float z = fmaxf(asf(r0), asf(r1));
  auto q = __builtin_amdgcn_permlane16_swap(asu(z), asu(z), false, false);
  unsigned q0 = q[0], q1 = q[1];
  z = fmaxf(asf(q0), asf(q1));
  auto p = __builtin_amdgcn_permlane32_swap(asu(z), asu(z), false, false);
  unsigned p0 = p[0], p1 = p[1];
  a = asf(p0); b = asf(p1);
}
DEV void xsum2(float& a, float& b) {
  auto r = __builtin_amdgcn_permlane32_swap(asu(a), asu(b), false, false);
  unsigned r0 = r[0], r1 = r[1];
  float z = asf(r0) + asf(r1);
  auto q = __builtin_amdgcn_permlane16_swap(asu(z), asu(z), false, false);
  unsigned q0 = q[0], q1 = q[1];
  z = asf(q0) + asf(q1);
  auto p = __builtin_amdgcn_permlane32_swap(asu(z), asu(z), false, false);
  unsigned p0 = p[0], p1 = p[1];
  a = asf(p0); b = asf(p1);
}
DEV float xmax1(float a) {
  auto r = __builtin_amdgcn_permlane32_swap(asu(a), asu(a), false, false);
  unsigned r0 = r[0], r1 = r[1];
  float z = fmaxf(asf(r0), asf(r1));
  auto q = __builtin_amdgcn_permlane16_swap(asu(z), asu(z), false, false);
  unsigned q0 = q[0], q1 = q[1];
  return fmaxf(asf(q0), asf(q1));
}
DEV float bcast_row3(float a) {
  auto r = __builtin_amdgcn_permlane32_swap(asu(a), asu(a), false, false);
  unsigned r1 = r[1];
  auto q = __builtin_amdgcn_permlane16_swap(r1, r1, false, false);
  unsigned q1 = q[1];
  return asf(q1);
}
DEV void dma16(unsigned ldsdst, unsigned voff, v4u srd, unsigned soff) {
  unsigned keep;
  asm volatile("s_nop 4\n\ts_mov_b32 %0, m0\n\ts_mov_b32 m0, %1\n\ts_nop 0\n\tbuffer_load_dwordx4 %2, %3, %4 offen lds\n\ts_mov_b32 m0, %0"
               : "=&s"(keep) : "s"(ldsdst), "v"(voff), "s"(srd), "s"(soff) : "memory");
}
DEV v4u mk_srd(const void* p, unsigned bytes) {
  unsigned long long a = (unsigned long long)p;
  return v4u{(unsigned)a, (unsigned)(a >> 32) & 0xffffu, bytes, 0x00020000u};
}
DEV bool has_sent(v4u a) { return max(max(a.x, a.y), max(a.z, a.w)) == SENT; }
DEV bool has_sent2(v4u a, v4u b) { return max(max(max(a.x, a.y), max(a.z, a.w)), max(max(b.x, b.y), max(b.z, b.w))) == SENT; }

DEV void respin2(v4u& a, v4u& b, v4u srd, unsigned voff, unsigned sa, unsigned sb, unsigned& budget) {
  while (budget != 0u) {
    --budget;
    asm volatile("s_sleep 2\n\tbuffer_load_dwordx4 %0, %2, %3, %4 offen sc1\n\tbuffer_load_dwordx4 %1, %2, %3, %5 offen sc1\n\ts_waitcnt vmcnt(0)"
                 : "=&v"(a), "=&v"(b) : "v"(voff), "s"(srd), "s"(sa), "s"(sb) : "memory");
    if (!__any(has_sent2(a, b))) break;
  }
}

DEV void feature_map(v4u vf, const v4u (&wphi)[2], bool g3, v4u& plus, v4u& minus) {
  const v4f z4 = {0.f, 0.f, 0.f, 0.f};
  v4f p0 = mfma(wphi[0], vf, z4), p1 = mfma(wphi[1], vf, z4);
  if (g3) p1 = p0;
  float mx = fmaxf(fmaxf(fmaxf(p0.x, p0.y), fmaxf(p0.z, p0.w)), fmaxf(fmaxf(p1.x, p1.y), fmaxf(p1.z, p1.w)));
  float mn = -fminf(fminf(fminf(p0.x, p0.y), fminf(p0.z, p0.w)), fminf(fminf(p1.x, p1.y), fminf(p1.z, p1.w)));
  xmax2(mx, mn);
  constexpr float L2E = 1.4426950408889634f;
  const float a = -mx * L2E, b = -mn * L2E;
  float e[8], f[8];
#pragma unroll
  for (int i = 0; i < 4; ++i) {
    e[i] = ex2(fmaf(p0[i], L2E, a)); f[i] = ex2(fmaf(p0[i], -L2E, b));
    e[4 + i] = ex2(fmaf(p1[i], L2E, a)); f[4 + i] = ex2(fmaf(p1[i], -L2E, b));
  }
  float se = (e[0] + e[1]) + (e[2] + e[3]), sf = (f[0] + f[1]) + (f[2] + f[3]);
  float se1 = (e[4] + e[5]) + (e[6] + e[7]), sf1 = (f[4] + f[5]) + (f[6] + f[7]);
  if (!g3) { se += se1; sf += sf1; }
  xsum2(se, sf);
  const float re = __builtin_amdgcn_rcpf(se), rf = __builtin_amdgcn_rcpf(sf);
  const float re1 = g3 ? 0.f : re, rf1 = g3 ? 0.f : rf;
  plus = v4u{pk(e[0] * re, e[1] * re), pk(e[2] * re, e[3] * re), pk(e[4] * re1, e[5] * re1), pk(e[6] * re1, e[7] * re1)};
  minus = v4u{pk(f[0] * rf, f[1] * rf), pk(f[2] * rf, f[3] * rf), pk(f[4] * rf1, f[5] * rf1), pk(f[6] * rf1, f[7] * rf1)};
}

DEV v4u pexp(v4f a, v4f b) {
  return v4u{pk(ex2(a.x), ex2(a.y)), pk(ex2(a.z), ex2(a.w)), pk(ex2(b.x), ex2(b.y)), pk(ex2(b.z), ex2(b.w))};
}

#define STAMP(i) do {} while (0)

struct Params {
  const void* x; const void* wA; const void* wphi; const void* woA; const void* wB; const void* woB; void* out; void* ws;
};

__global__ void __launch_bounds__(512, 2) fused_layers(Params p) {
  const int b = blockIdx.x, xg = b & 7, xi = b >> 3;
  constexpr int HPX = 32 / TPX, NGT = 16 / TPX;
  const int t = (xg % NGT) * TPX + xi / HPX;
  const int h = (xg / NGT) * HPX + xi % HPX;
  const int w = __builtin_amdgcn_readfirstlane(threadIdx.x >> 6);
  const int l = threadIdx.x & 63, c = l & 15, g = l >> 4;
  const bool g3 = g == 3;
  STAMP(0);
  const rsrc_t rs_ws = __builtin_amdgcn_make_buffer_rsrc(p.ws, 0, W_END, 0x00020000);
  const rsrc_t rs_x = __builtin_amdgcn_make_buffer_rsrc((void*)p.x, 0, SEQ * DM * 2, 0x00020000);
  const v4u srdA = mk_srd(p.wA, DM * 3 * DM * 2), srdB = mk_srd(p.wB, DM * 3 * DM * 2);
  const v4u srdW = mk_srd(p.ws, W_END);
  const v4u srdOA = mk_srd(p.woA, DM * DM * 2), srdOB = mk_srd(p.woB, DM * DM * 2);
  const unsigned long long epoch = *(const unsigned long long*)p.ws + 1ull;

  const unsigned chunk16 = ((l & 3) ^ ((l >> 5) << 1)) * 16;
  unsigned prow[4];
#pragma unroll
  for (int j = 0; j < 4; ++j) {
    const int i = w + 8 * j, n = 2 * i + (l >> 5), e = (l >> 2) & 7;
    const int hh = n / 3, gg = n - 3 * hh;
    prow[j] = n < 48 ? 28 * hh + 4 * gg + (e & 3) + 16 * (e >> 2) : 28 * (2 * (n - 48) + (e >> 2)) + 12 + (e & 3);
  }
  auto dma_part = [&](unsigned lds, v4u srd, unsigned colb, unsigned stride, bool natural) {
#pragma unroll
    for (int j = 0; j < 4; ++j) {
      const int i = w + 8 * j;
      if (i < 28) {
        const unsigned row = natural ? (unsigned)(16 * i + (l >> 2)) : prow[j];
        dma16(lds + i * 1024, row * stride + chunk16, srd, colb);
      }
    }
  };
  auto patch_k = [&]() {
    if (chunk16 == 48) {
#pragma unroll
      for (int j = 0; j < 4; ++j) {
        const int i = w + 8 * j;
        if (i < 28) *(LAS v2u*)(lbase(L_WK + w * 1024 + l * 16) + j * 8192 + 8) = v2u{0x3f80u, 0u};
      }
    }
  };
  const v4u ones4 = {SENT, SENT, SENT, SENT};
  auto init_tile = [&](unsigned off) {
    if (w < 7) __builtin_amdgcn_raw_buffer_store_b128(ones4, rs_ws, l * 16, off + t * 114688 + h * 7168 + w * 1024, SC1);
  };
  auto init_halo = [&](unsigned off) {
    __builtin_amdgcn_raw_buffer_store_b128(ones4, rs_ws, l * 16, off + ((h * 16 + t) * 8 + w) * 2048, SC1);
    __builtin_amdgcn_raw_buffer_store_b128(ones4, rs_ws, l * 16, off + ((h * 16 + t) * 8 + w) * 2048 + 1024, SC1);
  };

  dma_part(L_WK, srdA, (DM + 28 * h) * 2, 3 * DM * 2, true);
  dma_part(L_WV, srdA, (2 * DM + 28 * h) * 2, 3 * DM * 2, true);
  dma_part(L_WQ, srdA, (28 * h) * 2, 3 * DM * 2, true);
  if (w < 2) {
    const int rho = 16 * w + (l >> 2), gg = rho >> 3, e = rho & 7, d = 4 * gg + (e & 3) + 16 * (e >> 2);
    dma16(L_SCR + w * 1024, d < 28 ? d * 56 + chunk16 : 0x40000000u, mk_srd(p.wphi, 16 * 28 * 28 * 2), h * 28 * 28 * 2);
  }

  const unsigned q4 = (l & 15) >> 2, p4 = l & 3;
  const unsigned wb0 = (8 * g + q4) * 64 + (((p4 >> 1) ^ ((g & 1) << 1)) * 16) + (p4 & 1) * 8;
  const lptr wk0 = lbase(wb0), wk1 = lbase(wb0 ^ 32), wq0 = lbase(L_WQ + wb0), wq1 = lbase(L_WQ + (wb0 ^ 32));
  const lptr stw = lbase(L_SCR + c * 32 + ((g ^ (c >> 2)) * 8));
  const lptr str = lbase(L_SCR + (4 * g + q4) * 32 + ((p4 ^ g) * 8));
  const lptr sl16 = lbase(L_SCR + l * 16), csg = lbase(L_CS + g * 16);
  const int nch = g3 ? 48 + (h >> 1) : 3 * h + g;
  const unsigned pub = (nch >> 2) * 1024 + ((nch & 3) * 16 + c) * 16 + (g3 ? (h & 1) * 8 : 0);
  const unsigned tw = (t * 8 + w) * 14336;
  const int tok = t * 128 + w * 16 + c;

  v4f res0, res1;
  v4u wphi[2];
  unsigned budget = 1u << 16;
  v4u xf[14];

  for (int layer = 0; layer < 4; ++layer) {
    const bool isA = !(layer & 1), first = layer == 0;
    const rsrc_t rsx = first ? rs_x : rs_ws;
    const unsigned xv = first ? tok * (DM * 2) + g * 16 : l * 16;
    const unsigned xs = first ? 0u : W_X + (layer - 1) * TILEBUF + tw, xstep = first ? 64u : 1024u;
    if (first) {
#pragma unroll
      for (int s = 0; s < 14; ++s) xf[s] = __builtin_amdgcn_raw_buffer_load_b128(rsx, xv, xs + s * xstep, SC1);
      const v2u r0 = __builtin_amdgcn_raw_buffer_load_b64(rs_x, tok * (DM * 2) + g * 8, 56 * h, 0);
      const v2u r1 = __builtin_amdgcn_raw_buffer_load_b64(rs_x, tok * (DM * 2) + g * 8 + 32, 56 * h, 0);
      asm volatile("s_waitcnt vmcnt(16)" ::: "memory");
      patch_k();
      dma_part(L_WO, srdOA, 56 * h, DM * 2, false);
      init_tile(W_O);
      __builtin_amdgcn_raw_buffer_store_b128(ones4, rs_ws, l * 16, W_KVP + (w >> 2) * 1048576 + (h * 16 + t) * 4096 + (w & 3) * 1024, SC1);
      init_halo(W_HALO);
      res0 = v4f{asf(r0.x << 16), asf(r0.x & 0xffff0000u), asf(r0.y << 16), asf(r0.y & 0xffff0000u)};
      res1 = v4f{asf(r1.x << 16), asf(r1.x & 0xffff0000u), asf(r1.y << 16), asf(r1.y & 0xffff0000u)};
      wg_bar();
      wphi[0] = wfrag(wq0 + (L_SCR - L_WQ), 0); wphi[1] = wfrag(wq1 + (L_SCR - L_WQ), 0);
    }
    STAMP(1 + layer * 12);
    if (w < 6) {
      const lptr base = lbase((w >> 1) * 28672 + (wb0 ^ ((w & 1) * 32)));
      const v4u one = {0x3f803f80u, 0x3f803f80u, 0x3f803f80u, 0x3f803f80u};
      v4f cs = {0.f, 0.f, 0.f, 0.f};
#pragma unroll
      for (int s = 0; s < 14; ++s) cs = mfma(wfrag(base, s), one, cs);
      if (c == 0) *(LAS v4f*)(csg + w * 64) = cs;
    }
    if (!first) {
      __builtin_amdgcn_s_sleep(DLY_X);
      fence();
#pragma unroll
      for (int s = 0; s < 14; ++s) xf[s] = __builtin_amdgcn_raw_buffer_load_b128(rsx, xv, xs + s * xstep, SC1);
    }
    STAMP(2 + layer * 12);
    v4f k0 = {0.f, 0.f, 0.f, 0.f}, k1 = k0, v0 = k0, v1 = k0, gram = k0;
    {
      v4u a[8];
#pragma unroll
      for (int i = 0; i < 2; ++i) { a[4 * i] = wfrag(wk0, i); a[4 * i + 1] = wfrag(wk1, i); a[4 * i + 2] = wfrag(wk0 + L_WV, i); a[4 * i + 3] = wfrag(wk1 + L_WV, i); }
#pragma unroll
      for (int s = 0; s < 14; s += 2) {
        if (__builtin_expect(__any(has_sent2(xf[s], xf[s + 1])), 0) && !first) {
          respin2(xf[s], xf[s + 1], srdW, l * 16, xs + s * 1024, xs + (s + 1) * 1024, budget);
        }
#pragma unroll
        for (int i = 0; i < 2; ++i) {
          k0 = mfma(a[4 * i], xf[s + i], k0); k1 = mfma(a[4 * i + 1], xf[s + i], k1);
          v0 = mfma(a[4 * i + 2], xf[s + i], v0); v1 = mfma(a[4 * i + 3], xf[s + i], v1);
          gram = mfma(xf[s + i], xf[s + i], gram);
          if (s + 2 < 14) { a[4 * i] = wfrag(wk0, s + 2 + i); a[4 * i + 1] = wfrag(wk1, s + 2 + i); a[4 * i + 2] = wfrag(wk0 + L_WV, s + 2 + i); a[4 * i + 3] = wfrag(wk1 + L_WV, s + 2 + i); }
        }
      }
    }
    if (first) drain();
    wg_bar();
    STAMP(3 + layer * 12);
    if (first && threadIdx.x == 0) __builtin_amdgcn_raw_buffer_store_b64(v2u{(unsigned)epoch, (unsigned)(epoch >> 32)}, rs_ws, 0, W_FLAG + b * 8, SC1);
    const v4u srdN = isA ? srdB : srdA;
    if (isA) {
      dma_part(L_WK, srdN, (DM + 28 * h) * 2, 3 * DM * 2, false);
      dma_part(L_WV, srdN, (2 * DM + 28 * h) * 2, 3 * DM * 2, false);
      init_tile(W_X + layer * TILEBUF); init_tile(W_O + (layer + 1) * TILEBUF);
    }
    float rstd, nmr;
    {
      float ss = (c & 2) ? ((c & 1) ? gram.w : gram.z) : ((c & 1) ? gram.y : gram.x);
      ss = (g == (c >> 2)) ? ss : 0.f;
      float sx = g3 ? k1.x : 0.f;
      xsum2(ss, sx);
      const float mean = sx * (1.f / DM);
      const float var = fmaxf(fmaf(ss, 1.f / DM, -mean * mean), 0.f);
      rstd = __builtin_amdgcn_rsqf(var + 1e-5f);
      nmr = -mean * rstd;
    }
    auto ln_pack = [&](v4f a0, v4f a1, int part, float sc, float sh) {
      const v4f c0 = *(LAS v4f*)(csg + part * 128), c1 = *(LAS v4f*)(csg + part * 128 + 64);
      a0 = a0 * sc + c0 * sh; a1 = a1 * sc + c1 * sh;
      return v4u{pk(a0.x, a0.y), pk(a0.z, a0.w), pk(a1.x, a1.y), pk(a1.z, a1.w)};
    };
    auto q_gemm = [&](float sc, float sh) {
      v4f q0 = {0.f, 0.f, 0.f, 0.f}, q1 = q0;
#pragma unroll
      for (int s = 0; s < 14; ++s) { q0 = mfma(wfrag(wq0, s), xf[s], q0); q1 = mfma(wfrag(wq1, s), xf[s], q1); }
      return ln_pack(q0, q1, 2, sc, sh);
    };
    v4u kf = ln_pack(k0, k1, 0, rstd, nmr), vf = ln_pack(v0, v1, 1, rstd, nmr);
    if (g3) { kf.z = 0u; kf.w = 0u; vf.z = 0x3f80u; vf.w = 0u; }
    v4f o0 = {0.f, 0.f, 0.f, 0.f}, o1 = o0;
    float den;

    if (isA) {
      v4u pp, pm;
      feature_map(kf, wphi, g3, pp, pm);
      {
        const lptr tb = stw + w * 3072;
        *(LAS v2u*)(tb) = v2u{pp.x, pp.y}; *(LAS v2u*)(tb + 512) = v2u{pp.z, pp.w};
        *(LAS v2u*)(tb + 1024) = v2u{pm.x, pm.y}; *(LAS v2u*)(tb + 1536) = v2u{pm.z, pm.w};
        *(LAS v2u*)(tb + 2048) = v2u{vf.x, vf.y}; *(LAS v2u*)(tb + 2560) = v2u{vf.z, vf.w};
      }
      wg_bar();
      STAMP(4 + layer * 12);
      const unsigned kvp = W_KVP + (layer >> 1) * 1048576 + h * 65536;
      if (w < 4) {
        v4f d0 = {0.f, 0.f, 0.f, 0.f}, d1 = d0;
        const lptr pa = str + (w >> 1) * 1024, pb = str + (2048 + (w & 1) * 512);
#pragma unroll
        for (int kk = 0; kk < 4; ++kk) {
          const v4u bv = cat(trd(pb + kk * 6144), trd(pb + kk * 6144 + 3072));
          d0 = mfma(cat(trd(pa + kk * 6144), trd(pa + kk * 6144 + 3072)), bv, d0);
          d1 = mfma(cat(trd(pa + kk * 6144 + 512), trd(pa + kk * 6144 + 3072 + 512)), bv, d1);
        }
        __builtin_amdgcn_raw_buffer_store_b128(v4u{pk(d0.x, d0.y), pk(d0.z, d0.w), pk(d1.x, d1.y), pk(d1.z, d1.w)}, rs_ws, l * 16, kvp + t * 4096 + w * 1024, SC1);
      }
      STAMP(5 + layer * 12);
      v2u fl[4];
      if (first) {
#pragma unroll
        for (int i = 0; i < 4; ++i) fl[i] = __builtin_amdgcn_raw_buffer_load_b64(rs_ws, l * 8, W_FLAG + i * 512, SC1);
      }
      const v4u qf = q_gemm(rstd, nmr);
      if (first) {
        for (;;) {
          bool ok = true;
#pragma unroll
          for (int i = 0; i < 4; ++i) ok &= fl[i].x == (unsigned)epoch && fl[i].y == (unsigned)(epoch >> 32);
          if (__all(ok)) break;
          if (budget == 0u) break;
          --budget;
          __builtin_amdgcn_s_sleep(4);
          fence();
#pragma unroll
          for (int i = 0; i < 4; ++i) fl[i] = __builtin_amdgcn_raw_buffer_load_b64(rs_ws, l * 8, W_FLAG + i * 512, SC1);
        }
        if (b == 0 && threadIdx.x == 0) __builtin_amdgcn_raw_buffer_store_b64(v2u{(unsigned)epoch, (unsigned)(epoch >> 32)}, rs_ws, 0, 0, SC1);
      }
      fence();
      v4u kp[8];
      const unsigned kvs = kvp + (w >> 2) * 32768 + (w & 3) * 1024;
#pragma unroll
      for (int i = 0; i < 8; ++i) kp[i] = __builtin_amdgcn_raw_buffer_load_b128(rs_ws, l * 16, kvs + i * 4096, SC1);
      feature_map(qf, wphi, g3, pp, pm);
      STAMP(6 + layer * 12);
      v4f lo = {0.f, 0.f, 0.f, 0.f}, hi = lo;
#pragma unroll
      for (int i = 0; i < 8; i += 2) {
        if (__builtin_expect(__any(has_sent2(kp[i], kp[i + 1])), 0)) {
          respin2(kp[i], kp[i + 1], srdW, l * 16, kvs + i * 4096, kvs + (i + 1) * 4096, budget);
        }
        const v4u u0 = kp[i], u1 = kp[i + 1];
        lo += __builtin_bit_cast(v4f, u0 << 16) + __builtin_bit_cast(v4f, u1 << 16);
        hi += __builtin_bit_cast(v4f, u0 & 0xffff0000u) + __builtin_bit_cast(v4f, u1 & 0xffff0000u);
      }
      *(LAS v4u*)(sl16 + (L_KVF - L_SCR) + w * 1024) = v4u{pk(lo.x, hi.x), pk(lo.y, hi.y), pk(lo.z, hi.z), pk(lo.w, hi.w)};
      patch_k();
      wg_bar();
      STAMP(7 + layer * 12);
#pragma unroll
      for (int hf = 0; hf < 2; ++hf) {
        const lptr fb = sl16 + (L_KVF - L_SCR) + hf * 4096;
        o0 = mfma(*(LAS v4u*)(fb), pp, o0); o1 = mfma(*(LAS v4u*)(fb + 1024), pp, o1);
        o0 = mfma(*(LAS v4u*)(fb + 2048), pm, o0); o1 = mfma(*(LAS v4u*)(fb + 3072), pm, o1);
      }
      den = bcast_row3(o1.x) + 1e-6f;
    } else {
      const unsigned hb = W_HALO + (layer >> 1) * 4194304 + h * 262144;
      if (t < 15) {
        __builtin_amdgcn_raw_buffer_store_b128(kf, rs_ws, l * 16, hb + (t * 8 + w) * 2048, SC1);
        __builtin_amdgcn_raw_buffer_store_b128(vf, rs_ws, l * 16, hb + (t * 8 + w) * 2048 + 1024, SC1);
      }
      auto stage = [&](int n, v4u kk, v4u vv, bool withk) {
        if (withk) *(LAS v4u*)(sl16 + n * 2048) = kk;
        *(LAS v2u*)(stw + n * 2048 + 1024) = v2u{vv.x, vv.y}; *(LAS v2u*)(stw + n * 2048 + 1536) = v2u{vv.z, vv.w};
      };
      STAMP(4 + layer * 12);
      constexpr float QS = 0.18898223650461357f * 1.4426950408889634f;
      v4u qf = q_gemm(rstd * QS, nmr * QS);
      if (g3) { qf.z = 0x3f80u; qf.w = 0u; }
      stage(16 + w, kf, vf, w < 7);
      v4u hk[2], hv[2];
      const unsigned hs = hb + ((t - 2) * 8 + w) * 2048;
#pragma unroll
      for (int i = 0; i < 2; ++i) {
        if (t - 2 + i >= 0) {
          hk[i] = __builtin_amdgcn_raw_buffer_load_b128(rs_ws, l * 16, hs + i * 16384, SC1);
          hv[i] = __builtin_amdgcn_raw_buffer_load_b128(rs_ws, l * 16, hs + i * 16384 + 1024, SC1);
        } else {
          hk[i] = v4u{0u, 0u, g3 ? 0xff7fu : 0u, 0u};
          hv[i] = v4u{0u, 0u, 0u, 0u};
        }
      }
      wg_bar();
      STAMP(5 + layer * 12);
      const v4f z4 = {0.f, 0.f, 0.f, 0.f};
      const float NEG = -3.0e38f;
      v4f sd = mfma(kf, qf, z4);
#pragma unroll
      for (int r = 0; r < 4; ++r) sd[r] = (4 * g + r <= c) ? sd[r] : NEG;
      float m = xmax1(fmaxf(fmaxf(sd.x, sd.y), fmaxf(sd.z, sd.w)));
      auto diag = [&]() {
        const lptr vb = str + (16 + w) * 2048 + 1024;
        const v2u a0 = trd(vb), a1 = trd(vb + 512);
        const v4u pd = {pk(ex2(sd.x - m), ex2(sd.y - m)), pk(ex2(sd.z - m), ex2(sd.w - m)), 0u, 0u};
        o0 = mfma(cat(a0, a0), pd, z4); o1 = mfma(cat(a1, a1), pd, z4);
      };
      auto pair = [&](int n, bool far_tile) {
        const lptr kb = sl16 + n * 2048, vb = str + n * 2048;
        const v4f cm = {-m, -m, -m, -m};
        v4f sa = mfma(*(LAS v4u*)(kb), qf, cm), sb = mfma(*(LAS v4u*)(kb + 2048), qf, cm);
        const v4u a0 = cat(trd(vb + 1024), trd(vb + 3072)), a1 = cat(trd(vb + 1536), trd(vb + 3584));
        if (far_tile) {
#pragma unroll
          for (int r = 0; r < 4; ++r) sa[r] = (4 * g + r > c) ? sa[r] : NEG;
        }
        const v4u pf = pexp(sa, sb);
        o0 = mfma(a0, pf, o0); o1 = mfma(a1, pf, o1);
      };
      diag();
      int n = w + 14;
      for (; n >= 16; n -= 2) pair(n, false);
      STAMP(6 + layer * 12);
#pragma unroll
      for (int i = 0; i < 2; ++i) {
        if (__builtin_expect(__any(has_sent2(hk[i], hv[i])), 0)) {
          respin2(hk[i], hv[i], srdW, l * 16, hs + i * 16384, hs + i * 16384 + 1024, budget);
        }
        stage(8 * i + w, hk[i], hv[i], true);
      }
      if (layer < 3) {
        dma_part(L_WK, srdN, (DM + 28 * h) * 2, 3 * DM * 2, false);
        dma_part(L_WV, srdN, (2 * DM + 28 * h) * 2, 3 * DM * 2, false);
        dma_part(L_WQ, srdN, (28 * h) * 2, 3 * DM * 2, false);
        init_tile(W_X + layer * TILEBUF); init_tile(W_O + (layer + 1) * TILEBUF);
        init_halo(W_HALO + 4194304);
      }
      wg_bar();
      STAMP(7 + layer * 12);
      for (; n > w; n -= 2) pair(n, false);
      pair(w, true);
      den = bcast_row3(o1.x);
      if (__builtin_expect(__any(!(den < 1.0e18f)), 0)) {
        float mt = m;
        for (int k = w; k < w + 16; ++k) {
          v4f s = mfma(*(LAS v4u*)(sl16 + k * 2048), qf, z4);
          if (k == w) {
#pragma unroll
            for (int r = 0; r < 4; ++r) s[r] = (4 * g + r > c) ? s[r] : NEG;
          }
          mt = fmaxf(mt, fmaxf(fmaxf(s.x, s.y), fmaxf(s.z, s.w)));
        }
        m = xmax1(mt);
        diag();
        for (int k = w + 14; k >= w; k -= 2) pair(k, k == w);
        den = bcast_row3(o1.x);
      }
      if (layer < 3) { drain(); patch_k(); }
      wg_bar();
    }
    auto publish = [&](unsigned off, v4u v) {
      if (!g3) __builtin_amdgcn_raw_buffer_store_b128(v, rs_ws, pub, off + tw, SC1);
      else __builtin_amdgcn_raw_buffer_store_b64(v2u{v.x, v.y}, rs_ws, pub, off + tw, SC1);
    };
    {
      const float rd = __builtin_amdgcn_rcpf(den);
      o0 *= rd; o1 *= rd;
      publish(W_O + layer * TILEBUF, v4u{pk(o0.x, o0.y), pk(o0.z, o0.w), pk(o1.x, o1.y), pk(o1.z, o1.w)});
    }
    STAMP(8 + layer * 12);
    if (isA) dma_part(L_WQ, srdN, (28 * h) * 2, 3 * DM * 2, false);
    __builtin_amdgcn_s_sleep(DLY_O);
    fence();
    const unsigned os = W_O + layer * TILEBUF + tw;
#pragma unroll
    for (int s = 0; s < 14; ++s) xf[s] = __builtin_amdgcn_raw_buffer_load_b128(rs_ws, l * 16, os + s * 1024, SC1);
    STAMP(9 + layer * 12);
    {
      v4u a[4];
#pragma unroll
      for (int i = 0; i < 2; ++i) { a[2 * i] = wfrag(wq0 + (L_WO - L_WQ), i); a[2 * i + 1] = wfrag(wq1 + (L_WO - L_WQ), i); }
#pragma unroll
      for (int s = 0; s < 14; s += 2) {
        if (__builtin_expect(__any(has_sent2(xf[s], xf[s + 1])), 0)) {
          respin2(xf[s], xf[s + 1], srdW, l * 16, os + s * 1024, os + (s + 1) * 1024, budget);
        }
#pragma unroll
        for (int i = 0; i < 2; ++i) {
          res0 = mfma(a[2 * i], xf[s + i], res0); res1 = mfma(a[2 * i + 1], xf[s + i], res1);
          if (s + 2 < 14) { a[2 * i] = wfrag(wq0 + (L_WO - L_WQ), s + 2 + i); a[2 * i + 1] = wfrag(wq1 + (L_WO - L_WQ), s + 2 + i); }
        }
      }
    }
    const v4u xo = {pk(res0.x, res0.y), pk(res0.z, res0.w), pk(res1.x, res1.y), pk(res1.z, res1.w)};
    if (layer < 3) {
      publish(W_X + layer * TILEBUF, xo);
      STAMP(10 + layer * 12);
      wg_bar();
      dma_part(L_WO, isA ? srdOB : srdOA, 56 * h, DM * 2, false);
    } else {
      char* op = (char*)p.out + tok * (DM * 2) + 56 * h + g * 8;
      *(v2u*)op = v2u{xo.x, xo.y};
      if (!g3) *(v2u*)(op + 32) = v2u{xo.z, xo.w};
      STAMP(10 + layer * 12);
    }
  }
}
}

extern "C" void kernel_launch(void* const* d_in, const int* in_sizes, int n_in, void* d_out, int out_size, void* d_ws, size_t ws_size,
                              hipStream_t stream) {
  (void)in_sizes; (void)n_in; (void)out_size; (void)ws_size;
  (void)hipFuncSetAttribute((const void*)fused_layers, hipFuncAttributeMaxDynamicSharedMemorySize, (int)L_TOTAL);
  Params p;
  p.x = d_in[0]; p.wA = d_in[1]; p.wphi = d_in[2]; p.woA = d_in[3]; p.wB = d_in[5]; p.woB = d_in[6]; p.out = d_out; p.ws = d_ws;
  hipLaunchKernelGGL(fused_layers, dim3(256), dim3(512), L_TOTAL, stream, p);
}
```

```cpp
#include <hip/hip_runtime.h>
#include <stdint.h>

#define LAS __attribute__((address_space(3)))
#define DEV __device__ __forceinline__
typedef unsigned v4u __attribute__((ext_vector_type(4)));
typedef unsigned v2u __attribute__((ext_vector_type(2)));
typedef float v4f __attribute__((ext_vector_type(4)));
typedef float v2f __attribute__((ext_vector_type(2)));
typedef short v4s __attribute__((ext_vector_type(4)));
typedef __bf16 v8bf __attribute__((ext_vector_type(8)));
typedef __bf16 v2bf __attribute__((ext_vector_type(2)));
typedef __amdgpu_buffer_rsrc_t rsrc_t;

#define TPX 2
#define DLY_O 14
#define DLY_X 10

namespace {
constexpr int SEQ = 2048, DM = 448;
constexpr unsigned L_WK = 0, L_WV = 28672, L_WQ = 57344, L_WO = 86016, L_SCR = 114688, L_TOTAL = 163840;
constexpr unsigned L_CS = L_SCR + 23 * 2048;
constexpr unsigned L_KVF = L_SCR + 24576;
constexpr unsigned L_XS = L_WO, L_PT = L_XS + 65536, L_PHI = L_PT + 8192;
constexpr unsigned TILEBUF = 16 * 8 * 14 * 1024;
constexpr unsigned W_FLAG = 4096, W_O = 65536, W_X = W_O + 4 * TILEBUF, W_KVP = W_X + 3 * TILEBUF, W_HALO = W_KVP + 2 * 1048576,
                   W_PT = W_HALO + 2 * 4194304, W_END = W_PT + 16 * 114688;
constexpr unsigned W_DBG = 32u << 20;
constexpr int SC1 = 16;
constexpr unsigned SENT = 0xffffffffu;
constexpr unsigned OOB = 0x40000000u;

DEV v4f mfma(v4u a, v4u b, v4f c) {
  return __builtin_amdgcn_mfma_f32_16x16x32_bf16(__builtin_bit_cast(v8bf, a), __builtin_bit_cast(v8bf, b), c, 0, 0, 0);
}
DEV unsigned pk(float a, float b) { v2f f = {a, b}; v2bf r = __builtin_convertvector(f, v2bf); return __builtin_bit_cast(unsigned, r); }
typedef LAS char* lptr;
DEV lptr lbase(unsigned v) { asm volatile("" : "+v"(v)); return (lptr)(size_t)v; }
DEV v2u trd(lptr a) { v4s r = __builtin_amdgcn_ds_read_tr16_b64_v4i16((LAS v4s*)a); return __builtin_bit_cast(v2u, r); }
DEV v4u cat(v2u a, v2u b) { return v4u{a.x, a.y, b.x, b.y}; }
DEV v4u lrd(lptr a) { return *(LAS v4u*)a; }
DEV void lwr(lptr a, v4u v) { *(LAS v4u*)a = v; }
#define VMWAIT(n) asm volatile("s_waitcnt vmcnt(" #n ")" ::: "memory")
DEV float asf(unsigned u) { return __builtin_bit_cast(float, u); }
DEV unsigned asu(float f) { return __builtin_bit_cast(unsigned, f); }
DEV void wg_bar() { asm volatile("s_waitcnt lgkmcnt(0)\n\ts_barrier" ::: "memory"); }
DEV void drain() { asm volatile("s_waitcnt vmcnt(0)" ::: "memory"); }
DEV void fence() { asm volatile("" ::: "memory"); }
DEV float ex2(float x) { return __builtin_amdgcn_exp2f(x); }

DEV float mx(float a, float b) { return __builtin_elementwise_maximum(a, b); }
DEV float mn(float a, float b) { return __builtin_elementwise_minimum(a, b); }
DEV float mx3(float a, float b, float c) { return mx(mx(a, b), c); }
DEV float mn3(float a, float b, float c) { return mn(mn(a, b), c); }
DEV float xrow(float sel, float v) { const v4f z = {0.f, 0.f, 0.f, 0.f}; const v4f r = __builtin_amdgcn_mfma_f32_16x16x4f32(sel, v, z, 0, 0, 0); return r.x; }
DEV void xmax2(float& a, float& b) {
  auto r = __builtin_amdgcn_permlane32_swap(asu(a), asu(b), false, false);
  unsigned r0 = r[0], r1 = r[1];
  float z = mx(asf(r0), asf(r1));
  auto q = __builtin_amdgcn_permlane16_swap(asu(z), asu(z), false, false);
  unsigned q0 = q[0], q1 = q[1];
  z = mx(asf(q0), asf(q1));
  auto p = __builtin_amdgcn_permlane32_swap(asu(z), asu(z), false, false);
  unsigned p0 = p[0], p1 = p[1];
  a = asf(p0); b = asf(p1);
}
DEV float xmax1(float a) {
  auto r = __builtin_amdgcn_permlane32_swap(asu(a), asu(a), false, false);
  unsigned r0 = r[0], r1 = r[1];
  float z = mx(asf(r0), asf(r1));
  auto q = __builtin_amdgcn_permlane16_swap(asu(z), asu(z), false, false);
  unsigned q0 = q[0], q1 = q[1];
  return mx(asf(q0), asf(q1));
}
DEV void dma2(unsigned ldsdst, unsigned va, unsigned vb, v4u srd, unsigned sa, unsigned sb) {
  asm volatile("s_mov_b32 m0, %0\n\ts_nop 3\n\tbuffer_load_dwordx4 %1, %3, %4 offen lds\n\tbuffer_load_dwordx4 %2, %3, %5 offen offset:1024 lds"
               :: "s"(ldsdst), "v"(va), "v"(vb), "s"(srd), "s"(sa), "s"(sb) : "memory");
}
DEV void keep(v4u v) { asm volatile("s_nop 1" :: "v"(v) : "memory"); }
DEV void keep(v4u v, v4u u) { asm volatile("s_nop 1" :: "v"(v), "v"(u) : "memory"); }
DEV v4u mk_srd(const void* p, unsigned bytes) {
  unsigned long long a = (unsigned long long)p;
  return v4u{(unsigned)a, (unsigned)(a >> 32) & 0xffffu, bytes, 0x00020000u};
}
DEV bool has_sent(v4u a) { return max(max(a.x, a.y), max(a.z, a.w)) == SENT; }
DEV bool has_sent2(v4u a, v4u b) { return max(max(max(a.x, a.y), max(a.z, a.w)), max(max(b.x, b.y), max(b.z, b.w))) == SENT; }

DEV void respin2(v4u& a, v4u& b, v4u srd, unsigned voff, unsigned sa, unsigned sb, int& budget) {
  unsigned tmp;
  asm volatile("1:\n\ts_sleep 2\n\t"
               "buffer_load_dwordx4 v[248:251], %4, %5, %6 offen sc1\n\tbuffer_load_dwordx4 v[252:255], %4, %5, %7 offen sc1\n\t"
               "v_add_u32 %3, -1, %3\n\tv_max_i32 %3, 0, %3\n\ts_waitcnt vmcnt(0)\n\t"
               "v_max3_u32 %2, v248, v249, v250\n\tv_max3_u32 %2, %2, v251, v252\n\tv_max3_u32 %2, %2, v253, v254\n\tv_max_u32 %2, %2, v255\n\t"
               "v_cmp_eq_u32 vcc, -1, %2\n\ts_nop 4\n\ts_cbranch_vccz 2f\n\tv_cmp_ne_u32 vcc, 0, %3\n\ts_nop 4\n\ts_cbranch_vccnz 1b\n2:"
               : "=&{v[248:251]}"(a), "=&{v[252:255]}"(b), "=&v"(tmp), "+v"(budget) : "v"(voff), "s"(srd), "s"(sa), "s"(sb) : "memory", "vcc");
}

DEV void feature_map(v4u vf, const v4u (&wphi)[2], v4u& plus, v4u& minus) {
  const v4f z4 = {0.f, 0.f, 0.f, 0.f};
  const v4f p0 = mfma(wphi[0], vf, z4), p1 = mfma(wphi[1], vf, z4);
  float hi = mx3(mx3(mx3(p0.x, p0.y, p0.z), p0.w, p1.x), p1.y, p1.z);
  float lo = -mn3(mn3(mn3(p0.x, p0.y, p0.z), p0.w, p1.x), p1.y, p1.z);
  xmax2(hi, lo);
  const v4f a0 = p0 - hi, a1 = p1 - hi, b0 = -lo - p0, b1 = -lo - p1;
  const v2f e01 = {ex2(a0.x), ex2(a0.y)}, e23 = {ex2(a0.z), ex2(a0.w)}, e45 = {ex2(a1.x), ex2(a1.y)};
  const v2f f01 = {ex2(b0.x), ex2(b0.y)}, f23 = {ex2(b0.z), ex2(b0.w)}, f45 = {ex2(b1.x), ex2(b1.y)};
  const float e6 = ex2(a1.z), f6 = ex2(b1.z);
  const v2f se = (e01 + e23) + e45, sf = (f01 + f23) + f45;
  const float re = __builtin_amdgcn_rcpf(xrow(1.f, (se.x + e6) + se.y)), rf = __builtin_amdgcn_rcpf(xrow(1.f, (sf.x + f6) + sf.y));
  const v2f x01 = e01 * re, x23 = e23 * re, x45 = e45 * re, y01 = f01 * rf, y23 = f23 * rf, y45 = f45 * rf;
  plus = v4u{pk(x01.x, x01.y), pk(x23.x, x23.y), pk(x45.x, x45.y), pk(e6 * re, 0.f)};
  minus = v4u{pk(y01.x, y01.y), pk(y23.x, y23.y), pk(y45.x, y45.y), pk(f6 * rf, 0.f)};
}

DEV v4u pexp(v4f a, v4f b) {
  return v4u{pk(ex2(a.x), ex2(a.y)), pk(ex2(a.z), ex2(a.w)), pk(ex2(b.x), ex2(b.y)), pk(ex2(b.z), ex2(b.w))};
}
#define SB() __builtin_amdgcn_sched_barrier(0)

#define STAMP(i) do {} while (0)

struct TagA { static constexpr bool value = true; };
struct TagB { static constexpr bool value = false; };
struct Params {
  const void* x; const void* wA; const void* wphi; const void* woA; const void* wB; const void* woB; void* out; void* ws;
};

__global__ void __launch_bounds__(512, 2) fused_layers(Params p) {
  const int b = blockIdx.x, xg = b & 7, xi = b >> 3;
  constexpr int HPX = 32 / TPX, NGT = 16 / TPX;
  const int t = (xg % NGT) * TPX + xi / HPX;
  const int h = (xg / NGT) * HPX + xi % HPX;
  const int wid = __builtin_amdgcn_readfirstlane(threadIdx.x >> 6) & 7;
  const int w = wid < 4 ? wid : 11 - wid;
  const int l = threadIdx.x & 63, c = l & 15, g = l >> 4;
  const bool g3 = g == 3;
  const unsigned long long epoch = *(const unsigned long long*)p.ws + 1ull;
  STAMP(0);
  const unsigned chunk16 = ((l & 3) ^ ((l >> 5) << 1)) * 16;
  const unsigned vqkv = (l >> 2) * (3 * DM * 2) + chunk16;
  auto gather_q = [&](int part, int s, const void* wq, unsigned dst) __attribute__((always_inline)) {
    const unsigned so = s * (32 * 3 * DM * 2) + (part == 2 ? 0 : (part + 1) * DM * 2) + 56 * h;
    dma2(dst, vqkv, vqkv, mk_srd(wq, DM * 3 * DM * 2), so, so + 16 * 3 * DM * 2 - 1024);
  };
  auto gather_o = [&](int s, const void* wo, unsigned dst) __attribute__((always_inline)) {
    unsigned v[2], lo = l;
    asm volatile("" : "+v"(lo));
#pragma unroll
    for (int i = 0; i < 2; ++i) {
      const int n = 4 * s + 2 * i + (lo >> 5), e = (lo >> 2) & 7, hh = n / 3, gg = n - 3 * hh;
      const unsigned ra = 28 * hh + 4 * gg + 16 * (e >> 2), rb = 28 * (2 * (n - 48) + (e >> 2)) + 12;
      v[i] = ((s < 12 ? ra : rb) + (e & 3)) * (DM * 2) + (((lo & 3) ^ ((lo >> 5) << 1)) * 16) + 1024 - i * 1024;
    }
    const unsigned so = 56 * h - 8 * (h & 1);
    dma2(dst, v[0], v[1], mk_srd((const char*)wo - 1024, DM * DM * 2 + 1024), so, so);
  };
  auto convert = [&](int part, unsigned at, v4u& f0, v4u& f1, unsigned& o1) __attribute__((always_inline)) {
    unsigned lo = l;
    asm volatile("" : "+v"(lo));
    const unsigned rowb = at + (8 * (lo >> 4) + ((lo >> 2) & 3)) * 64, sw = (lo >> 3) & 2, p4 = lo & 3, cc = lo & 15;
    const unsigned a0 = part < 3 ? rowb + ((p4 >> 1) ^ sw) * 16 + (p4 & 1) * 8 : rowb + (p4 ^ sw) * 16;
    const lptr b0 = lbase(a0), b1 = lbase(part < 3 ? a0 ^ 32 : a0 + 8);
    f0 = cat(trd(b0), trd(b0 + 256)); f1 = cat(trd(b1), trd(b1 + 256));
    const bool spread = !(part & 1), pad = spread && cc >= 12;
    const unsigned one = 0x3f803f80u, third = (cc * 11) >> 5;
    f1 = v4u{pad ? one : f1.x, pad ? one : f1.y, pad ? one : f1.z, pad ? one : f1.w};
    const unsigned slot = cc >= 12 ? 4 * cc - 45 : cc + third;
    o1 = 1024 + ((lo & 48) + (spread ? slot : cc)) * 16;
  };
  const lptr l16 = lbase(l * 16);
  auto convert_in_place = [&](int part, unsigned at) __attribute__((always_inline)) {
    v4u f0, f1; unsigned o1;
    convert(part, at, f0, f1, o1);
    lwr(l16 + at, f0); lwr((lptr)(size_t)(at + o1), f1);
  };
  const v4u srdX = mk_srd(p.x, SEQ * DM * 2);
  const unsigned xsb = L_XS + w * 8192, xso = (t * 128 + w * 16) * (DM * 2);
  const unsigned vx0 = (l >> 3) * (DM * 2) + ((l & 7) ^ (l >> 4)) * 16, vx1 = ((l >> 3) + 8) * (DM * 2) + ((l & 7) ^ (4 + (l >> 4))) * 16 - 1024;
  auto xline = [&](int j) __attribute__((always_inline)) { dma2(xsb + (j & 3) * 2048, vx0, vx1, srdX, xso + j * 128, xso + j * 128); };
  const int kvpart = w >> 2, kvs0 = w & 3;
  const unsigned kvat = kvpart * 28672 + kvs0 * 2048;
  gather_q(kvpart, kvs0, p.wA, kvat);
  xline(0); xline(1);
  const int bpt = t + 16 * (w - 4), ptpart = bpt / 14;
  if (w >= 4) {
    if (bpt < 42) gather_q(ptpart, bpt - 14 * ptpart, p.wB, L_PT + (w - 4) * 2048);
    else if (bpt < 56) gather_o(bpt - 42, p.woB, L_PT + (w - 4) * 2048);
  } else if (w == 0) {
    const int e = (l >> 2) & 7, g0 = l >> 5;
    const unsigned d0 = e < 4 ? 4 * g0 + e : 12 + 3 * g0 + e, d1 = d0 + (e < 4 ? 8 : 6);
    dma2(L_PHI, e < 7 ? d0 * 56 + chunk16 + 1024 : OOB, e < 7 ? d1 * 56 + chunk16 : OOB, mk_srd((const char*)p.wphi - 1024, 16 * 28 * 28 * 2 + 1024),
         h * 28 * 28 * 2, h * 28 * 28 * 2);
  }
  gather_q(kvpart, kvs0 + 4, p.wA, kvat + 8192);
  xline(2); xline(3);

  const rsrc_t rs_ws = __builtin_amdgcn_make_buffer_rsrc(p.ws, 0, W_END, 0x00020000);
  const v4u srdW = mk_srd(p.ws, W_END);
  const unsigned vl16 = l * 16, vinit = w < 7 ? l * 16 : OOB;
  const unsigned ini_t = t * 114688 + h * 7168 + w * 1024, ini_h = ((h * 16 + t) * 8 + w) * 2048;
  v4u sent4 = {SENT, SENT, SENT, SENT};
  asm volatile("" : "+v"(sent4));
  auto fill = [&](unsigned v, unsigned off) __attribute__((always_inline)) {
    __builtin_amdgcn_raw_buffer_store_b128(sent4, rs_ws, v, off, SC1);
  };
  const unsigned q4 = (l & 15) >> 2, p4 = l & 3;
  const lptr lq16 = lbase(L_WQ + l * 16);
  const lptr stw = lbase(L_SCR + c * 32 + ((g ^ (c >> 2)) * 8));
  const lptr str = lbase(L_SCR + (4 * g + q4) * 32 + ((p4 ^ g) * 8));
  const lptr sl16 = lbase(L_SCR + l * 16), csg = lbase(L_CS + g * 16);
  const int nob = 48 + (h >> 1), noa = 3 * h + g, no = g3 ? nob : noa;
  const unsigned pubo = (no >> 2) * 1024 + ((no & 3) * 16 + c) * 16 + (g3 ? (h & 1) * 8 : 0);
  const int nx = ((7 * h) >> 1) + g;
  const bool xhalf = g == ((h & 1) ? 0 : 3);
  const unsigned pubx = (nx >> 2) * 1024 + ((nx & 3) * 16 + c) * 16 + (xhalf ? (h & 1) * 8 : 0);
  const unsigned tw = (t * 8 + w) * 14336;
  const float sel3 = g3 ? 1.f : 0.f;
  const v4u one8 = {0x3f803f80u, 0x3f803f80u, 0x3f803f80u, 0x3f803f80u};
  auto colsum = [&](int tile, int set) __attribute__((always_inline)) {
    const lptr base = lbase((tile >> 1) * 28672 + (tile & 1) * 1024 + l * 16);
    v4f c0 = {0.f, 0.f, 0.f, 0.f}, c1 = c0;
#pragma unroll
    for (int s0 = 0; s0 < 14; s0 += 7) {
      v4u f[7];
#pragma unroll
      for (int s = 0; s < 7; ++s) f[s] = lrd(base + (s0 + s) * 2048);
      SB();
#pragma unroll
      for (int s = 0; s < 7; ++s) { if (s & 1) c1 = mfma(f[s], one8, c1); else c0 = mfma(f[s], one8, c0); }
    }
    *(LAS v4f*)(csg + tile * 64 + set * 384) = c0 + c1;
  };

  v4u park[14];
  v4u wphi[2];
  v4f res0, res1;
  int budget = 1 << 16;
  v4u xf[14];
  v4f k0 = {0.f, 0.f, 0.f, 0.f}, k1 = k0, v0 = k0, v1 = k0, gram = k0;
  STAMP(1);
  {
    const unsigned xa = xsb + c * 128 + (g ^ (c >> 1)) * 16;
    const lptr xa0 = lbase(xa), xa1 = lbase(xa ^ 64);
    v4f s0 = k0, s1 = k0, s2 = k0, s3 = k0;
    unsigned pto = 0;
#pragma unroll
    for (int st = 0; st < 4; ++st) {
      if (st < 2) VMWAIT(6); else if (st == 2) VMWAIT(2); else VMWAIT(10);
      if (st < 3 || kvs0 < 2) convert_in_place(kvpart, kvat + st * 8192);
      if (st == 0) {
        if (w >= 4) convert(ptpart, L_PT + (w - 4) * 2048, park[0], park[1], pto);
        else if (w == 0) convert_in_place(2, L_PHI);
      }
#pragma unroll
      for (int j = 2 * st; j < 2 * st + 2 && j < 7; ++j) { xf[2 * j] = lrd(xa0 + (j & 3) * 2048); xf[2 * j + 1] = lrd(xa1 + (j & 3) * 2048); }
      wg_bar();
      if (st == 0) { gather_q(kvpart, kvs0 + 8, p.wA, kvat + 16384); xline(4); xline(5); }
      if (st == 1) { if (kvs0 < 2) gather_q(kvpart, kvs0 + 12, p.wA, kvat + 24576); xline(6); }
      if (st == 2) {
        const unsigned po = (w >= 4 && bpt < 56) ? W_PT + h * 114688 + bpt * 2048 : W_END;
        __builtin_amdgcn_raw_buffer_store_b128(park[0], rs_ws, vl16, po, SC1);
        __builtin_amdgcn_raw_buffer_store_b128(park[1], rs_ws, pto, po, SC1);
        keep(park[0], park[1]);
        fill(vinit, W_O + ini_t);
        fill(vl16, W_KVP + (w >> 2) * 1048576 + (h * 16 + t) * 4096 + (w & 3) * 1024);
        fill(vl16, W_HALO + ini_h); fill(vl16, W_HALO + ini_h + 1024);
        gather_q(2, w, p.wA, L_WQ + w * 2048);
        const int s2q = w < 6 ? 8 + w : w;
        gather_q(2, s2q, p.wA, L_WQ + s2q * 2048);
      }
      if (st == 0) { wphi[0] = lrd(lq16 + (L_PHI - L_WQ - 65536) + 65536); wphi[1] = lrd(lq16 + (L_PHI - L_WQ - 65536) + 65536 + 1024); }
#pragma unroll
      for (int s = 4 * st; s < 4 * st + 4 && s < 14; ++s) {
        const v4u a0 = lrd(l16 + s * 2048), a1 = lrd(l16 + s * 2048 + 1024), a2 = lrd(l16 + L_WV + s * 2048), a3 = lrd(l16 + L_WV + s * 2048 + 1024);
        k0 = mfma(a0, xf[s], k0); k1 = mfma(a1, xf[s], k1); v0 = mfma(a2, xf[s], v0); v1 = mfma(a3, xf[s], v1);
        gram = mfma(xf[s], xf[s], gram);
        s0 = mfma(a0, one8, s0); s1 = mfma(a1, one8, s1); s2 = mfma(a2, one8, s2); s3 = mfma(a3, one8, s3);
      }
    }
    if (w == 0) { *(LAS v4f*)(csg) = s0; *(LAS v4f*)(csg + 64) = s1; *(LAS v4f*)(csg + 128) = s2; *(LAS v4f*)(csg + 192) = s3; }
  }
  STAMP(2);
  VMWAIT(4);
  wg_bar();
  if (w == 0) __builtin_amdgcn_raw_buffer_store_b64(v2u{(unsigned)epoch, (unsigned)(epoch >> 32)}, rs_ws, 0, W_FLAG + b * 8, SC1);

  const int w_ = w;
  auto run_layer = [&](auto tag, const int layer) __attribute__((always_inline)) {
    constexpr bool isA = decltype(tag)::value;
    const bool first = isA && layer == 0, more = isA || layer < 3;
    int w = w_;
    asm volatile("" : "+s"(w));
    __builtin_assume(w >= 0 && w < 8);
    STAMP(3 + layer * 12);
    const lptr cs = csg + (isA ? 0 : 384);
    const v4f ck0 = *(LAS v4f*)(cs), ck1 = *(LAS v4f*)(cs + 64), cv0 = *(LAS v4f*)(cs + 128), cv1 = *(LAS v4f*)(cs + 192);
    float rstd, nmr;
    {
      float ss = (c & 2) ? ((c & 1) ? gram.w : gram.z) : ((c & 1) ? gram.y : gram.x);
      ss = xrow(1.f / DM, (g == (c >> 2)) ? ss : 0.f);
      const float mean = k1.w * (1.f / DM);
      rstd = __builtin_amdgcn_rsqf(mx(fmaf(-mean, mean, ss), 0.f) + 1e-5f);
      nmr = -mean * rstd;
    }
    auto ln7 = [&](v4f a0, v4f a1, v4f c0, v4f c1, float sc, float sh, float pad) __attribute__((always_inline)) {
      a0 = a0 * sc + c0 * sh;
      const v2f u = v2f{a1.x, a1.y} * sc + v2f{c1.x, c1.y} * sh;
      return v4u{pk(a0.x, a0.y), pk(a0.z, a0.w), pk(u.x, u.y), pk(fmaf(a1.z, sc, c1.z * sh), pad)};
    };
    auto q_gemm = [&](float sc, float pad) __attribute__((always_inline)) {
      v4f q0 = {0.f, 0.f, 0.f, 0.f}, q1 = q0;
      v4u a[8];
#pragma unroll
      for (int i = 0; i < 8; ++i) a[i] = lrd(lq16 + i * 1024);
#pragma unroll
      for (int s = 0; s < 14; s += 2) {
        SB();
#pragma unroll
        for (int i = 0; i < 2; ++i) { q0 = mfma(a[(2 * s + 2 * i) & 7], xf[s + i], q0); q1 = mfma(a[(2 * s + 2 * i + 1) & 7], xf[s + i], q1); }
        if (s + 4 < 14) {
#pragma unroll
          for (int i = 0; i < 4; ++i) a[(2 * s + i) & 7] = lrd(lq16 + (s + 4) * 2048 + i * 1024);
        }
      }
      if (first) wg_bar();
      return ln7(q0, q1, *(LAS v4f*)(cs + 256), *(LAS v4f*)(cs + 320), rstd * sc, nmr * sc, pad);
    };
    constexpr float L2E = 1.4426950408889634f;
    const v4u kf = isA ? ln7(k0, k1, ck0, ck1, rstd * L2E, nmr * L2E, 0.f) : ln7(k0, k1, ck0, ck1, rstd, nmr, 0.f);
    v4u vf;
    {
      const v4f a0 = v0 * rstd + cv0 * nmr, a1 = v1 * rstd + cv1 * nmr;
      vf = v4u{pk(a0.x, a0.y), pk(a0.z, a0.w), g3 ? 0x3f80u : pk(a1.x, a1.y), g3 ? 0u : pk(a1.z, a1.w)};
    }
    const v4f z4 = {0.f, 0.f, 0.f, 0.f};
    v4f o0, o1;
    float den;

    if constexpr (isA) {
      v4u pp, pm;
      feature_map(kf, wphi, pp, pm);
      {
        const lptr tb = stw + w * 3072;
        *(LAS v2u*)(tb) = v2u{pp.x, pp.y}; *(LAS v2u*)(tb + 512) = v2u{pp.z, pp.w};
        *(LAS v2u*)(tb + 1024) = v2u{pm.x, pm.y}; *(LAS v2u*)(tb + 1536) = v2u{pm.z, pm.w};
        *(LAS v2u*)(tb + 2048) = v2u{vf.x, vf.y}; *(LAS v2u*)(tb + 2560) = v2u{vf.z, vf.w};
      }
      if (first) {
        drain();
        convert_in_place(2, L_WQ + w * 2048);
        if (w < 6) convert_in_place(2, L_WQ + (8 + w) * 2048);
      }
      wg_bar();
      STAMP(4 + layer * 12);
      const unsigned kvp = W_KVP + (layer >> 1) * 1048576 + h * 65536;
      if (w < 4) {
        const lptr pa = str + (w >> 1) * 1024, pb = str + (2048 + (w & 1) * 512);
        v4u bv[4], x0[4], x1[4];
#pragma unroll
        for (int kk = 0; kk < 4; ++kk) {
          bv[kk] = cat(trd(pb + kk * 6144), trd(pb + kk * 6144 + 3072));
          x0[kk] = cat(trd(pa + kk * 6144), trd(pa + kk * 6144 + 3072));
          x1[kk] = cat(trd(pa + kk * 6144 + 512), trd(pa + kk * 6144 + 3072 + 512));
        }
        v4f d0 = z4, d1 = z4;
#pragma unroll
        for (int kk = 0; kk < 4; ++kk) { d0 = mfma(x0[kk], bv[kk], d0); d1 = mfma(x1[kk], bv[kk], d1); }
        const v4u dv = {pk(d0.x, d0.y), pk(d0.z, d0.w), pk(d1.x, d1.y), pk(d1.z, d1.w)};
        __builtin_amdgcn_raw_buffer_store_b128(dv, rs_ws, vl16, kvp + t * 4096 + w * 1024, SC1);
        keep(dv);
      }
      fill(vinit, W_X + layer * TILEBUF + ini_t); fill(vinit, W_O + (layer + 1) * TILEBUF + ini_t);
      STAMP(5 + layer * 12);
      v4u fl[2];
      if (first) {
#pragma unroll
        for (int i = 0; i < 2; ++i) fl[i] = __builtin_amdgcn_raw_buffer_load_b128(rs_ws, vl16, W_FLAG + i * 1024, SC1);
        gather_o(w, p.woA, L_WO + w * 2048);
        if (w < 6) gather_o(8 + w, p.woA, L_WO + (8 + w) * 2048);
        else colsum(w - 2, 0);
      }
      const v4u qf = q_gemm(L2E, 0.f);
      if (first) {
        const unsigned e0 = (unsigned)epoch, e1 = (unsigned)(epoch >> 32);
        for (;;) {
          const bool ok = fl[0].x == e0 && fl[0].y == e1 && fl[0].z == e0 && fl[0].w == e1 && fl[1].x == e0 && fl[1].y == e1 && fl[1].z == e0 && fl[1].w == e1;
          if (__all(ok || budget == 0)) break;
          --budget;
          asm volatile("s_sleep 4\n\tbuffer_load_dwordx4 %0, %2, %3, %4 offen sc1\n\tbuffer_load_dwordx4 %1, %2, %3, %4 offen offset:1024 sc1\n\ts_waitcnt vmcnt(0)"
                       : "=&v"(fl[0]), "=&v"(fl[1]) : "v"(vl16), "s"(srdW), "s"(W_FLAG) : "memory");
        }
        if (b == 0 && w == 0) __builtin_amdgcn_raw_buffer_store_b64(v2u{e0, e1}, rs_ws, 0, 0, SC1);
      }
      fence();
      v4u kp[8];
      const unsigned kvs = kvp + (w >> 2) * 32768 + (w & 3) * 1024;
      if (w < 4) {
#pragma unroll
        for (int i = 0; i < 8; ++i) kp[i] = __builtin_amdgcn_raw_buffer_load_b128(rs_ws, vl16, kvs + i * 4096, SC1);
      }
      feature_map(qf, wphi, pp, pm);
      asm volatile("" : "+v"(pp), "+v"(pm) :: "memory");
      if (w >= 4) {
#pragma unroll
        for (int i = 0; i < 8; ++i) kp[i] = __builtin_amdgcn_raw_buffer_load_b128(rs_ws, vl16, kvs + i * 4096, SC1);
      }
      STAMP(6 + layer * 12);
      v4u sa, sb;
      {
        unsigned lo = l;
        asm volatile("" : "+v"(lo));
        const unsigned bit = ((lo >> 4) == ((lo >> 2) & 3)) ? ((lo & 1) ? 0x3f800000u : 0x3f80u) : 0u;
        const unsigned u0 = (lo & 2) ? 0u : bit, u1 = (lo & 2) ? bit : 0u;
        sa = v4u{u0, u1, 0u, 0u}; sb = v4u{0u, 0u, u0, u1};
      }
      v4f lo4 = z4, hi4 = z4;
#pragma unroll
      for (int i = 0; i < 8; ++i) { lo4 = mfma(sa, kp[i], lo4); hi4 = mfma(sb, kp[i], hi4); }
      if (__builtin_expect(__any(lo4.x != lo4.x), 0)) {
#pragma unroll
        for (int i = 0; i < 8; i += 2)
          if (__any(has_sent2(kp[i], kp[i + 1]))) respin2(kp[i], kp[i + 1], srdW, vl16, kvs + i * 4096, kvs + (i + 1) * 4096, budget);
        lo4 = z4; hi4 = z4;
#pragma unroll
        for (int i = 0; i < 8; ++i) { lo4 = mfma(sa, kp[i], lo4); hi4 = mfma(sb, kp[i], hi4); }
      }
      lwr(sl16 + (L_KVF - L_SCR) + w * 1024, v4u{pk(lo4.x, lo4.y), pk(lo4.z, lo4.w), pk(hi4.x, hi4.y), pk(hi4.z, hi4.w)});
      drain();
      wg_bar();
      STAMP(7 + layer * 12);
      v4u kv[8];
#pragma unroll
      for (int i = 0; i < 8; ++i) kv[i] = lrd(sl16 + (L_KVF - L_SCR) + i * 1024);
      o1 = mfma(kv[1], pp, z4); o0 = mfma(kv[0], pp, z4);
      o1 = mfma(kv[3], pm, o1); o0 = mfma(kv[2], pm, o0);
      o1 = mfma(kv[5], pp, o1); o0 = mfma(kv[4], pp, o0);
      o1 = mfma(kv[7], pm, o1); o0 = mfma(kv[6], pm, o0);
      den = xrow(sel3, o1.x) + 1e-6f;
    } else {
      const unsigned hb = W_HALO + (layer >> 1) * 4194304 + h * 262144;
      const unsigned hp = t < 15 ? hb + (t * 8 + w) * 2048 : W_END;
      __builtin_amdgcn_raw_buffer_store_b128(kf, rs_ws, vl16, hp, SC1);
      __builtin_amdgcn_raw_buffer_store_b128(vf, rs_ws, vl16 + 1024, hp, SC1);
      const lptr kw = sl16 + w * 2048, vw = stw + w * 2048, vr = str + w * 2048;
      if (w < 7) lwr(kw + 32768, kf);
      *(LAS v2u*)(vw + 32768 + 1024) = v2u{vf.x, vf.y}; *(LAS v2u*)(vw + 32768 + 1536) = v2u{vf.z, vf.w};
      STAMP(4 + layer * 12);
      const v4u qf = q_gemm(0.18898223650461357f * L2E, 1.f);
      v4u hk[2], hv[2];
      unsigned hs[2];
#pragma unroll
      for (int i = 0; i < 2; ++i) {
        hs[i] = t - 2 + i >= 0 ? hb + ((t - 2 + i) * 8 + w) * 2048 : W_END;
        hk[i] = __builtin_amdgcn_raw_buffer_load_b128(rs_ws, vl16, hs[i], SC1);
        hv[i] = __builtin_amdgcn_raw_buffer_load_b128(rs_ws, vl16 + 1024, hs[i], SC1);
      }
      if (more) { fill(vinit, W_X + layer * TILEBUF + ini_t); fill(vinit, W_O + (layer + 1) * TILEBUF + ini_t); fill(vl16, W_HALO + 4194304 + ini_h); fill(vl16, W_HALO + 4194304 + 1024 + ini_h); }
      const float NEG = -3.0e38f;
      v4f sd = mfma(kf, qf, z4);
#pragma unroll
      for (int r = 0; r < 4; ++r) sd[r] = (4 * g + r <= c) ? sd[r] : NEG;
      float m = xmax1(mx(mx3(sd.x, sd.y, sd.z), sd.w));
      wg_bar();
      STAMP(5 + layer * 12);
      auto diag = [&]() __attribute__((always_inline)) {
        const v2u a0 = trd(vr + 32768 + 1024), a1 = trd(vr + 32768 + 1536);
        const v4f e = sd - m;
        const v2u pd = {pk(ex2(e.x), ex2(e.y)), pk(ex2(e.z), ex2(e.w))};
        typedef short v4h __attribute__((ext_vector_type(4)));
        o0 = __builtin_amdgcn_mfma_f32_16x16x16bf16_1k(__builtin_bit_cast(v4h, a0), __builtin_bit_cast(v4h, pd), z4, 0, 0, 0);
        o1 = __builtin_amdgcn_mfma_f32_16x16x16bf16_1k(__builtin_bit_cast(v4h, a1), __builtin_bit_cast(v4h, pd), z4, 0, 0, 0);
      };
      auto take_halo = [&]() __attribute__((always_inline)) {
        STAMP(6 + layer * 12);
#pragma unroll
        for (int i = 0; i < 2; ++i) {
          if (__builtin_expect(__any(has_sent2(hk[i], hv[i])), 0)) respin2(hk[i], hv[i], srdW, vl16, hs[i], hs[i] + 1024, budget);
          if (t - 2 + i < 0) hk[i].w = 0xfe000000u;
          lwr(kw + i * 16384, hk[i]);
          *(LAS v2u*)(vw + i * 16384 + 1024) = v2u{hv[i].x, hv[i].y}; *(LAS v2u*)(vw + i * 16384 + 1536) = v2u{hv[i].z, hv[i].w};
        }
        drain();
        wg_bar();
        STAMP(7 + layer * 12);
      };
      {
        const v4f cm = {-m, -m, -m, -m};
        v4f sa, sb; v4u a0, a1;
#pragma unroll
        for (int j = 7; j >= 0; --j) {
          if (j >= 4 && (w >> 1) == 7 - j) take_halo();
          const v4u ka = lrd(kw + j * 4096), kb = lrd(kw + j * 4096 + 2048);
          const v4u n0 = cat(trd(vr + j * 4096 + 1024), trd(vr + j * 4096 + 3072)), n1 = cat(trd(vr + j * 4096 + 1536), trd(vr + j * 4096 + 3584));
          SB();
          if (j == 7) diag();
          else { const v4u pf = pexp(sa, sb); o0 = mfma(a0, pf, o0); o1 = mfma(a1, pf, o1); }
          sa = mfma(ka, qf, cm); sb = mfma(kb, qf, cm);
          a0 = n0; a1 = n1;
          SB();
        }
#pragma unroll
        for (int r = 0; r < 4; ++r) sa[r] = (4 * g + r > c) ? sa[r] : NEG;
        const v4u pf = pexp(sa, sb);
        o1 = mfma(a1, pf, o1); o0 = mfma(a0, pf, o0);
      }
      den = xrow(sel3, o1.x);
      if (__builtin_expect(__any(!(den < 1.0e18f)), 0)) {
        float mt = m;
#pragma unroll 1
        for (int k = 0; k < 16; ++k) {
          v4f s = mfma(lrd(kw + k * 2048), qf, z4);
#pragma unroll
          for (int r = 0; r < 4; ++r) s[r] = (k > 0 || 4 * g + r > c) ? s[r] : NEG;
          mt = mx3(mt, mx(s.x, s.y), mx(s.z, s.w));
        }
        m = xmax1(mt);
        diag();
        const v4f cm = {-m, -m, -m, -m};
#pragma unroll 1
        for (int j = 7; j >= 0; --j) {
          v4f sa = mfma(lrd(kw + j * 4096), qf, cm);
          const v4f sb = mfma(lrd(kw + j * 4096 + 2048), qf, cm);
#pragma unroll
          for (int r = 0; r < 4; ++r) sa[r] = (j > 0 || 4 * g + r > c) ? sa[r] : NEG;
          const v4u pf = pexp(sa, sb);
          o0 = mfma(cat(trd(vr + j * 4096 + 1024), trd(vr + j * 4096 + 3072)), pf, o0);
          o1 = mfma(cat(trd(vr + j * 4096 + 1536), trd(vr + j * 4096 + 3584)), pf, o1);
        }
        den = xrow(sel3, o1.x);
      }
    }
    const unsigned os = W_O + layer * TILEBUF + tw;
    {
      const float rd = __builtin_amdgcn_rcpf(den);
      o0 *= rd; o1 *= rd;
      const v4u v = {pk(o0.x, o0.y), pk(o0.z, o0.w), pk(o1.x, o1.y), pk(o1.z, o1.w)};
      __builtin_amdgcn_raw_buffer_store_b128(v, rs_ws, g3 ? OOB : pubo, os, SC1);
      __builtin_amdgcn_raw_buffer_store_b64(v2u{v.x, v.y}, rs_ws, g3 ? pubo : OOB, os, SC1);
      keep(v);
    }
    STAMP(8 + layer * 12);
    const lptr sw0 = lbase(w * 2048 + l * 16), sw1 = lbase(w * 2048 + 65536 + l * 16);
    auto swap_block = [&](int j) __attribute__((always_inline)) {
      const lptr a = j < 4 ? sw0 + j * 16384 : sw1 + (j - 4) * 16384;
      const v4u t0 = lrd(a), t1 = lrd(a + 1024);
      lwr(a, park[2 * j]); lwr(a + 1024, park[2 * j + 1]);
      park[2 * j] = t0; park[2 * j + 1] = t1;
    };
    if (first) {
#pragma unroll
      for (int j = 0; j < 14; ++j) park[j] = __builtin_amdgcn_raw_buffer_load_b128(rs_ws, vl16, W_PT + h * 114688 + (w + 8 * (j >> 1)) * 2048 + (j & 1) * 1024, SC1);
      unsigned lo = l;
      asm volatile("" : "+v"(lo));
      const v4u xr = __builtin_amdgcn_raw_buffer_load_b128(__builtin_amdgcn_make_buffer_rsrc((void*)p.x, 0, SEQ * DM * 2, 0x00020000),
                                                           (t * 128 + w * 16 + (lo & 15)) * (DM * 2) + (((7 * h) >> 1) + (lo >> 4)) * 16, 0, 0);
      convert_in_place(3, L_WO + w * 2048);
      if (w < 6) convert_in_place(3, L_WO + (8 + w) * 2048);
      res0 = v4f{asf(xr.x << 16), asf(xr.x & 0xffff0000u), asf(xr.y << 16), asf(xr.y & 0xffff0000u)};
      res1 = v4f{asf(xr.z << 16), asf(xr.z & 0xffff0000u), asf(xr.w << 16), asf(xr.w & 0xffff0000u)};
    }
    if (!isA || first) wg_bar();
    if (more) { swap_block(0); swap_block(1); }
    __builtin_amdgcn_s_sleep(DLY_O);
    fence();
    v4u of[14];
#pragma unroll
    for (int s = 0; s < 6; ++s) of[s] = __builtin_amdgcn_raw_buffer_load_b128(rs_ws, vl16, os + s * 1024, SC1);
    SB();
    if (more) {
      swap_block(2); swap_block(3); swap_block(4);
      if (w < 2) swap_block(5);
    }
    STAMP(9 + layer * 12);
    {
      v4u a[4];
#pragma unroll
      for (int i = 0; i < 4; ++i) a[i] = lrd(lq16 + (L_WO - L_WQ) + i * 1024);
#pragma unroll
      for (int s = 0; s < 14; s += 2) {
        if (__builtin_expect(__any(has_sent2(of[s], of[s + 1])), 0)) respin2(of[s], of[s + 1], srdW, vl16, os + s * 1024, os + (s + 1) * 1024, budget);
        if (s + 6 < 14) {
          of[s + 6] = __builtin_amdgcn_raw_buffer_load_b128(rs_ws, vl16, os + (s + 6) * 1024, SC1);
          of[s + 7] = __builtin_amdgcn_raw_buffer_load_b128(rs_ws, vl16, os + (s + 7) * 1024, SC1);
        }
        SB();
#pragma unroll
        for (int i = 0; i < 2; ++i) {
          res0 = mfma(a[2 * i], of[s + i], res0); res1 = mfma(a[2 * i + 1], of[s + i], res1);
          if (s + 2 < 14) { a[2 * i] = lrd(lq16 + (L_WO - L_WQ) + (s + 2 + i) * 2048); a[2 * i + 1] = lrd(lq16 + (L_WO - L_WQ) + (s + 2 + i) * 2048 + 1024); }
        }
        SB();
      }
    }
    {
      v4u undef; v4f undf;
#pragma unroll
      for (int s = 0; s < 14; ++s) xf[s] = undef;
      k0 = undf; k1 = undf; v0 = undf; v1 = undf; gram = undf;
    }
    if (more) {
      const v4u xo = v4u{pk(res0.x, res0.y), pk(res0.z, res0.w), pk(res1.x, res1.y), pk(res1.z, res1.w)};
      const unsigned xs = W_X + layer * TILEBUF + tw;
      __builtin_amdgcn_raw_buffer_store_b128(xo, rs_ws, xhalf ? OOB : pubx, xs, SC1);
      __builtin_amdgcn_raw_buffer_store_b64((h & 1) ? v2u{xo.z, xo.w} : v2u{xo.x, xo.y}, rs_ws, xhalf ? pubx : OOB, xs, SC1);
      keep(xo);
      STAMP(10 + layer * 12);
      wg_bar();
      if (w >= 2) swap_block(5);
      swap_block(6);
      STAMP(13 + layer * 12);
      __builtin_amdgcn_s_sleep(DLY_X);
      fence();
#pragma unroll
      for (int s = 0; s < 6; ++s) xf[s] = __builtin_amdgcn_raw_buffer_load_b128(rs_ws, vl16, xs + s * 1024, SC1);
      SB();
      if (first && w < 6) colsum(w, 1);
      STAMP(14 + layer * 12);
      k0 = z4; k1 = z4; v0 = z4; v1 = z4; gram = z4;
      v4u a[8];
#pragma unroll
      for (int i = 0; i < 2; ++i) { a[4 * i] = lrd(l16 + i * 2048); a[4 * i + 1] = lrd(l16 + i * 2048 + 1024); a[4 * i + 2] = lrd(l16 + L_WV + i * 2048); a[4 * i + 3] = lrd(l16 + L_WV + i * 2048 + 1024); }
#pragma unroll
      for (int s = 0; s < 14; s += 2) {
        if (__builtin_expect(__any(has_sent2(xf[s], xf[s + 1])), 0)) respin2(xf[s], xf[s + 1], srdW, vl16, xs + s * 1024, xs + (s + 1) * 1024, budget);
        if (s + 6 < 14) {
          xf[s + 6] = __builtin_amdgcn_raw_buffer_load_b128(rs_ws, vl16, xs + (s + 6) * 1024, SC1);
          xf[s + 7] = __builtin_amdgcn_raw_buffer_load_b128(rs_ws, vl16, xs + (s + 7) * 1024, SC1);
        }
        SB();
#pragma unroll
        for (int i = 0; i < 2; ++i) {
          gram = mfma(xf[s + i], xf[s + i], gram);
          k1 = mfma(a[4 * i + 1], xf[s + i], k1); k0 = mfma(a[4 * i], xf[s + i], k0);
          v0 = mfma(a[4 * i + 2], xf[s + i], v0); v1 = mfma(a[4 * i + 3], xf[s + i], v1);
          if (s + 2 < 14) {
            const int o = (s + 2 + i) * 2048;
            a[4 * i] = lrd(l16 + o); a[4 * i + 1] = lrd(l16 + o + 1024); a[4 * i + 2] = lrd(l16 + L_WV + o); a[4 * i + 3] = lrd(l16 + L_WV + o + 1024);
          }
        }
        SB();
      }
      if (first) wg_bar();
    }
  };
#pragma unroll 1
  for (int rep = 0; rep < 2; ++rep) {
    run_layer(TagA{}, 2 * rep);
    run_layer(TagB{}, 2 * rep + 1);
  }
  {
    const rsrc_t rs_o = __builtin_amdgcn_make_buffer_rsrc(p.out, 0, SEQ * DM * 2, 0x00020000);
    unsigned lo = l;
    asm volatile("" : "+v"(lo));
    const unsigned oo = (t * 128 + w * 16 + (lo & 15)) * (DM * 2) + (((7 * h) >> 1) + (lo >> 4)) * 16;
    const v4u xo = {pk(res0.x, res0.y), pk(res0.z, res0.w), pk(res1.x, res1.y), pk(res1.z, res1.w)};
    __builtin_amdgcn_raw_buffer_store_b128(xo, rs_o, xhalf ? OOB : oo, 0, 0);
    __builtin_amdgcn_raw_buffer_store_b64((h & 1) ? v2u{xo.z, xo.w} : v2u{xo.x, xo.y}, rs_o, xhalf ? oo : OOB, (h & 1) * 8, 0);
    asm volatile("" :: "v"(sent4));
    STAMP(46);
  }
}
}

extern "C" void kernel_launch(void* const* d_in, const int* in_sizes, int n_in, void* d_out, int out_size, void* d_ws, size_t ws_size,
                              hipStream_t stream) {
  (void)in_sizes; (void)n_in; (void)out_size; (void)ws_size;
  (void)hipFuncSetAttribute((const void*)fused_layers, hipFuncAttributeMaxDynamicSharedMemorySize, (int)L_TOTAL);
  Params p;
  p.x = d_in[0]; p.wA = d_in[1]; p.wphi = d_in[2]; p.woA = d_in[3]; p.wB = d_in[5]; p.woB = d_in[6]; p.out = d_out; p.ws = d_ws;
  hipLaunchKernelGGL(fused_layers, dim3(256), dim3(512), L_TOTAL, stream, p);
}
```

```cpp
#include <hip/hip_runtime.h>
#include <stdint.h>

#define LAS __attribute__((address_space(3)))
#define DEV __device__ __forceinline__
typedef unsigned v4u __attribute__((ext_vector_type(4)));
typedef unsigned v2u __attribute__((ext_vector_type(2)));
typedef float v4f __attribute__((ext_vector_type(4)));
typedef float v2f __attribute__((ext_vector_type(2)));
typedef short v4s __attribute__((ext_vector_type(4)));
typedef __bf16 v8bf __attribute__((ext_vector_type(8)));
typedef __bf16 v2bf __attribute__((ext_vector_type(2)));
typedef __amdgpu_buffer_rsrc_t rsrc_t;

#define TPX 4
#define DLY_X 5
#define DLY_T 5
#define HINT_OA 10
#define HINT_OB 3
#define WIN 8
#define LATEFILL 1

namespace {
constexpr int SEQ = 2048, DM = 448;
constexpr unsigned L_WK = 0, L_WV = 28672, L_WQ = 57344, L_WO = 86016, L_SCR = 114688, L_TOTAL = 163840;
constexpr unsigned L_CS = L_SCR + 23 * 2048;
constexpr unsigned L_KVF = L_SCR + 24576;
constexpr unsigned TILEBUF = 16 * 8 * 14 * 1024;
constexpr unsigned W_HINT = 0, W_KVP = 8 * 32768, W_O0 = W_KVP + 2 * 1048576, W_X0 = W_O0 + TILEBUF, W_O1 = W_X0 + TILEBUF,
                   W_HALO = W_O1 + TILEBUF, W_EARLY = W_HALO + 4194304, W_X1 = W_HALO + 2 * 4194304, W_FILL_END = W_X1 + 4 * TILEBUF,
                   W_XIN = W_FILL_END, W_PT = W_XIN + TILEBUF, W_CS = W_PT + 2 * 16 * 114688, W_PHI = W_CS + 16 * 1024,
                   W_END = W_PHI + 16 * 2048;
constexpr unsigned o_buf(int layer) { return layer == 0 ? W_O0 : layer == 1 ? W_O1 : W_X1 + (2 * layer - 3) * TILEBUF; }
constexpr unsigned x_buf(int layer) { return layer < 0 ? W_XIN : layer == 0 ? W_X0 : W_X1 + (2 * layer - 2) * TILEBUF; }
constexpr unsigned W_DBG = 64u << 20;
constexpr int SC1 = 16;
constexpr unsigned SENT = 0xffffffffu;
constexpr unsigned OOB = 0x40000000u;

DEV v4f mfma(v4u a, v4u b, v4f c) {
  return __builtin_amdgcn_mfma_f32_16x16x32_bf16(__builtin_bit_cast(v8bf, a), __builtin_bit_cast(v8bf, b), c, 0, 0, 0);
}
DEV unsigned pk(float a, float b) { v2f f = {a, b}; v2bf r = __builtin_convertvector(f, v2bf); return __builtin_bit_cast(unsigned, r); }
typedef LAS char* lptr;
DEV lptr lbase(unsigned v) { asm volatile("" : "+v"(v)); return (lptr)(size_t)v; }
DEV v2u trd(lptr a) { v4s r = __builtin_amdgcn_ds_read_tr16_b64_v4i16((LAS v4s*)a); return __builtin_bit_cast(v2u, r); }
DEV v4u cat(v2u a, v2u b) { return v4u{a.x, a.y, b.x, b.y}; }
DEV v4u lrd(lptr a) { return *(LAS v4u*)a; }
DEV void lwr(lptr a, v4u v) { *(LAS v4u*)a = v; }
#define VMWAIT(n) asm volatile("s_waitcnt vmcnt(" #n ")" ::: "memory")
DEV float asf(unsigned u) { return __builtin_bit_cast(float, u); }
DEV unsigned asu(float f) { return __builtin_bit_cast(unsigned, f); }
DEV void wg_bar() { asm volatile("s_waitcnt lgkmcnt(0)\n\ts_barrier" ::: "memory"); }
DEV void drain() { asm volatile("s_waitcnt vmcnt(0)" ::: "memory"); }
DEV void fence() { asm volatile("" ::: "memory"); }
DEV float ex2(float x) { return __builtin_amdgcn_exp2f(x); }

DEV unsigned lane_id() { unsigned r; asm volatile("v_mbcnt_lo_u32_b32 %0, -1, 0\n\tv_mbcnt_hi_u32_b32 %0, -1, %0" : "=v"(r)); __builtin_assume(r < 64); return r; }
DEV float mx(float a, float b) { return __builtin_elementwise_maximum(a, b); }
DEV float mn(float a, float b) { return __builtin_elementwise_minimum(a, b); }
DEV float mx3(float a, float b, float c) { return mx(mx(a, b), c); }
DEV float mn3(float a, float b, float c) { return mn(mn(a, b), c); }
DEV float xrow(float sel, float v) { const v4f z = {0.f, 0.f, 0.f, 0.f}; const v4f r = __builtin_amdgcn_mfma_f32_16x16x4f32(sel, v, z, 0, 0, 0); return r.x; }
DEV void xmax2(float& a, float& b) {
  auto r = __builtin_amdgcn_permlane32_swap(asu(a), asu(b), false, false);
  unsigned r0 = r[0], r1 = r[1];
  float z = mx(asf(r0), asf(r1));
  auto q = __builtin_amdgcn_permlane16_swap(asu(z), asu(z), false, false);
  unsigned q0 = q[0], q1 = q[1];
  z = mx(asf(q0), asf(q1));
  auto p = __builtin_amdgcn_permlane32_swap(asu(z), asu(z), false, false);
  unsigned p0 = p[0], p1 = p[1];
  a = asf(p0); b = asf(p1);
}
DEV float xmax1(float a) {
  auto r = __builtin_amdgcn_permlane32_swap(asu(a), asu(a), false, false);
  unsigned r0 = r[0], r1 = r[1];
  float z = mx(asf(r0), asf(r1));
  auto q = __builtin_amdgcn_permlane16_swap(asu(z), asu(z), false, false);
  unsigned q0 = q[0], q1 = q[1];
  return mx(asf(q0), asf(q1));
}
DEV void dma2(unsigned ldsdst, unsigned va, unsigned vb, v4u srd, unsigned sa, unsigned sb) {
  asm volatile("s_mov_b32 m0, %0\n\ts_nop 3\n\tbuffer_load_dwordx4 %1, %3, %4 offen lds\n\tbuffer_load_dwordx4 %2, %3, %5 offen offset:1024 lds"
               :: "s"(ldsdst), "v"(va), "v"(vb), "s"(srd), "s"(sa), "s"(sb) : "memory");
}
DEV void keep(v4u v) { asm volatile("s_nop 1" :: "v"(v) : "memory"); }
DEV void keep(v4u v, v4u u) { asm volatile("s_nop 1" :: "v"(v), "v"(u) : "memory"); }
DEV v4u mk_srd(const void* p, unsigned bytes) {
  unsigned long long a = (unsigned long long)p;
  return v4u{(unsigned)a, (unsigned)(a >> 32) & 0xffffu, bytes, 0x00020000u};
}
DEV bool has_sent(v4u a) { return max(max(a.x, a.y), max(a.z, a.w)) == SENT; }
DEV bool has_sent2(v4u a, v4u b) { return max(max(max(a.x, a.y), max(a.z, a.w)), max(max(b.x, b.y), max(b.z, b.w))) == SENT; }

DEV void respin2(v4u& a, v4u& b, v4u srd, unsigned voff, unsigned sa, unsigned sb, int& budget) {
  unsigned tmp;
  asm volatile("1:\n\ts_sleep 2\n\t"
               "buffer_load_dwordx4 v[248:251], %4, %5, %6 offen sc1\n\tbuffer_load_dwordx4 v[252:255], %4, %5, %7 offen sc1\n\t"
               "v_add_u32 %3, -1, %3\n\tv_max_i32 %3, 0, %3\n\ts_waitcnt vmcnt(0)\n\t"
               "v_max3_u32 %2, v248, v249, v250\n\tv_max3_u32 %2, %2, v251, v252\n\tv_max3_u32 %2, %2, v253, v254\n\tv_max_u32 %2, %2, v255\n\t"
               "v_cmp_eq_u32 vcc, -1, %2\n\ts_nop 4\n\ts_cbranch_vccz 2f\n\tv_cmp_ne_u32 vcc, 0, %3\n\ts_nop 4\n\ts_cbranch_vccnz 1b\n2:"
               : "=&{v[248:251]}"(a), "=&{v[252:255]}"(b), "=&v"(tmp), "+v"(budget) : "v"(voff), "s"(srd), "s"(sa), "s"(sb) : "memory", "vcc");
}

DEV void hint_wait(v4u srd, unsigned voff, unsigned soff, int& budget) {
  unsigned tmp;
  asm volatile("1:\n\ts_sleep 1\n\tbuffer_load_dword %0, %2, %3, %4 offen sc1\n\tv_add_u32 %1, -1, %1\n\tv_max_i32 %1, 0, %1\n\ts_waitcnt vmcnt(0)\n\t"
               "v_cmp_eq_u32 vcc, -1, %0\n\ts_nop 4\n\ts_cbranch_vccz 2f\n\tv_cmp_ne_u32 vcc, 0, %1\n\ts_nop 4\n\ts_cbranch_vccnz 1b\n2:"
               : "=&v"(tmp), "+v"(budget) : "v"(voff), "s"(srd), "s"(soff) : "memory", "vcc");
}

DEV void feature_map(v4u vf, const v4u (&wphi)[2], v4u& plus, v4u& minus) {
  const v4f z4 = {0.f, 0.f, 0.f, 0.f};
  const v4f p0 = mfma(wphi[0], vf, z4), p1 = mfma(wphi[1], vf, z4);
  float hi = mx3(mx3(mx3(p0.x, p0.y, p0.z), p0.w, p1.x), p1.y, p1.z);
  float lo = -mn3(mn3(mn3(p0.x, p0.y, p0.z), p0.w, p1.x), p1.y, p1.z);
  xmax2(hi, lo);
  const v2f p01 = {p0.x, p0.y}, p23 = {p0.z, p0.w}, p45 = {p1.x, p1.y}, h2 = {hi, hi}, l2 = {lo, lo};
  v2f a01 = p01 - h2, a23 = p23 - h2, a45 = p45 - h2, b01 = p01 + l2, b23 = p23 + l2, b45 = p45 + l2;
  asm volatile("" : "+v"(a01), "+v"(a23), "+v"(a45), "+v"(b01), "+v"(b23), "+v"(b45));
  const v2f e01 = {ex2(a01.x), ex2(a01.y)}, e23 = {ex2(a23.x), ex2(a23.y)}, e45 = {ex2(a45.x), ex2(a45.y)};
  const float e6 = ex2(p1.z - hi);
  const v2f se = (e01 + e23) + e45;
  const float te = xrow(1.f, (se.x + e6) + se.y);
  __builtin_amdgcn_sched_barrier(0);
  const v2f f01 = {ex2(-b01.x), ex2(-b01.y)}, f23 = {ex2(-b23.x), ex2(-b23.y)}, f45 = {ex2(-b45.x), ex2(-b45.y)};
  const float f6 = ex2(-(p1.z + lo));
  const v2f sf = (f01 + f23) + f45;
  const float tf = xrow(1.f, (sf.x + f6) + sf.y);
  __builtin_amdgcn_sched_barrier(0);
  const float re = __builtin_amdgcn_rcpf(te);
  const v2f x01 = e01 * re, x23 = e23 * re, x45 = e45 * re;
  plus = v4u{pk(x01.x, x01.y), pk(x23.x, x23.y), pk(x45.x, x45.y), pk(e6 * re, 0.f)};
  __builtin_amdgcn_sched_barrier(0);
  const float rf = __builtin_amdgcn_rcpf(tf);
  const v2f y01 = f01 * rf, y23 = f23 * rf, y45 = f45 * rf;
  minus = v4u{pk(y01.x, y01.y), pk(y23.x, y23.y), pk(y45.x, y45.y), pk(f6 * rf, 0.f)};
}

DEV v4u pexp(v4f a, v4f b) {
  return v4u{pk(ex2(a.x), ex2(a.y)), pk(ex2(a.z), ex2(a.w)), pk(ex2(b.x), ex2(b.y)), pk(ex2(b.z), ex2(b.w))};
}
#define SB() __builtin_amdgcn_sched_barrier(0)
#define INL __attribute__((always_inline))

#define STAMP(i) do {} while (0)

struct TagA { static constexpr bool value = true; };
struct TagB { static constexpr bool value = false; };
struct Params {
  const void* x; const void* wA; const void* wphi; const void* woA; const void* wB; const void* woB; void* out; void* ws;
};

__global__ void __launch_bounds__(512) prepare(Params p) {
  const int b = blockIdx.x, w = __builtin_amdgcn_readfirstlane(threadIdx.x >> 6) & 7;
  const unsigned l = threadIdx.x & 63, c = l & 15, g = l >> 4;
  const rsrc_t rs_ws = __builtin_amdgcn_make_buffer_rsrc(p.ws, 0, W_END, 0x00020000);
  const unsigned at = w * 4096, chunk16 = ((l & 3) ^ ((l >> 5) << 1)) * 16;
  const int type = (b >> 6) & 1, h = (b >> 2) & 15, part = b & 3;
  const bool phi = b < 128 && w == 7 && type == 0 && part == 2;
  if (b < 128) {
    if (w < 7) {
#pragma unroll
      for (int i = 0; i < 2; ++i) {
        const int s = 2 * w + i;
        if (part < 3) {
          const unsigned v = (l >> 2) * (3 * DM * 2) + chunk16, so = s * (32 * 3 * DM * 2) + (part == 2 ? 0 : (part + 1) * DM * 2) + 56 * h;
          dma2(at + i * 2048, v, v, mk_srd(type ? p.wB : p.wA, DM * 3 * DM * 2), so, so + 16 * 3 * DM * 2 - 1024);
        } else {
          unsigned v[2];
#pragma unroll
          for (int j = 0; j < 2; ++j) {
            const int n = 4 * s + 2 * j + (l >> 5), e = (l >> 2) & 7, hh = n / 3, gg = n - 3 * hh;
            const unsigned ra = 28 * hh + 4 * gg + 16 * (e >> 2), rb = 28 * (2 * (n - 48) + (e >> 2)) + 12;
            v[j] = ((s < 12 ? ra : rb) + (e & 3)) * (DM * 2) + chunk16 + 1024 - j * 1024;
          }
          const unsigned so = 56 * h - 8 * (h & 1);
          dma2(at + i * 2048, v[0], v[1], mk_srd((const char*)(type ? p.woB : p.woA) - 1024, DM * DM * 2 + 1024), so, so);
        }
      }
    } else if (phi) {
      const unsigned e = (l >> 2) & 7, g0 = l >> 5, d0 = e < 4 ? 4 * g0 + e : 12 + 3 * g0 + e, d1 = d0 + (e < 4 ? 8 : 6);
      dma2(at, e < 7 ? d0 * 56 + chunk16 + 1024 : OOB, e < 7 ? d1 * 56 + chunk16 : OOB,
           mk_srd((const char*)p.wphi - 1024, 16 * 28 * 28 * 2 + 1024), h * 28 * 28 * 2, h * 28 * 28 * 2);
    }
  } else if (w < 7) {
    const unsigned v0 = (l >> 3) * (DM * 2) + ((l & 7) ^ (l >> 4)) * 16, v1 = ((l >> 3) + 8) * (DM * 2) + ((l & 7) ^ (4 + (l >> 4))) * 16 - 1024;
    const unsigned so = (b - 128) * 16 * (DM * 2) + w * 128;
    dma2(at, v0, v1, mk_srd(p.x, SEQ * DM * 2), so, so);
  }
  constexpr unsigned NFILL = (LATEFILL ? W_EARLY : W_FILL_END) / 1024, PER = (NFILL + 2047) / 2048;
  {
    v4u s4 = {SENT, SENT, SENT, SENT};
    asm volatile("" : "+v"(s4));
#pragma unroll
    for (unsigned i = 0; i < PER; ++i) {
      const unsigned u = b * 8 + w + i * 2048;
      __builtin_amdgcn_raw_buffer_store_b128(s4, rs_ws, l * 16, u < NFILL ? u * 1024 : W_END, SC1);
    }
    keep(s4);
  }
  asm volatile("s_waitcnt vmcnt(%0)" :: "n"(PER) : "memory");
  if (b < 128) {
    v4f c0 = {0.f, 0.f, 0.f, 0.f}, c1 = c0;
    if (w < 7 || phi) {
      const unsigned rowb = at + (8 * g + (c >> 2)) * 64, sw = (g & 1) << 1, p4 = l & 3;
      const unsigned a0 = part < 3 ? rowb + ((p4 >> 1) ^ sw) * 16 + (p4 & 1) * 8 : rowb + (p4 ^ sw) * 16;
      const lptr r0 = lbase(a0), r1 = lbase(part < 3 ? a0 ^ 32 : a0 + 8);
      const bool spread = !(part & 1), pad = spread && c >= 12;
      const unsigned slot = c >= 12 ? 4 * c - 45 : c + ((c * 11) >> 5), o1 = 1024 + ((l & 48) + (spread ? slot : c)) * 16;
      const unsigned one = 0x3f803f80u;
      const v4u one8 = {one, one, one, one};
      const unsigned dst = phi ? W_PHI + h * 2048 : W_PT + (type * 16 + h) * 114688 + (part * 14 + 2 * w) * 2048;
#pragma unroll
      for (int i = 0; i < 2; ++i) {
        const v4u f0 = cat(trd(r0 + i * 2048), trd(r0 + i * 2048 + 256));
        v4u f1 = cat(trd(r1 + i * 2048), trd(r1 + i * 2048 + 256));
        f1 = v4u{pad ? one : f1.x, pad ? one : f1.y, pad ? one : f1.z, pad ? one : f1.w};
        const unsigned so = (phi && i) ? W_END : dst + i * 2048;
        __builtin_amdgcn_raw_buffer_store_b128(f0, rs_ws, l * 16, so, 0);
        __builtin_amdgcn_raw_buffer_store_b128(f1, rs_ws, o1, so, 0);
        keep(f0, f1);
        c0 = mfma(one8, f0, c0); c1 = mfma(one8, f1, c1);
      }
      *(LAS float*)(lbase(32768 + w * 128 + c * 4)) = c0.x;
      *(LAS float*)(lbase(32768 + w * 128 + 64 + (spread ? slot : c) * 4)) = c1.x;
    }
    wg_bar();
    if (w == 0 && part < 3) {
      float s = 0.f;
#pragma unroll
      for (int i = 0; i < 7; ++i) s += *(LAS float*)(lbase(32768 + (l & 31) * 4) + i * 128);
      __builtin_amdgcn_raw_buffer_store_b32(asu(s), rs_ws, l < 32 ? l * 4 : OOB, W_CS + h * 1024 + type * 384 + part * 128, 0);
    }
  } else if (w < 7) {
    const unsigned xa = at + c * 128 + (g ^ (c >> 1)) * 16;
    v4u f0 = lrd(lbase(xa)), f1 = lrd(lbase(xa ^ 64));
    const unsigned top = SENT - 1;
    f0 = v4u{min(f0.x, top), min(f0.y, top), min(f0.z, top), min(f0.w, top)};
    f1 = v4u{min(f1.x, top), min(f1.y, top), min(f1.z, top), min(f1.w, top)};
    const unsigned so = W_XIN + (b - 128) * 14336 + w * 2048;
    __builtin_amdgcn_raw_buffer_store_b128(f0, rs_ws, l * 16, so, 0);
    __builtin_amdgcn_raw_buffer_store_b128(f1, rs_ws, l * 16 + 1024, so, 0);
    keep(f0, f1);
  }
}

__global__ void __launch_bounds__(512, 2) fused_layers(Params p) {
  const int b = blockIdx.x, xg = b & 7, xi = b >> 3;
  constexpr int HPX = 32 / TPX, NGT = 16 / TPX;
  const int t = (xg % NGT) * TPX + xi / HPX;
  const int h = (xg / NGT) * HPX + xi % HPX;
  const int wid = __builtin_amdgcn_readfirstlane(threadIdx.x >> 6) & 7;
  const int w_ = wid < 4 ? 7 - wid : wid - 4;
  const int l = threadIdx.x & 63, c = l & 15, g = l >> 4;
  const bool g3 = g == 3;
  STAMP(0);
  const rsrc_t rs_ws = __builtin_amdgcn_make_buffer_rsrc(p.ws, 0, W_END, 0x00020000);
  const v4u srdW = mk_srd(p.ws, W_END);
  unsigned vl16 = l * 16;
  asm volatile("" : "+v"(vl16));
  const unsigned ptA = W_PT + h * 114688 + w_ * 2048;
#pragma unroll
  for (int j = 0; j < 5; ++j) dma2(w_ * 2048 + j * 16384, vl16, vl16, srdW, ptA + j * 16384, ptA + j * 16384);
  if (w_ < 2) dma2(w_ * 2048 + 81920, vl16, vl16, srdW, ptA + 81920, ptA + 81920);
  else if (w_ == 2) dma2(L_CS - 1024, OOB, vl16, srdW, 0, W_CS + h * 1024 - 1024);
  const int nx = ((7 * h) >> 1) + g;
  const unsigned pubx0 = (nx >> 2) * 1024 + ((nx & 3) * 16 + c) * 16;
  const unsigned tw = (t * 8 + w_) * 14336;
  v4u wphi[2];
  wphi[0] = __builtin_amdgcn_raw_buffer_load_b128(rs_ws, vl16, W_PHI + h * 2048, 0);
  wphi[1] = __builtin_amdgcn_raw_buffer_load_b128(rs_ws, vl16 + 1024, W_PHI + h * 2048, 0);
  v4u park[14];
  park[0] = __builtin_amdgcn_raw_buffer_load_b128(rs_ws, pubx0, W_XIN + tw, 0);

  const unsigned ini_t = t * 114688 + h * 7168 + w_ * 1024, ini_h = ((h * 16 + t) * 8 + w_) * 2048;
  auto fill2 = [&](unsigned o0, unsigned o1) INL {
    v4u s4 = {SENT, SENT, SENT, SENT};
    asm volatile("" : "+v"(s4));
    __builtin_amdgcn_raw_buffer_store_b128(s4, rs_ws, vl16, o0, SC1); __builtin_amdgcn_raw_buffer_store_b128(s4, rs_ws, vl16, o1, SC1);
    keep(s4);
  };
  auto tile_share = [&](unsigned off) INL { return w_ < 7 ? off + ini_t : W_END; };
  auto hint_put = [&](int slot, bool on) INL { __builtin_amdgcn_raw_buffer_store_b32(0u, rs_ws, 0, on ? W_HINT + slot * 32768 + (t * 16 + h) * 128 : W_END, SC1); };
  const unsigned q4 = (l & 15) >> 2, p4 = l & 3;
  const lptr l16 = (lptr)(size_t)vl16, lq16 = lbase(L_WQ + l * 16);
  const lptr stw = lbase(L_SCR + c * 32 + ((g ^ (c >> 2)) * 8));
  const lptr str = lbase(L_SCR + (4 * g + q4) * 32 + ((p4 ^ g) * 8));
  const lptr sl16 = lbase(L_SCR + l * 16), csg = lbase(L_CS + g * 16);
  const int nob = 48 + (h >> 1), noa = 3 * h + g, no = g3 ? nob : noa;
  const unsigned pubo = (no >> 2) * 1024 + ((no & 3) * 16 + c) * 16 + (g3 ? (h & 1) * 8 : 0);
  const bool xhalf = g == ((h & 1) ? 0 : 3);
  const unsigned pubx = pubx0 + (xhalf ? (h & 1) * 8 : 0);
  const unsigned vcol = l < 16 ? l * 2048 : OOB, vrow = l < 16 ? l * 128 : OOB;
  const float sel3 = g3 ? 1.f : 0.f;

  v4f res0, res1;
  int budget = 1 << 16;

  auto run_layer = [&](auto kind, const int rep_) INL {
    constexpr bool isA = decltype(kind)::value;
    const int rep = __builtin_amdgcn_readfirstlane(rep_);
    const int layer = 2 * rep + (isA ? 0 : 1);
    const bool first = isA && rep == 0, more = isA || rep == 0;
    int w = w_;
    asm volatile("" : "+s"(w));
    __builtin_assume(w >= 0 && w < 8);
    const bool lead = w >= 4;
    const v4f z4 = {0.f, 0.f, 0.f, 0.f};
    const unsigned xs = (isA ? (rep ? x_buf(1) : x_buf(-1)) : (rep ? x_buf(2) : x_buf(0))) + tw;
    v4u xf[14];
#pragma unroll
    for (int s = 0; s < WIN; ++s) xf[s] = __builtin_amdgcn_raw_buffer_load_b128(rs_ws, vl16, xs + s * 1024, SC1);
    SB();
    if (first) {
      asm volatile("s_waitcnt vmcnt(%0)" :: "n"(WIN) : "memory");
      const v4u xr = park[0];
      res0 = v4f{asf(xr.x << 16), asf(xr.x & 0xffff0000u), asf(xr.y << 16), asf(xr.y & 0xffff0000u)};
      res1 = v4f{asf(xr.z << 16), asf(xr.z & 0xffff0000u), asf(xr.w << 16), asf(xr.w & 0xffff0000u)};
      wg_bar();
    }
    STAMP(2 + layer * 12);
    v4f k0 = z4, k1 = z4, v0 = z4, v1 = z4, gram = z4;
    {
      v4u a[8];
#pragma unroll
      for (int i = 0; i < 2; ++i) { a[4 * i] = lrd(l16 + i * 2048); a[4 * i + 1] = lrd(l16 + i * 2048 + 1024); a[4 * i + 2] = lrd(l16 + L_WV + i * 2048); a[4 * i + 3] = lrd(l16 + L_WV + i * 2048 + 1024); }
#pragma unroll
      for (int s = 0; s < 14; s += 2) {
        if (__builtin_expect(__any(has_sent2(xf[s], xf[s + 1])), 0)) respin2(xf[s], xf[s + 1], srdW, vl16, xs + s * 1024, xs + (s + 1) * 1024, budget);
        if (s + WIN < 14) {
          xf[s + WIN] = __builtin_amdgcn_raw_buffer_load_b128(rs_ws, vl16, xs + (s + WIN) * 1024, SC1);
          xf[s + WIN + 1] = __builtin_amdgcn_raw_buffer_load_b128(rs_ws, vl16, xs + (s + WIN + 1) * 1024, SC1);
        }
        if (!isA && s == 12 && w == 0) hint_put(2 * layer, true);
        SB();
#pragma unroll
        for (int i = 0; i < 2; ++i) {
          gram = mfma(xf[s + i], xf[s + i], gram);
          k1 = mfma(a[4 * i + 1], xf[s + i], k1); k0 = mfma(a[4 * i], xf[s + i], k0);
          v0 = mfma(a[4 * i + 2], xf[s + i], v0); v1 = mfma(a[4 * i + 3], xf[s + i], v1);
          if (s + 2 < 14) {
            const int o = (s + 2 + i) * 2048;
            a[4 * i] = lrd(l16 + o); a[4 * i + 1] = lrd(l16 + o + 1024); a[4 * i + 2] = lrd(l16 + L_WV + o); a[4 * i + 3] = lrd(l16 + L_WV + o + 1024);
          }
        }
        SB();
      }
    }
    if (!lead) __builtin_amdgcn_s_setprio(1);
    STAMP(3 + layer * 12);
    const lptr cs = csg + (isA ? 0 : 384);
    const v4f ck0 = *(LAS v4f*)(cs), ck1 = *(LAS v4f*)(cs + 64), cv0 = *(LAS v4f*)(cs + 128), cv1 = *(LAS v4f*)(cs + 192);
    float rstd, nmr;
    {
      float ss = (c & 2) ? ((c & 1) ? gram.w : gram.z) : ((c & 1) ? gram.y : gram.x);
      ss = xrow(1.f / DM, (g == (c >> 2)) ? ss : 0.f);
      const float mean = k1.w * (1.f / DM);
      rstd = __builtin_amdgcn_rsqf(mx(fmaf(-mean, mean, ss), 0.f) + 1e-5f);
      nmr = -mean * rstd;
    }
    auto ln7 = [&](v4f a0, v4f a1, v4f c0, v4f c1, float sc, float sh, float pad) INL {
      a0 = a0 * sc + c0 * sh;
      const v2f u = v2f{a1.x, a1.y} * sc + v2f{c1.x, c1.y} * sh;
      return v4u{pk(a0.x, a0.y), pk(a0.z, a0.w), pk(u.x, u.y), pk(fmaf(a1.z, sc, c1.z * sh), pad)};
    };
    auto q_gemm = [&](float sc, float pad) INL {
      v4f q0 = z4, q1 = z4;
      v4u a[8];
#pragma unroll
      for (int i = 0; i < 8; ++i) a[i] = lrd(lq16 + i * 1024);
#pragma unroll
      for (int s = 0; s < 14; s += 2) {
        SB();
#pragma unroll
        for (int i = 0; i < 2; ++i) { q0 = mfma(a[(2 * s + 2 * i) & 7], xf[s + i], q0); q1 = mfma(a[(2 * s + 2 * i + 1) & 7], xf[s + i], q1); }
        if (s + 4 < 14) {
#pragma unroll
          for (int i = 0; i < 4; ++i) a[(2 * s + i) & 7] = lrd(lq16 + (s + 4) * 2048 + i * 1024);
        }
      }
      return ln7(q0, q1, *(LAS v4f*)(cs + 256), *(LAS v4f*)(cs + 320), rstd * sc, nmr * sc, pad);
    };
    auto swap_block = [&](int j) INL {
      const lptr a = lbase(w * 2048 + j * 16384 + vl16);
      const v4u t0 = lrd(a), t1 = lrd(a + 1024);
      lwr(a, park[2 * j]); lwr(a + 1024, park[2 * j + 1]);
      park[2 * j] = t0; park[2 * j + 1] = t1;
    };
    auto swap_wo = [&]() INL { if (w >= 2) swap_block(5); swap_block(6); };
    constexpr float L2E = 1.4426950408889634f;
    const v4u kf = isA ? ln7(k0, k1, ck0, ck1, rstd * L2E, nmr * L2E, 0.f) : ln7(k0, k1, ck0, ck1, rstd, nmr, 0.f);
    v4u vf;
    {
      const v4f a0 = v0 * rstd + cv0 * nmr, a1 = v1 * rstd + cv1 * nmr;
      vf = v4u{pk(a0.x, a0.y), pk(a0.z, a0.w), g3 ? 0x3f80u : pk(a1.x, a1.y), g3 ? 0u : pk(a1.z, a1.w)};
    }
    v4f o0, o1;
    float den;

    if constexpr (isA) {
      v4u pp, pm;
      if (w == 0) hint_put(2 * layer, true);
      feature_map(kf, wphi, pp, pm);
      {
        const lptr tb = stw + w * 3072;
        *(LAS v2u*)(tb) = v2u{pp.x, pp.y}; *(LAS v2u*)(tb + 512) = v2u{pp.z, pp.w};
        *(LAS v2u*)(tb + 1024) = v2u{pm.x, pm.y}; *(LAS v2u*)(tb + 1536) = v2u{pm.z, pm.w};
        *(LAS v2u*)(tb + 2048) = v2u{vf.x, vf.y}; *(LAS v2u*)(tb + 2560) = v2u{vf.z, vf.w};
      }
      auto side_work = [&]() INL {
        if (first) {
          if (w >= 2) dma2(w * 2048 + 81920, vl16, vl16, srdW, ptA + 81920, ptA + 81920);
          dma2(w * 2048 + 98304, vl16, vl16, srdW, ptA + 98304, ptA + 98304);
#pragma unroll
          for (int j = 0; j < 14; ++j) park[j] = __builtin_amdgcn_raw_buffer_load_b128(rs_ws, vl16, ptA + 16 * 114688 + (j >> 1) * 16384 + (j & 1) * 1024, 0);
        } else if (LATEFILL) {
          fill2(tile_share(x_buf(2)), tile_share(o_buf(3)));
        }
      };
      if (!lead) { wg_bar(); __builtin_amdgcn_s_setprio(0); __builtin_amdgcn_s_sleep(DLY_T); }
      side_work();
      if (!lead && !first) swap_wo();
      feature_map(q_gemm(L2E, 0.f), wphi, pp, pm);
      asm volatile("" : "+v"(pp), "+v"(pm) :: "memory");
      if (lead) {
        wg_bar();
        __builtin_amdgcn_s_setprio(1);
        STAMP(4 + layer * 12);
        const unsigned kvp = W_KVP + rep * 1048576 + h * 65536 + (w & 3) * 1024, sh = W_HINT + 2 * layer * 32768 + h * 128;
        const lptr pa = str + (w & 2) * 512, pb = str + (2048 + (w & 1) * 512);
        v4u bv[4], x0[4], x1[4];
#pragma unroll
        for (int kk = 0; kk < 4; ++kk) {
          bv[kk] = cat(trd(pb + kk * 6144), trd(pb + kk * 6144 + 3072));
          x0[kk] = cat(trd(pa + kk * 6144), trd(pa + kk * 6144 + 3072));
          x1[kk] = cat(trd(pa + kk * 6144 + 512), trd(pa + kk * 6144 + 3072 + 512));
        }
        const unsigned hv = __builtin_amdgcn_raw_buffer_load_b32(rs_ws, vcol, sh, SC1);
        v4f d0 = z4, d1 = z4;
#pragma unroll
        for (int kk = 0; kk < 4; ++kk) { d0 = mfma(x0[kk], bv[kk], d0); d1 = mfma(x1[kk], bv[kk], d1); }
        const v4u dv = {pk(d0.x, d0.y), pk(d0.z, d0.w), pk(d1.x, d1.y), pk(d1.z, d1.w)};
        __builtin_amdgcn_raw_buffer_store_b128(dv, rs_ws, vl16, kvp + t * 4096, SC1);
        keep(dv);
        STAMP(5 + layer * 12);
        if (!first) swap_wo();
        const unsigned lo = lane_id();
        const unsigned bit = ((lo >> 4) == ((lo >> 2) & 3)) ? ((lo & 1) ? 0x3f800000u : 0x3f80u) : 0u;
        const unsigned u0 = (lo & 2) ? 0u : bit, u1 = (lo & 2) ? bit : 0u;
        v4u sa = {u0, u1, 0u, 0u}, sb = {0u, 0u, u0, u1};
        asm volatile("" : "+v"(sa), "+v"(sb));
        if (__any(hv == SENT)) hint_wait(srdW, vcol, sh, budget);
        fence();
        STAMP(6 + layer * 12);
        v4u kp[16];
#pragma unroll
        for (int i = 0; i < 16; ++i) kp[i] = __builtin_amdgcn_raw_buffer_load_b128(rs_ws, vl16, kvp + i * 4096, SC1);
        SB();
        v4f lo4 = z4, hi4 = z4;
#pragma unroll
        for (int i = 0; i < 16; i += 2) {
          if (__builtin_expect(__any(has_sent2(kp[i], kp[i + 1])), 0)) respin2(kp[i], kp[i + 1], srdW, vl16, kvp + i * 4096, kvp + (i + 1) * 4096, budget);
          if (i == HINT_OA) hint_put(2 * layer + 1, w == 4);
          lo4 = mfma(sa, kp[i], lo4); hi4 = mfma(sb, kp[i], hi4);
          lo4 = mfma(sa, kp[i + 1], lo4); hi4 = mfma(sb, kp[i + 1], hi4);
          SB();
        }
        lwr(sl16 + (L_KVF - L_SCR) + (w & 3) * 1024, v4u{pk(lo4.x, lo4.y), pk(lo4.z, lo4.w), pk(hi4.x, hi4.y), pk(hi4.z, hi4.w)});
        VMWAIT(1);
      } else {
        drain();
      }
      wg_bar();
      __builtin_amdgcn_s_setprio(0);
      STAMP(7 + layer * 12);
      v4u kv[4];
#pragma unroll
      for (int i = 0; i < 4; ++i) kv[i] = lrd(sl16 + (L_KVF - L_SCR) + (i ^ 1) * 1024);
      o1 = mfma(kv[0], pp, z4); o0 = mfma(kv[1], pp, z4);
      o1 = mfma(kv[2], pm, o1); o0 = mfma(kv[3], pm, o0);
      den = xrow(sel3, o1.x) + 1e-6f;
    } else {
      const unsigned hb = W_HALO + rep * 4194304 + h * 262144;
      const unsigned hp = t < 15 ? hb + (t * 8 + w) * 2048 : W_END;
      __builtin_amdgcn_raw_buffer_store_b128(kf, rs_ws, vl16, hp, SC1);
      __builtin_amdgcn_raw_buffer_store_b128(vf, rs_ws, vl16 + 1024, hp, SC1);
      const lptr kw = sl16 + w * 2048, vw = stw + w * 2048, vr = str + w * 2048;
      if (w < 7) lwr(kw + 32768, kf);
      *(LAS v2u*)(vw + 32768 + 1024) = v2u{vf.x, vf.y}; *(LAS v2u*)(vw + 32768 + 1536) = v2u{vf.z, vf.w};
      STAMP(4 + layer * 12);
      if (!lead) { wg_bar(); __builtin_amdgcn_s_setprio(0); }
      const v4u qf = q_gemm(0.18898223650461357f * L2E, 1.f);
      const float NEG = -3.0e38f;
      v4f sd = mfma(kf, qf, z4);
#pragma unroll
      for (int r = 0; r < 4; ++r) sd[r] = (4 * g + r <= c) ? sd[r] : NEG;
      float m = xmax1(mx(mx3(sd.x, sd.y, sd.z), sd.w));
      auto diag = [&]() INL {
        const v2u a0 = trd(vr + 32768 + 1024), a1 = trd(vr + 32768 + 1536);
        const v4f e = sd - m;
        const v2u pd = {pk(ex2(e.x), ex2(e.y)), pk(ex2(e.z), ex2(e.w))};
        typedef short v4h __attribute__((ext_vector_type(4)));
        o0 = __builtin_amdgcn_mfma_f32_16x16x16bf16_1k(__builtin_bit_cast(v4h, a0), __builtin_bit_cast(v4h, pd), z4, 0, 0, 0);
        o1 = __builtin_amdgcn_mfma_f32_16x16x16bf16_1k(__builtin_bit_cast(v4h, a1), __builtin_bit_cast(v4h, pd), z4, 0, 0, 0);
      };
      diag();
      v4u hk[4], hv[4];
      unsigned hs[2];
#pragma unroll
      for (int i = 0; i < 2; ++i) hs[i] = t - 2 + i >= 0 ? hb + ((t - 2 + i) * 8 + (w & 3)) * 2048 : W_END;
      if (lead) {
        wg_bar();
        if (t > 0) hint_wait(srdW, (l < 2 && l < t) ? (1 - l) * 2048 : OOB, W_HINT + 2 * layer * 32768 + (t - 2) * 2048 + h * 128 + 0, budget);
#pragma unroll
        for (int i = 0; i < 4; ++i) {
          hk[i] = __builtin_amdgcn_raw_buffer_load_b128(rs_ws, vl16 + (i & 1) * 8192, hs[i >> 1], SC1);
          hv[i] = __builtin_amdgcn_raw_buffer_load_b128(rs_ws, vl16 + (i & 1) * 8192 + 1024, hs[i >> 1], SC1);
        }
      }
      swap_wo();
      STAMP(5 + layer * 12);
      auto take_halo = [&](bool ld) INL {
        STAMP(6 + layer * 12);
        if (ld) {
#pragma unroll
          for (int i = 0; i < 4; ++i) {
            if (__builtin_expect(__any(has_sent2(hk[i], hv[i])), 0))
              respin2(hk[i], hv[i], srdW, vl16, hs[i >> 1] + (i & 1) * 8192, hs[i >> 1] + (i & 1) * 8192 + 1024, budget);
            if (t - 2 + (i >> 1) < 0) hk[i].w = 0xfe000000u;
            lwr(kw + (i - 1) * 8192, hk[i]);
            *(LAS v2u*)(vw + (i - 1) * 8192 + 1024) = v2u{hv[i].x, hv[i].y}; *(LAS v2u*)(vw + (i - 1) * 8192 + 1536) = v2u{hv[i].z, hv[i].w};
          }
        }
        wg_bar();
        if (LATEFILL && more) { fill2(tile_share(x_buf(1)), tile_share(o_buf(2))); fill2(W_HALO + 4194304 + ini_h, W_HALO + 4194304 + 1024 + ini_h); }
        STAMP(7 + layer * 12);
      };
      {
        const v4f cm = {-m, -m, -m, -m};
        v4f sa, sb; v4u a0, a1;
#pragma unroll
        for (int j = 7; j >= 0; --j) {
          if (j >= 4 && (w >> 1) == 7 - j) take_halo(j < 6);
          if (j == HINT_OB && w == 0) hint_put(2 * layer + 1, true);
          const v4u ka = lrd(kw + j * 4096), kb = lrd(kw + j * 4096 + 2048);
          const v4u n0 = cat(trd(vr + j * 4096 + 1024), trd(vr + j * 4096 + 3072)), n1 = cat(trd(vr + j * 4096 + 1536), trd(vr + j * 4096 + 3584));
          SB();
          if (j < 7) { const v4u pf = pexp(sa, sb); o0 = mfma(a0, pf, o0); o1 = mfma(a1, pf, o1); }
          sa = mfma(ka, qf, cm); sb = mfma(kb, qf, cm);
          a0 = n0; a1 = n1;
          SB();
        }
#pragma unroll
        for (int r = 0; r < 4; ++r) sa[r] = (4 * g + r > c) ? sa[r] : NEG;
        const v4u pf = pexp(sa, sb);
        o1 = mfma(a1, pf, o1); o0 = mfma(a0, pf, o0);
      }
      den = xrow(sel3, o1.x);
      if (__builtin_expect(__any(!(den < 1.0e18f)), 0)) {
        float mt = m;
#pragma unroll 1
        for (int k = 0; k < 16; ++k) {
          v4f s = mfma(lrd(kw + k * 2048), qf, z4);
#pragma unroll
          for (int r = 0; r < 4; ++r) s[r] = (k > 0 || 4 * g + r > c) ? s[r] : NEG;
          mt = mx3(mt, mx(s.x, s.y), mx(s.z, s.w));
        }
        m = xmax1(mt);
        diag();
        const v4f cm = {-m, -m, -m, -m};
#pragma unroll 1
        for (int j = 7; j >= 0; --j) {
          v4f sa = mfma(lrd(kw + j * 4096), qf, cm);
          const v4f sb = mfma(lrd(kw + j * 4096 + 2048), qf, cm);
#pragma unroll
          for (int r = 0; r < 4; ++r) sa[r] = (j > 0 || 4 * g + r > c) ? sa[r] : NEG;
          const v4u pf = pexp(sa, sb);
          o0 = mfma(cat(trd(vr + j * 4096 + 1024), trd(vr + j * 4096 + 3072)), pf, o0);
          o1 = mfma(cat(trd(vr + j * 4096 + 1536), trd(vr + j * 4096 + 3584)), pf, o1);
        }
        den = xrow(sel3, o1.x);
      }
      if (LATEFILL && more) { drain(); wg_bar(); }
    }
    const unsigned os = (isA ? (rep ? o_buf(2) : o_buf(0)) : (rep ? o_buf(3) : o_buf(1))) + tw;
    const unsigned soh = W_HINT + (2 * layer + 1) * 32768 + t * 2048;
    unsigned oh = 0u;
    {
      const float rd = __builtin_amdgcn_rcpf(den);
      o0 *= rd; o1 *= rd;
      const v4u v = {pk(o0.x, o0.y), pk(o0.z, o0.w), pk(o1.x, o1.y), pk(o1.z, o1.w)};
      if (w == 0) oh = __builtin_amdgcn_raw_buffer_load_b32(rs_ws, vrow, soh, SC1);
      __builtin_amdgcn_raw_buffer_store_b128(v, rs_ws, g3 ? OOB : pubo, os, SC1);
      __builtin_amdgcn_raw_buffer_store_b64(v2u{v.x, v.y}, rs_ws, g3 ? pubo : OOB, os, SC1);
      keep(v);
    }
    STAMP(8 + layer * 12);
    if (more) { swap_block(0); swap_block(1); }
    if (__any(oh == SENT)) hint_wait(srdW, vrow, soh, budget);
    wg_bar();
    v4u of[14];
#pragma unroll
    for (int s = 0; s < WIN; ++s) of[s] = __builtin_amdgcn_raw_buffer_load_b128(rs_ws, vl16, os + s * 1024, SC1);
    SB();
    if (more) {
      swap_block(2); swap_block(3); swap_block(4);
      if (w < 2) swap_block(5);
    }
    STAMP(9 + layer * 12);
    {
      v4u a[4];
#pragma unroll
      for (int i = 0; i < 4; ++i) a[i] = lrd(lq16 + (L_WO - L_WQ) + i * 1024);
#pragma unroll
      for (int s = 0; s < 14; s += 2) {
        if (__builtin_expect(__any(has_sent2(of[s], of[s + 1])), 0)) respin2(of[s], of[s + 1], srdW, vl16, os + s * 1024, os + (s + 1) * 1024, budget);
        if (s + WIN < 14) {
          of[s + WIN] = __builtin_amdgcn_raw_buffer_load_b128(rs_ws, vl16, os + (s + WIN) * 1024, SC1);
          of[s + WIN + 1] = __builtin_amdgcn_raw_buffer_load_b128(rs_ws, vl16, os + (s + WIN + 1) * 1024, SC1);
        }
        SB();
#pragma unroll
        for (int i = 0; i < 2; ++i) {
          res0 = mfma(a[2 * i], of[s + i], res0); res1 = mfma(a[2 * i + 1], of[s + i], res1);
          if (s + 2 < 14) { a[2 * i] = lrd(lq16 + (L_WO - L_WQ) + (s + 2 + i) * 2048); a[2 * i + 1] = lrd(lq16 + (L_WO - L_WQ) + (s + 2 + i) * 2048 + 1024); }
        }
        SB();
      }
    }
    if (more) {
      const v4u xo = v4u{pk(res0.x, res0.y), pk(res0.z, res0.w), pk(res1.x, res1.y), pk(res1.z, res1.w)};
      const unsigned xd = (isA ? (rep ? x_buf(2) : x_buf(0)) : x_buf(1)) + tw;
      __builtin_amdgcn_raw_buffer_store_b128(xo, rs_ws, xhalf ? OOB : pubx, xd, SC1);
      __builtin_amdgcn_raw_buffer_store_b64((h & 1) ? v2u{xo.z, xo.w} : v2u{xo.x, xo.y}, rs_ws, xhalf ? pubx : OOB, xd, SC1);
      keep(xo);
      STAMP(10 + layer * 12);
      wg_bar();
      __builtin_amdgcn_s_sleep(DLY_X);
      fence();
    }
  };
#pragma unroll 1
  for (int rep = 0; rep < 2; ++rep) {
    run_layer(TagA{}, rep);
    run_layer(TagB{}, rep);
  }
  {
    const rsrc_t rs_o = __builtin_amdgcn_make_buffer_rsrc(p.out, 0, SEQ * DM * 2, 0x00020000);
    const unsigned lo = lane_id();
    const unsigned oo = (t * 128 + w_ * 16 + (lo & 15)) * (DM * 2) + (((7 * h) >> 1) + (lo >> 4)) * 16;
    const v4u xo = {pk(res0.x, res0.y), pk(res0.z, res0.w), pk(res1.x, res1.y), pk(res1.z, res1.w)};
    __builtin_amdgcn_raw_buffer_store_b128(xo, rs_o, xhalf ? OOB : oo, 0, 0);
    __builtin_amdgcn_raw_buffer_store_b64((h & 1) ? v2u{xo.z, xo.w} : v2u{xo.x, xo.y}, rs_o, xhalf ? oo : OOB, (h & 1) * 8, 0);
    STAMP(46);
  }
}
}

extern "C" void kernel_launch(void* const* d_in, const int* in_sizes, int n_in, void* d_out, int out_size, void* d_ws, size_t ws_size,
                              hipStream_t stream) {
  (void)in_sizes; (void)n_in; (void)out_size; (void)ws_size;
  (void)hipFuncSetAttribute((const void*)fused_layers, hipFuncAttributeMaxDynamicSharedMemorySize, (int)L_TOTAL);
  Params p;
  p.x = d_in[0]; p.wA = d_in[1]; p.wphi = d_in[2]; p.woA = d_in[3]; p.wB = d_in[5]; p.woB = d_in[6]; p.out = d_out; p.ws = d_ws;
  hipLaunchKernelGGL(prepare, dim3(256), dim3(512), 36864, stream, p);
  hipLaunchKernelGGL(fused_layers, dim3(256), dim3(512), L_TOTAL, stream, p);
}
```

```cpp
#include <hip/hip_runtime.h>
#include <stdint.h>

#define LAS __attribute__((address_space(3)))
#define DEV __device__ __forceinline__
typedef unsigned v4u __attribute__((ext_vector_type(4)));
typedef unsigned v2u __attribute__((ext_vector_type(2)));
typedef float v4f __attribute__((ext_vector_type(4)));
typedef float v2f __attribute__((ext_vector_type(2)));
typedef short v4s __attribute__((ext_vector_type(4)));
typedef __bf16 v8bf __attribute__((ext_vector_type(8)));
typedef __bf16 v2bf __attribute__((ext_vector_type(2)));
typedef __amdgpu_buffer_rsrc_t rsrc_t;

#define TPX 4
#define DLY_X 7
#define DLY_T 3
#define HINT_OA 2
#define HINT_OB 4
#define HINT_H 12
#define PRIO_L 2
#define PRIO_AT 1
#define DLY_F 2
#define DLY_OA 6
#define DLY_OB 8
#define POLL_OB 1
#define WO1 6
#define WO2 6
#define T_KV 0
#define T_H 8
#define T_OA 0
#define T_OB 4
#define P_X 10
#define T_X 12
#define FILLMAP 1
#define FILLAUX 0
#define WIN 4
#define LATEFILL 0

namespace {
constexpr int SEQ = 2048, DM = 448;
constexpr unsigned L_WK = 0, L_WV = 28672, L_WQ = 57344, L_WQB = 86016, L_SCR = 114688, L_TOTAL = 163840;
constexpr unsigned L_CS = L_SCR + 23 * 2048;
constexpr unsigned L_KVF = L_SCR + 24576;
constexpr unsigned TILEBUF = 16 * 8 * 14 * 1024;
constexpr unsigned W_HINT = 0, W_ST = 12 * 32768, W_KVP = W_ST + 3 * 32768, W_O0 = W_KVP + 2 * 1048576, W_X0 = W_O0 + TILEBUF, W_O1 = W_X0 + TILEBUF,
                   W_HALO = W_O1 + TILEBUF, W_EARLY = W_HALO + 4194304, W_X1 = W_HALO + 2 * 4194304, W_FILL_END = W_X1 + 4 * TILEBUF,
                   W_XIN = W_FILL_END, W_PT = W_XIN + TILEBUF, W_WO = W_PT + 2 * 16 * 86016, W_CS = W_WO + 2 * 28 * 14336, W_PHI = W_CS + 16 * 1024,
                   W_STIN = W_PHI + 16 * 2048, W_END = W_STIN + 32768;
constexpr unsigned o_buf(int layer) { return layer == 0 ? W_O0 : layer == 1 ? W_O1 : W_X1 + (2 * layer - 3) * TILEBUF; }
constexpr unsigned x_buf(int layer) { return layer < 0 ? W_XIN : layer == 0 ? W_X0 : W_X1 + (2 * layer - 2) * TILEBUF; }
constexpr unsigned W_DBG = 64u << 20;
constexpr int SC1 = 16;
constexpr unsigned SENT = 0xffffffffu;
constexpr unsigned OOB = 0x40000000u;

DEV v4f mfma(v4u a, v4u b, v4f c) {
  return __builtin_amdgcn_mfma_f32_16x16x32_bf16(__builtin_bit_cast(v8bf, a), __builtin_bit_cast(v8bf, b), c, 0, 0, 0);
}
DEV unsigned pk(float a, float b) { v2f f = {a, b}; v2bf r = __builtin_convertvector(f, v2bf); return __builtin_bit_cast(unsigned, r); }
typedef LAS char* lptr;
DEV lptr lbase(unsigned v) { asm volatile("" : "+v"(v)); return (lptr)(size_t)v; }
DEV v2u trd(lptr a) { v4s r = __builtin_amdgcn_ds_read_tr16_b64_v4i16((LAS v4s*)a); return __builtin_bit_cast(v2u, r); }
DEV v4u cat(v2u a, v2u b) { return v4u{a.x, a.y, b.x, b.y}; }
DEV v4u lrd(lptr a) { return *(LAS v4u*)a; }
DEV void lwr(lptr a, v4u v) { *(LAS v4u*)a = v; }
#define VMWAIT(n) asm volatile("s_waitcnt vmcnt(" #n ")" ::: "memory")
DEV float asf(unsigned u) { return __builtin_bit_cast(float, u); }
DEV unsigned asu(float f) { return __builtin_bit_cast(unsigned, f); }
DEV void wg_bar() { asm volatile("s_waitcnt lgkmcnt(0)\n\ts_barrier" ::: "memory"); }
DEV void drain() { asm volatile("s_waitcnt vmcnt(0)" ::: "memory"); }
DEV void fence() { asm volatile("" ::: "memory"); }
DEV float ex2(float x) { return __builtin_amdgcn_exp2f(x); }

DEV unsigned lane_id() { unsigned r; asm volatile("v_mbcnt_lo_u32_b32 %0, -1, 0\n\tv_mbcnt_hi_u32_b32 %0, -1, %0" : "=v"(r)); __builtin_assume(r < 64); return r; }
DEV float mx(float a, float b) { return __builtin_elementwise_maximum(a, b); }
DEV float mn(float a, float b) { return __builtin_elementwise_minimum(a, b); }
DEV float mx3(float a, float b, float c) { return mx(mx(a, b), c); }
DEV float mn3(float a, float b, float c) { return mn(mn(a, b), c); }
DEV float xrow(float sel, float v) { const v4f z = {0.f, 0.f, 0.f, 0.f}; const v4f r = __builtin_amdgcn_mfma_f32_16x16x4f32(sel, v, z, 0, 0, 0); return r.x; }
DEV v4f xrow4(float sel, float v) { const v4f z = {0.f, 0.f, 0.f, 0.f}; return __builtin_amdgcn_mfma_f32_16x16x4f32(sel, v, z, 0, 0, 0); }
DEV v4u swap16(v4u v) {
  auto r = __builtin_amdgcn_permlane16_swap(v.x, v.z, false, false);
  auto q = __builtin_amdgcn_permlane16_swap(v.y, v.w, false, false);
  const unsigned r0 = r[0], r1 = r[1], q0 = q[0], q1 = q[1];
  return v4u{r0, q0, r1, q1};
}
DEV float sumsq8(v4u v) {
  const v4f lo = __builtin_bit_cast(v4f, v << 16), hi = __builtin_bit_cast(v4f, v & 0xffff0000u), q = lo * lo + hi * hi;
  return (q.x + q.y) + (q.z + q.w);
}
DEV void xmax2(float& a, float& b) {
  auto r = __builtin_amdgcn_permlane32_swap(asu(a), asu(b), false, false);
  unsigned r0 = r[0], r1 = r[1];
  float z = mx(asf(r0), asf(r1));
  auto q = __builtin_amdgcn_permlane16_swap(asu(z), asu(z), false, false);
  unsigned q0 = q[0], q1 = q[1];
  z = mx(asf(q0), asf(q1));
  auto p = __builtin_amdgcn_permlane32_swap(asu(z), asu(z), false, false);
  unsigned p0 = p[0], p1 = p[1];
  a = asf(p0); b = asf(p1);
}
DEV float xmax1(float a) {
  auto r = __builtin_amdgcn_permlane32_swap(asu(a), asu(a), false, false);
  unsigned r0 = r[0], r1 = r[1];
  float z = mx(asf(r0), asf(r1));
  auto q = __builtin_amdgcn_permlane16_swap(asu(z), asu(z), false, false);
  unsigned q0 = q[0], q1 = q[1];
  return mx(asf(q0), asf(q1));
}
DEV void dma2(unsigned ldsdst, unsigned va, unsigned vb, v4u srd, unsigned sa, unsigned sb) {
  asm volatile("s_mov_b32 m0, %0\n\ts_nop 3\n\tbuffer_load_dwordx4 %1, %3, %4 offen lds\n\tbuffer_load_dwordx4 %2, %3, %5 offen offset:1024 lds"
               :: "s"(ldsdst), "v"(va), "v"(vb), "s"(srd), "s"(sa), "s"(sb) : "memory");
}
DEV void keep(v4u v) { asm volatile("s_nop 1" :: "v"(v) : "memory"); }
DEV void keep(v4u v, v4u u) { asm volatile("s_nop 1" :: "v"(v), "v"(u) : "memory"); }
DEV v4u ld16(rsrc_t rs, unsigned v, unsigned so) {
  const v4u r = __builtin_amdgcn_raw_buffer_load_b128(rs, v, so, SC1);
  __builtin_amdgcn_sched_barrier(0);
  return r;
}
DEV bool missing(unsigned v) { asm volatile("" : "+v"(v) :: "memory"); return __any(v == SENT); }
DEV v4u mk_srd(const void* p, unsigned bytes) {
  unsigned long long a = (unsigned long long)p;
  return v4u{(unsigned)a, (unsigned)(a >> 32) & 0xffffu, bytes, 0x00020000u};
}
DEV bool has_sent(v4u a) { return max(max(a.x, a.y), max(a.z, a.w)) == SENT; }
DEV bool has_sent2(v4u a, v4u b) { return max(max(max(a.x, a.y), max(a.z, a.w)), max(max(b.x, b.y), max(b.z, b.w))) == SENT; }

DEV void respin2(v4u& a, v4u& b, v4u srd, unsigned voff, unsigned sa, unsigned sb, int& budget) {
  unsigned tmp;
  asm volatile("1:\n\ts_sleep 2\n\t"
               "buffer_load_dwordx4 v[248:251], %4, %5, %6 offen sc1\n\tbuffer_load_dwordx4 v[252:255], %4, %5, %7 offen sc1\n\t"
               "v_add_u32 %3, -1, %3\n\tv_max_i32 %3, 0, %3\n\ts_waitcnt vmcnt(0)\n\t"
               "v_max3_u32 %2, v248, v249, v250\n\tv_max3_u32 %2, %2, v251, v252\n\tv_max3_u32 %2, %2, v253, v254\n\tv_max_u32 %2, %2, v255\n\t"
               "v_cmp_eq_u32 vcc, -1, %2\n\ts_nop 4\n\ts_cbranch_vccz 2f\n\tv_cmp_ne_u32 vcc, 0, %3\n\ts_nop 4\n\ts_cbranch_vccnz 1b\n2:"
               : "=&{v[248:251]}"(a), "=&{v[252:255]}"(b), "=&v"(tmp), "+v"(budget) : "v"(voff), "s"(srd), "s"(sa), "s"(sb) : "memory", "vcc");
}

DEV unsigned clock32() { unsigned long long t; asm volatile("s_memrealtime %0\n\ts_waitcnt lgkmcnt(0)" : "=s"(t) :: "memory"); return (unsigned)t & ~1u; }
DEV int lateness(unsigned v, int own, v4u srd, unsigned voff, unsigned soff, int& budget) {
  asm volatile("" : "+v"(v) :: "memory");
  int waited = 0;
  if (__builtin_expect(__any(v == SENT), 0)) {
    waited = clock32();
    asm volatile("1:\n\ts_sleep 1\n\tbuffer_load_dword %0, %2, %3, %4 offen sc1\n\tv_add_u32 %1, -1, %1\n\tv_max_i32 %1, 0, %1\n\ts_waitcnt vmcnt(0)\n\t"
                 "v_cmp_eq_u32 vcc, -1, %0\n\ts_nop 4\n\ts_cbranch_vccz 2f\n\tv_cmp_ne_u32 vcc, 0, %1\n\ts_nop 4\n\ts_cbranch_vccnz 1b\n2:"
                 : "=&v"(v), "+v"(budget) : "v"(voff), "s"(srd), "s"(soff) : "memory", "vcc");
    waited = clock32() - waited;
  }
  int d = (int)(v - __builtin_amdgcn_readlane(v, own));
  d = max(d, __builtin_amdgcn_update_dpp(0, d, 0x128, 0xf, 0xf, false));
  d = max(d, __builtin_amdgcn_update_dpp(0, d, 0x124, 0xf, 0xf, false));
  d = max(d, __builtin_amdgcn_update_dpp(0, d, 0x122, 0xf, 0xf, false));
  d = max(d, __builtin_amdgcn_update_dpp(0, d, 0x121, 0xf, 0xf, false));
  return min(__builtin_amdgcn_readfirstlane(d), 1000) - waited;
}
DEV void pause(int ticks) {
  asm volatile("s_cmp_lt_i32 %0, 1\n\ts_cbranch_scc1 2f\n1:\n\ts_sleep 1\n\ts_sub_i32 %0, %0, 4\n\ts_cmp_gt_i32 %0, 0\n\ts_cbranch_scc1 1b\n2:" : "+s"(ticks) :: "memory", "scc");
}

template <class F>
DEV void feature_map(v4u vf, const v4u (&wphi)[2], v4u& plus, v4u& minus, F mid) {
  const v4f z4 = {0.f, 0.f, 0.f, 0.f};
  const v4f p0 = mfma(wphi[0], vf, z4), p1 = mfma(wphi[1], vf, z4);
  float hi = mx3(mx3(mx3(p0.x, p0.y, p0.z), p0.w, p1.x), p1.y, p1.z);
  float lo = -mn3(mn3(mn3(p0.x, p0.y, p0.z), p0.w, p1.x), p1.y, p1.z);
  xmax2(hi, lo);
  mid();
  const v2f p01 = {p0.x, p0.y}, p23 = {p0.z, p0.w}, p45 = {p1.x, p1.y}, h2 = {hi, hi}, l2 = {lo, lo};
  v2f a01 = p01 - h2, a23 = p23 - h2, a45 = p45 - h2, b01 = p01 + l2, b23 = p23 + l2, b45 = p45 + l2;
  asm volatile("" : "+v"(a01), "+v"(a23), "+v"(a45), "+v"(b01), "+v"(b23), "+v"(b45));
  const v2f e01 = {ex2(a01.x), ex2(a01.y)}, e23 = {ex2(a23.x), ex2(a23.y)}, e45 = {ex2(a45.x), ex2(a45.y)};
  const float e6 = ex2(p1.z - hi);
  const v2f se = (e01 + e23) + e45;
  v4f te = xrow4(1.f, (se.x + e6) + se.y);
  __builtin_amdgcn_sched_barrier(0);
  const v2f f01 = {ex2(-b01.x), ex2(-b01.y)}, f23 = {ex2(-b23.x), ex2(-b23.y)}, f45 = {ex2(-b45.x), ex2(-b45.y)};
  const float f6 = ex2(-(p1.z + lo));
  const v2f sf = (f01 + f23) + f45;
  v4f tf = xrow4(1.f, (sf.x + f6) + sf.y);
  __builtin_amdgcn_sched_barrier(0);
  const float re = __builtin_amdgcn_rcpf(te.x);
  asm volatile("" :: "v"(te));
  const v2f x01 = e01 * re, x23 = e23 * re, x45 = e45 * re;
  plus = v4u{pk(x01.x, x01.y), pk(x23.x, x23.y), pk(x45.x, x45.y), pk(e6 * re, 0.f)};
  __builtin_amdgcn_sched_barrier(0);
  const float rf = __builtin_amdgcn_rcpf(tf.x);
  asm volatile("" :: "v"(tf));
  const v2f y01 = f01 * rf, y23 = f23 * rf, y45 = f45 * rf;
  minus = v4u{pk(y01.x, y01.y), pk(y23.x, y23.y), pk(y45.x, y45.y), pk(f6 * rf, 0.f)};
}

DEV v4u pexp(v4f a, v4f b) {
  return v4u{pk(ex2(a.x), ex2(a.y)), pk(ex2(a.z), ex2(a.w)), pk(ex2(b.x), ex2(b.y)), pk(ex2(b.z), ex2(b.w))};
}
#define VMKNOWN(n) __builtin_amdgcn_s_waitcnt(((n) & 15) | (((n) >> 4) << 14) | 0xf70)
#define LGKNOWN(n) __builtin_amdgcn_s_waitcnt(0xc07f | ((n) << 8))
#define SB() __builtin_amdgcn_sched_barrier(0)
#define INL __attribute__((always_inline))

#define STAMP(i) do {} while (0)

struct TagA { static constexpr bool value = true; };
struct TagB { static constexpr bool value = false; };
struct Params {
  const void* x; const void* wA; const void* wphi; const void* woA; const void* wB; const void* woB; void* out; void* ws;
};

__global__ void __launch_bounds__(512) prepare(Params p) {
  const int b = blockIdx.x, w = __builtin_amdgcn_readfirstlane(threadIdx.x >> 6) & 7;
  const unsigned l = threadIdx.x & 63, c = l & 15, g = l >> 4;
#define PSTAMP(i) do {} while (0)
  const rsrc_t rs_ws = __builtin_amdgcn_make_buffer_rsrc(p.ws, 0, W_END, 0x00020000);
  const unsigned at = w * 4096, chunk16 = ((l & 3) ^ ((l >> 5) << 1)) * 16;
  const int type = (b >> 6) & 1, h = (b >> 2) & 15, part = b & 3;
  const bool phi = b < 128 && w == 7 && type == 0 && part == 2;
  if (b < 128) {
    if (w < 7) {
#pragma unroll
      for (int i = 0; i < 2; ++i) {
        const int s = 2 * w + i;
        if (part < 3) {
          const unsigned v = (l >> 2) * (3 * DM * 2) + chunk16, so = s * (32 * 3 * DM * 2) + (part == 2 ? 0 : (part + 1) * DM * 2) + 56 * h;
          dma2(at + i * 2048, v, v, mk_srd(type ? p.wB : p.wA, DM * 3 * DM * 2), so, so + 16 * 3 * DM * 2 - 1024);
        } else if (h < 14) {
          unsigned v[2];
#pragma unroll
          for (int j = 0; j < 2; ++j) {
            const int n = 4 * s + 2 * j + (l >> 5), e = (l >> 2) & 7, hh = n / 3, gg = n - 3 * hh;
            const unsigned ra = 28 * hh + 4 * gg + 16 * (e >> 2), rb = 28 * (2 * (n - 48) + (e >> 2)) + 12;
            v[j] = ((s < 12 ? ra : rb) + (e & 3)) * (DM * 2) + chunk16 + 1024 - j * 1024;
          }
          dma2(at + i * 2048, v[0], v[1], mk_srd((const char*)(type ? p.woB : p.woA) - 1024, DM * DM * 2 + 1024), 64 * h, 64 * h);
        }
      }
    } else if (phi) {
      const unsigned e = (l >> 2) & 7, g0 = l >> 5, d0 = e < 4 ? 4 * g0 + e : 12 + 3 * g0 + e, d1 = d0 + (e < 4 ? 8 : 6);
      dma2(at, e < 7 ? d0 * 56 + chunk16 + 1024 : OOB, e < 7 ? d1 * 56 + chunk16 : OOB,
           mk_srd((const char*)p.wphi - 1024, 16 * 28 * 28 * 2 + 1024), h * 28 * 28 * 2, h * 28 * 28 * 2);
    }
  } else if (w < 7) {
    const unsigned v0 = (l >> 3) * (DM * 2) + ((l & 7) ^ (l >> 4)) * 16, v1 = ((l >> 3) + 8) * (DM * 2) + ((l & 7) ^ (4 + (l >> 4))) * 16 - 1024;
    const unsigned so = (b - 128) * 16 * (DM * 2) + w * 128;
    dma2(at, v0, v1, mk_srd(p.x, SEQ * DM * 2), so, so);
  }
  constexpr unsigned NFILL = (LATEFILL ? W_EARLY : W_FILL_END) / 1024,
 PER = (NFILL + 2047) / 2048;
  {
    v4u s4 = {SENT, SENT, SENT, SENT};
    asm volatile("" : "+v"(s4));
#pragma unroll
    for (unsigned i = 0; i < PER; ++i) {
      const unsigned u = FILLMAP == 0 ? b * 8 + w + i * 2048 : FILLMAP == 1 ? (b * PER + i) * 8 + w : (((b & 7) * 32 + (b >> 3)) * PER + i) * 8 + w;
      __builtin_amdgcn_raw_buffer_store_b128(s4, rs_ws, l * 16, u < NFILL ? u * 1024 : W_END, FILLAUX);
    }
    keep(s4);
  }
  asm volatile("s_waitcnt vmcnt(%0)" :: "n"(PER) : "memory");
  if (b < 128) {
    v4f c0 = {0.f, 0.f, 0.f, 0.f}, c1 = c0;
    if (w < 7 || phi) {
      const unsigned rowb = at + (8 * g + (c >> 2)) * 64, sw = (g & 1) << 1, p4 = l & 3;
      const unsigned a0 = rowb + ((p4 >> 1) ^ sw) * 16 + (p4 & 1) * 8;
      const lptr r0 = lbase(a0), r1 = lbase(a0 ^ 32);
      const bool spread = !(part & 1), pad = spread && c >= 12;
      const unsigned slot = c >= 12 ? 4 * c - 45 : c + ((c * 11) >> 5), o1 = (part == 3 ? 14336 : 1024) + ((l & 48) + (spread ? slot : c)) * 16;
      const unsigned one = 0x3f803f80u;
      const v4u one8 = {one, one, one, one};
      const unsigned dst = phi ? W_PHI + h * 2048 : part == 3 ? W_WO + (type * 28 + 2 * h) * 14336 + 2 * w * 1024
                                                  : W_PT + (type * 16 + h) * 86016 + (part * 14 + 2 * w) * 2048;
#pragma unroll
      for (int i = 0; i < 2; ++i) {
        const v4u f0 = cat(trd(r0 + i * 2048), trd(r0 + i * 2048 + 256));
        v4u f1 = cat(trd(r1 + i * 2048), trd(r1 + i * 2048 + 256));
        f1 = v4u{pad ? one : f1.x, pad ? one : f1.y, pad ? one : f1.z, pad ? one : f1.w};
        const unsigned so = ((phi && i) || (part == 3 && h >= 14)) ? W_END : dst + i * (part == 3 ? 1024 : 2048);
        __builtin_amdgcn_raw_buffer_store_b128(f0, rs_ws, l * 16, so, 0);
        __builtin_amdgcn_raw_buffer_store_b128(f1, rs_ws, o1, so, 0);
        keep(f0, f1);
        c0 = mfma(one8, f0, c0); c1 = mfma(one8, f1, c1);
      }
      *(LAS float*)(lbase(32768 + w * 128 + c * 4)) = c0.x;
      *(LAS float*)(lbase(32768 + w * 128 + 64 + (spread ? slot : c) * 4)) = c1.x;
    }
    wg_bar();
    if (w == 0 && part < 3) {
      float s = 0.f;
#pragma unroll
      for (int i = 0; i < 7; ++i) s += *(LAS float*)(lbase(32768 + (l & 31) * 4) + i * 128);
      __builtin_amdgcn_raw_buffer_store_b32(asu(s), rs_ws, l < 32 ? l * 4 : OOB, W_CS + h * 1024 + type * 384 + part * 128, 0);
    }
  } else if (w < 7) {
    const unsigned xa = at + c * 128 + (g ^ (c >> 1)) * 16;
    v4u f0 = lrd(lbase(xa)), f1 = lrd(lbase(xa ^ 64));
    const unsigned top = SENT - 1;
    f0 = v4u{min(f0.x, top), min(f0.y, top), min(f0.z, top), min(f0.w, top)};
    f1 = v4u{min(f1.x, top), min(f1.y, top), min(f1.z, top), min(f1.w, top)};
    const unsigned so = W_XIN + (b - 128) * 14336 + w * 2048;
    __builtin_amdgcn_raw_buffer_store_b128(f0, rs_ws, l * 16, so, 0);
    __builtin_amdgcn_raw_buffer_store_b128(f1, rs_ws, l * 16 + 1024, so, 0);
    keep(f0, f1);
    *(LAS float*)(lbase(32768 + w * 256 + l * 4)) = sumsq8(f0) + sumsq8(f1);
  }
  if (b >= 128) {
    wg_bar();
    if (w == 0) {
      float s = 0.f;
#pragma unroll
      for (int i = 0; i < 7; ++i) s += *(LAS float*)(lbase(32768 + l * 4) + i * 256);
      __builtin_amdgcn_raw_buffer_store_b32(min(asu(s), SENT - 1), rs_ws, l * 4, W_STIN + (b - 128) * 256, 0);
    }
  }
}

__global__ void __launch_bounds__(512, 2) fused_layers(Params p) {
  const int b = blockIdx.x, xg = b & 7, xi = b >> 3;
  constexpr int HPX = 32 / TPX, NGT = 16 / TPX;
  const int t = (xg % NGT) * TPX + xi / HPX;
  const int h = (xg / NGT) * HPX + xi % HPX;
  const int wid = __builtin_amdgcn_readfirstlane(threadIdx.x >> 6) & 7;
  const int w_ = wid < 4 ? 7 - wid : wid - 4;
  const int l = threadIdx.x & 63, c = l & 15, g = l >> 4;
  const bool g3 = g == 3;
  STAMP(0);
  const rsrc_t rs_ws = __builtin_amdgcn_make_buffer_rsrc(p.ws, 0, W_END, 0x00020000);
  const v4u srdW = mk_srd(p.ws, W_END);
  unsigned vl16 = l * 16;
  asm volatile("" : "+v"(vl16));
  const unsigned ptA = W_PT + h * 86016 + w_ * 2048;
#pragma unroll
  for (int j = 0; j < 3; ++j) dma2(w_ * 2048 + j * 16384, vl16, vl16, srdW, ptA + j * 16384, ptA + j * 16384);
  if (w_ < 4) dma2(w_ * 2048 + 49152, vl16, vl16, srdW, ptA + 49152, ptA + 49152);
  else if (w_ == 4) dma2(L_CS - 1024, OOB, vl16, srdW, 0, W_CS + h * 1024 - 1024);
  const int ft = 7 * (h & 3) + min(w_, 6), nx = 2 * ft + (g >> 1);
  const unsigned pubx = (g & 1) * 14336 + (nx >> 2) * 1024 + ((nx & 3) * 16 + c) * 16;
  const unsigned tw = (t * 8 + w_) * 14336, tp = (t * 8 + 2 * (h >> 2)) * 14336;
  v4u wphi[2];
  wphi[0] = __builtin_amdgcn_raw_buffer_load_b128(rs_ws, vl16, W_PHI + h * 2048, 0);
  wphi[1] = __builtin_amdgcn_raw_buffer_load_b128(rs_ws, vl16 + 1024, W_PHI + h * 2048, 0);
  v4u park[8];
  park[0] = __builtin_amdgcn_raw_buffer_load_b128(rs_ws, pubx, W_XIN + tp, 0);

  const unsigned ini_t = t * 114688 + h * 7168 + w_ * 1024, ini_h = ((h * 16 + t) * 8 + w_) * 2048;
  auto fill2 = [&](unsigned o0, unsigned o1, unsigned v) INL {
    v4u s4 = {SENT, SENT, SENT, SENT};
    asm volatile("" : "+v"(s4));
    __builtin_amdgcn_raw_buffer_store_b128(s4, rs_ws, v, o0, SC1); __builtin_amdgcn_raw_buffer_store_b128(s4, rs_ws, v, o1, SC1);
    keep(s4);
  };
  auto tile_share = [&](unsigned off) INL { return w_ < 7 ? off + ini_t : W_END; };
  auto hint_put = [&](int slot) INL { __builtin_amdgcn_raw_buffer_store_b32(clock32(), rs_ws, 0, W_HINT + slot * 32768 + (t * 16 + h) * 128, SC1); };
  const unsigned q4 = (l & 15) >> 2, p4 = l & 3;
  const lptr l16 = (lptr)(size_t)vl16, lq16 = lbase(L_WQ + l * 16);
  const lptr stw = lbase(L_SCR + c * 32 + ((g ^ (c >> 2)) * 8));
  const lptr str = lbase(L_SCR + (4 * g + q4) * 32 + ((p4 ^ g) * 8));
  const lptr sl16 = lbase(L_SCR + l * 16), csg = lbase(L_CS + g * 16);
  const int nob = 48 + (h >> 1), noa = 3 * h + g, no = g3 ? nob : noa;
  const unsigned pubo = (no >> 2) * 1024 + ((no & 3) * 16 + c) * 16 + (g3 ? (h & 1) * 8 : 0);
  const unsigned vcol = l < 16 ? l * 2048 : OOB, vrow = l < 16 ? l * 128 : OOB;
  const float sel3 = g3 ? 1.f : 0.f;

  v4u wof[14];
  const unsigned wos = W_WO + ft * 14336;
  v4f res0, res1;
  int budget = 1 << 16;

  auto run_layer = [&](auto kind, const int rep_) INL {
    constexpr bool isA = decltype(kind)::value;
    const int rep = __builtin_amdgcn_readfirstlane(rep_);
    const int layer = 2 * rep + (isA ? 0 : 1);
    const bool first = isA && rep == 0, more = isA || rep == 0;
    int w = w_;
    asm volatile("" : "+s"(w));
    __builtin_assume(w >= 0 && w < 8);
    const bool lead = w >= 4;
    const v4f z4 = {0.f, 0.f, 0.f, 0.f};
    const unsigned xs = (isA ? (rep ? x_buf(1) : x_buf(-1)) : (rep ? x_buf(2) : x_buf(0))) + tw;
    auto load_wo = [&](int s, bool typeA, bool on) INL {
      wof[s] = __builtin_amdgcn_raw_buffer_load_b128(rs_ws, vl16 + (s & 3) * 1024, on ? wos + (typeA ? 0 : 28 * 14336) + (s >> 2) * 4096 : W_END, 0); SB();
    };
    auto load_wo_rest = [&]() INL {
#pragma unroll
      for (int s = WO1; s < 14; ++s) load_wo(s, isA, w < 7);
    };
    v4u xf[14];
    v4f k0, k1, v0, v1;
    float mean, var;
    const unsigned sts = (first ? W_STIN : W_ST + (layer - 1) * 32768) + (t * 8 + w) * 256;
    if (lead) __builtin_amdgcn_s_setprio(PRIO_L); else __builtin_amdgcn_s_setprio(1);
    auto ldx = [&](int s) INL { xf[s] = ld16(rs_ws, vl16 + (s & 3) * 1024, xs + (s >> 2) * 4096); };
    for (bool again = false;; again = true) {
#pragma unroll
      for (int s = 0; s < WIN; ++s) ldx(s);
      if (first && !again) {
        asm volatile("s_waitcnt vmcnt(%0)" :: "n"(WIN) : "memory");
        wg_bar();
      }
      STAMP(2 + layer * 12);
      k0 = z4; k1 = z4; v0 = z4; v1 = z4;
      unsigned st;
      v4u a[8];
#pragma unroll
      for (int i = 0; i < 2; ++i) { a[4 * i] = lrd(l16 + i * 2048); a[4 * i + 1] = lrd(l16 + i * 2048 + 1024); a[4 * i + 2] = lrd(l16 + L_WV + i * 2048); a[4 * i + 3] = lrd(l16 + L_WV + i * 2048 + 1024); SB(); }
#pragma unroll
      for (int s = 0; s < 14; ++s) {
        const int i = 4 * (s & 1), o = (s + 2) * 2048;
        if (s < 13) LGKNOWN(4); else LGKNOWN(0);
        SB();
        k1 = mfma(a[i + 1], xf[s], k1); SB();
        if (s + WIN < 14) ldx(s + WIN);
        if (s + WIN == 14) { st = __builtin_amdgcn_raw_buffer_load_b32(rs_ws, vl16 >> 2, sts, SC1); SB(); }
        if (s == 13) { var = xrow(1.f / DM, asf(st)); SB(); }
        if (s + 2 < 14) { a[i + 1] = lrd(l16 + o + 1024); SB(); }
        k0 = mfma(a[i], xf[s], k0); SB(); if (s + 2 < 14) { a[i] = lrd(l16 + o); SB(); }
        v0 = mfma(a[i + 2], xf[s], v0); SB(); if (s + 2 < 14) { a[i + 2] = lrd(l16 + L_WV + o); SB(); }
        v1 = mfma(a[i + 3], xf[s], v1); SB(); if (s + 2 < 14) { a[i + 3] = lrd(l16 + L_WV + o + 1024); SB(); }
      }
      mean = k1.w * (1.f / DM);
      var = fmaf(-mean, mean, var);
      if (__builtin_expect(!__any(var != var), 1)) break;
      unsigned top = st;
#pragma unroll
      for (int s = 0; s < 14; ++s) top = max(max(top, max(xf[s].x, xf[s].y)), max(xf[s].z, xf[s].w));
      budget = max(budget - 8, 0);
      if (!__any(top == SENT && budget != 0)) break;
      __builtin_amdgcn_s_sleep(4);
    }
    if (lead) __builtin_amdgcn_s_setprio(0);
    if (w == 4) hint_put(2 * layer);
    STAMP(3 + layer * 12);
    const lptr cs = csg + (isA ? 0 : 384);
    const v4f ck0 = *(LAS v4f*)(cs), ck1 = *(LAS v4f*)(cs + 64), cv0 = *(LAS v4f*)(cs + 128), cv1 = *(LAS v4f*)(cs + 192);
    float rstd, nmr;
    {
      rstd = __builtin_amdgcn_rsqf(mx(var, 0.f) + 1e-5f);
      nmr = -mean * rstd;
    }
    auto ln7 = [&](v4f a0, v4f a1, v4f c0, v4f c1, float sc, float sh, float pad) INL {
      a0 = a0 * sc + c0 * sh;
      const v2f u = v2f{a1.x, a1.y} * sc + v2f{c1.x, c1.y} * sh;
      return v4u{pk(a0.x, a0.y), pk(a0.z, a0.w), pk(u.x, u.y), pk(fmaf(a1.z, sc, c1.z * sh), pad)};
    };
    auto q_gemm = [&](float sc, float pad) INL {
      v4f q0 = z4, q1 = z4;
      const lptr lq = lq16 + (isA ? 0 : L_WQB - L_WQ);
      v4u a[8];
#pragma unroll
      for (int i = 0; i < 8; ++i) { a[i] = lrd(lq + i * 1024); SB(); }
#pragma unroll
      for (int s = 0; s < 14; ++s) {
        const int i = (2 * s) & 7;
        if (!(s & 1)) { if (s < 12) LGKNOWN(4); else LGKNOWN(0); SB(); }
        q0 = mfma(a[i], xf[s], q0); SB(); if (s + 4 < 14) { a[i] = lrd(lq + (s + 4) * 2048); SB(); }
        q1 = mfma(a[i + 1], xf[s], q1); SB(); if (s + 4 < 14) { a[i + 1] = lrd(lq + (s + 4) * 2048 + 1024); SB(); }
      }
      return ln7(q0, q1, *(LAS v4f*)(cs + 256), *(LAS v4f*)(cs + 320), rstd * sc, nmr * sc, pad);
    };
    auto swap_block = [&](int j) INL {
      const lptr a = lbase(w * 2048 + j * 16384 + vl16);
      const v4u t0 = lrd(a), t1 = lrd(a + 1024);
      lwr(a, park[2 * j]); lwr(a + 1024, park[2 * j + 1]);
      park[2 * j] = t0; park[2 * j + 1] = t1;
    };
    constexpr float L2E = 1.4426950408889634f;
    const v4u kf = isA ? ln7(k0, k1, ck0, ck1, rstd * L2E, nmr * L2E, 0.f) : ln7(k0, k1, ck0, ck1, rstd, nmr, 0.f);
    v4u vf;
    {
      const v4f a0 = v0 * rstd + cv0 * nmr, a1 = v1 * rstd + cv1 * nmr;
      vf = v4u{pk(a0.x, a0.y), pk(a0.z, a0.w), g3 ? 0x3f80u : pk(a1.x, a1.y), g3 ? 0u : pk(a1.z, a1.w)};
    }
    v4f o0, o1;
    float den;
    const unsigned soh = W_HINT + (2 * layer + 1) * 32768 + t * 2048;
    unsigned oh;
    auto o_poll = [&]() INL { SB(); oh = __builtin_amdgcn_raw_buffer_load_b32(rs_ws, vrow, w == 7 ? soh : W_END, SC1); SB(); };

    if constexpr (isA) {
      if (first) {
        const v4u xr = swap16(park[0]);
        res0 = v4f{asf(xr.x << 16), asf(xr.x & 0xffff0000u), asf(xr.y << 16), asf(xr.y & 0xffff0000u)};
        res1 = v4f{asf(xr.z << 16), asf(xr.z & 0xffff0000u), asf(xr.w << 16), asf(xr.w & 0xffff0000u)};
        dma2(L_WQ + w * 2048, vl16, vl16, srdW, ptA + L_WQ, ptA + L_WQ);
        if (w < 6) dma2(L_WQ + 16384 + w * 2048, vl16, vl16, srdW, ptA + L_WQ + 16384, ptA + L_WQ + 16384);
#pragma unroll
        for (int s = 0; s < WO1; ++s) load_wo(s, true, w < 7);
      }
      else if (LATEFILL && lead) { fill2(tile_share(x_buf(2)), tile_share(o_buf(3)), vl16); fill2(x_buf(2) + ini_t - 4096, o_buf(3) + ini_t - 4096, vl16); }
      v4u pp, pm;
      feature_map(kf, wphi, pp, pm, []() {});
      {
        const lptr tb = stw + w * 3072;
        *(LAS v2u*)(tb) = v2u{pp.x, pp.y}; *(LAS v2u*)(tb + 512) = v2u{pp.z, pp.w};
        *(LAS v2u*)(tb + 1024) = v2u{pm.x, pm.y}; *(LAS v2u*)(tb + 1536) = v2u{pm.z, pm.w};
        *(LAS v2u*)(tb + 2048) = v2u{vf.x, vf.y}; *(LAS v2u*)(tb + 2560) = v2u{vf.z, vf.w};
      }
      if (first) VMKNOWN(0);
      wg_bar();
      STAMP(4 + layer * 12);
      const unsigned kvp = W_KVP + rep * 1048576 + h * 65536 + (w & 3) * 1024, sh = W_HINT + 2 * layer * 32768 + h * 128;
      if (!lead) __builtin_amdgcn_s_sleep(DLY_T);
      SB();
      const unsigned hv = __builtin_amdgcn_raw_buffer_load_b32(rs_ws, vcol, lead ? W_END : sh, SC1);
      SB();
      if (lead) {
        __builtin_amdgcn_s_setprio(2);
        const lptr pa = str + (w & 2) * 512, pb = str + (2048 + (w & 1) * 512);
        v4u bv[4], x0[4], x1[4];
#pragma unroll
        for (int kk = 0; kk < 4; ++kk) {
          bv[kk] = cat(trd(pb + kk * 6144), trd(pb + kk * 6144 + 3072));
          x0[kk] = cat(trd(pa + kk * 6144), trd(pa + kk * 6144 + 3072));
          x1[kk] = cat(trd(pa + kk * 6144 + 512), trd(pa + kk * 6144 + 3072 + 512));
        }
        v4f d0 = z4, d1 = z4;
#pragma unroll
        for (int kk = 0; kk < 4; ++kk) { d0 = mfma(x0[kk], bv[kk], d0); d1 = mfma(x1[kk], bv[kk], d1); }
        const v4u dv = {pk(d0.x, d0.y), pk(d0.z, d0.w), pk(d1.x, d1.y), pk(d1.z, d1.w)};
        __builtin_amdgcn_raw_buffer_store_b128(dv, rs_ws, vl16, kvp + t * 4096, SC1);
        keep(dv);
        __builtin_amdgcn_s_setprio(0);
        STAMP(5 + layer * 12);
      }
      load_wo_rest();
      v4u qf = q_gemm(L2E, 0.f);
      asm volatile("" : "+v"(qf) :: "memory");
      if (lead) {
        feature_map(qf, wphi, pp, pm, []() {});
        drain();
      } else {
        pause(lateness(hv, t, srdW, vcol, sh, budget) + T_KV);
        if (w == 0) hint_put(2 * layer + 1);
        fence();
        STAMP(6 + layer * 12);
        v4u kp[16];
#pragma unroll
        for (int i = 0; i < 8; ++i) kp[i] = ld16(rs_ws, vl16, kvp + i * 4096);
        feature_map(qf, wphi, pp, pm, [&]() INL {
          SB();
#pragma unroll
          for (int i = 8; i < 16; ++i) kp[i] = ld16(rs_ws, vl16, kvp + i * 4096);
        });
        const unsigned lo = lane_id();
        const unsigned bit = ((lo >> 4) == ((lo >> 2) & 3)) ? ((lo & 1) ? 0x3f800000u : 0x3f80u) : 0u;
        const unsigned u0 = (lo & 2) ? 0u : bit, u1 = (lo & 2) ? bit : 0u;
        const v4u sa = {u0, u1, 0u, 0u}, sb = {0u, 0u, u0, u1};
        v4f lo4 = z4, hi4 = z4;
        SB();
#pragma unroll
        for (int i = 0; i < 16; i += 2) {
          if (__builtin_expect(__any(has_sent2(kp[i], kp[i + 1])), 0)) respin2(kp[i], kp[i + 1], srdW, vl16, kvp + i * 4096, kvp + (i + 1) * 4096, budget);
          lo4 = mfma(sa, kp[i], lo4); hi4 = mfma(sb, kp[i], hi4);
          lo4 = mfma(sa, kp[i + 1], lo4); hi4 = mfma(sb, kp[i + 1], hi4);
          SB();
        }
        lwr(sl16 + (L_KVF - L_SCR) + (w & 3) * 1024, v4u{pk(lo4.x, lo4.y), pk(lo4.z, lo4.w), pk(hi4.x, hi4.y), pk(hi4.z, hi4.w)});
      }
      VMKNOWN(0);
      wg_bar();
      __builtin_amdgcn_s_setprio(0);
      STAMP(7 + layer * 12);
      o_poll();
      v4u kv[4];
#pragma unroll
      for (int i = 0; i < 4; ++i) kv[i] = lrd(sl16 + (L_KVF - L_SCR) + (i ^ 1) * 1024);
      o1 = mfma(kv[0], pp, z4); o0 = mfma(kv[1], pp, z4);
      o1 = mfma(kv[2], pm, o1); o0 = mfma(kv[3], pm, o0);
      den = xrow(sel3, o1.x) + 1e-6f;
    } else {
      const unsigned hb = W_HALO + rep * 4194304 + h * 262144;
      const unsigned hp = t < 15 ? hb + (t * 8 + w) * 2048 : W_END;
      __builtin_amdgcn_raw_buffer_store_b128(kf, rs_ws, vl16, hp, SC1);
      __builtin_amdgcn_raw_buffer_store_b128(vf, rs_ws, vl16 + 1024, hp, SC1);
      const lptr kw = sl16 + w * 2048, vw = stw + w * 2048, vr = str + w * 2048;
      if (w < 7) lwr(kw + 32768, kf);
      *(LAS v2u*)(vw + 32768 + 1024) = v2u{vf.x, vf.y}; *(LAS v2u*)(vw + 32768 + 1536) = v2u{vf.z, vf.w};
      STAMP(4 + layer * 12);
      if (!lead) { wg_bar(); __builtin_amdgcn_s_setprio(0); }
      load_wo_rest();
      const v4u qf = q_gemm(0.18898223650461357f * L2E, 1.f);
      const float NEG = -3.0e38f;
      v4f sd = mfma(kf, qf, z4);
#pragma unroll
      for (int r = 0; r < 4; ++r) sd[r] = (4 * g + r <= c) ? sd[r] : NEG;
      float m = xmax1(mx(mx3(sd.x, sd.y, sd.z), sd.w));
      auto diag = [&]() INL {
        const v2u a0 = trd(vr + 32768 + 1024), a1 = trd(vr + 32768 + 1536);
        const v4f e = sd - m;
        const v2u pd = {pk(ex2(e.x), ex2(e.y)), pk(ex2(e.z), ex2(e.w))};
        typedef short v4h __attribute__((ext_vector_type(4)));
        o0 = __builtin_amdgcn_mfma_f32_16x16x16bf16_1k(__builtin_bit_cast(v4h, a0), __builtin_bit_cast(v4h, pd), z4, 0, 0, 0);
        o1 = __builtin_amdgcn_mfma_f32_16x16x16bf16_1k(__builtin_bit_cast(v4h, a1), __builtin_bit_cast(v4h, pd), z4, 0, 0, 0);
      };
      diag();
      v4u hk[4], hv[4];
      unsigned hs[2];
#pragma unroll
      for (int i = 0; i < 2; ++i) hs[i] = t - 2 + i >= 0 ? hb + ((t - 2 + i) * 8 + (w & 3)) * 2048 : W_END;
      if (lead) {
        const unsigned lo = lane_id(), vh = lo >= 16 ? OOB : (lo < 2 && (int)lo < t) ? (1 - lo) * 2048 : 4096, sh = W_HINT + 2 * layer * 32768 + (t - 2) * 2048 + h * 128;
        const unsigned hh = __builtin_amdgcn_raw_buffer_load_b32(rs_ws, vh, sh, SC1);
        wg_bar();
        pause(lateness(hh, 2, srdW, vh, sh, budget) + T_H);
        fence();
#pragma unroll
        for (int i = 0; i < 4; ++i) {
          hk[i] = ld16(rs_ws, vl16 + (i & 1) * 8192, hs[i >> 1]);
          hv[i] = ld16(rs_ws, vl16 + (i & 1) * 8192 + 1024, hs[i >> 1]);
        }
        if (LATEFILL) {
          unsigned vf_ = more ? vl16 : OOB;
          fill2(tile_share(x_buf(1)), tile_share(o_buf(2)), vf_); fill2(x_buf(1) + ini_t - 4096, o_buf(2) + ini_t - 4096, vf_);
          fill2(W_HALO + 4194304 + ini_h, W_HALO + 4194304 + 1024 + ini_h, vf_); fill2(W_HALO + 4194304 + ini_h - 8192, W_HALO + 4194304 + 1024 + ini_h - 8192, vf_);
        }
      }
      STAMP(5 + layer * 12);
      auto take_halo = [&](bool ld) INL {
        STAMP(6 + layer * 12);
        if (ld) {
#pragma unroll
          for (int i = 0; i < 4; ++i) {
            if (__builtin_expect(__any(has_sent2(hk[i], hv[i])), 0))
              respin2(hk[i], hv[i], srdW, vl16, hs[i >> 1] + (i & 1) * 8192, hs[i >> 1] + (i & 1) * 8192 + 1024, budget);
            if (t - 2 + (i >> 1) < 0) hk[i].w = 0xfe000000u;
            lwr(kw + (i - 1) * 8192, hk[i]);
            *(LAS v2u*)(vw + (i - 1) * 8192 + 1024) = v2u{hv[i].x, hv[i].y}; *(LAS v2u*)(vw + (i - 1) * 8192 + 1536) = v2u{hv[i].z, hv[i].w};
          }
        }
        wg_bar();
        if (!ld) __builtin_amdgcn_s_setprio(PRIO_AT);
        STAMP(7 + layer * 12);
      };
      {
        const v4f cm = {-m, -m, -m, -m};
        v4f sa, sb; v4u a0, a1;
#pragma unroll
        for (int j = 7; j >= 0; --j) {
          if (j >= 4 && (w >> 1) == 7 - j) take_halo(j < 6);
          if (j == 4) { VMKNOWN(LATEFILL ? 8 : 0); if (w == 7) hint_put(2 * layer + 1); }
          if (j == POLL_OB) o_poll();
          const v4u ka = lrd(kw + j * 4096), kb = lrd(kw + j * 4096 + 2048);
          const v4u n0 = cat(trd(vr + j * 4096 + 1024), trd(vr + j * 4096 + 3072)), n1 = cat(trd(vr + j * 4096 + 1536), trd(vr + j * 4096 + 3584));
          SB();
          if (j < 7) { const v4u pf = pexp(sa, sb); o0 = mfma(a0, pf, o0); o1 = mfma(a1, pf, o1); }
          sa = mfma(ka, qf, cm); sb = mfma(kb, qf, cm);
          a0 = n0; a1 = n1;
          SB();
        }
#pragma unroll
        for (int r = 0; r < 4; ++r) sa[r] = (4 * g + r > c) ? sa[r] : NEG;
        const v4u pf = pexp(sa, sb);
        o1 = mfma(a1, pf, o1); o0 = mfma(a0, pf, o0);
      }
      den = xrow(sel3, o1.x);
      if (__builtin_expect(__any(!(den < 1.0e18f)), 0)) {
        float mt = m;
#pragma unroll 1
        for (int k = 0; k < 16; ++k) {
          v4f s = mfma(lrd(kw + k * 2048), qf, z4);
#pragma unroll
          for (int r = 0; r < 4; ++r) s[r] = (k > 0 || 4 * g + r > c) ? s[r] : NEG;
          mt = mx3(mt, mx(s.x, s.y), mx(s.z, s.w));
        }
        m = xmax1(mt);
        diag();
        const v4f cm = {-m, -m, -m, -m};
#pragma unroll 1
        for (int j = 7; j >= 0; --j) {
          v4f sa = mfma(lrd(kw + j * 4096), qf, cm);
          const v4f sb = mfma(lrd(kw + j * 4096 + 2048), qf, cm);
#pragma unroll
          for (int r = 0; r < 4; ++r) sa[r] = (j > 0 || 4 * g + r > c) ? sa[r] : NEG;
          const v4u pf = pexp(sa, sb);
          o0 = mfma(cat(trd(vr + j * 4096 + 1024), trd(vr + j * 4096 + 3072)), pf, o0);
          o1 = mfma(cat(trd(vr + j * 4096 + 1536), trd(vr + j * 4096 + 3584)), pf, o1);
        }
        den = xrow(sel3, o1.x);
      }
      if (LATEFILL && more) { drain(); wg_bar(); }
    }
    const unsigned os = (isA ? (rep ? o_buf(2) : o_buf(0)) : (rep ? o_buf(3) : o_buf(1))) + tw;
    {
      const float rd = __builtin_amdgcn_rcpf(den);
      o0 *= rd; o1 *= rd;
      const v4u v = {pk(o0.x, o0.y), pk(o0.z, o0.w), pk(o1.x, o1.y), pk(o1.z, o1.w)};
      __builtin_amdgcn_raw_buffer_store_b128(v, rs_ws, g3 ? OOB : pubo, os, SC1);
      __builtin_amdgcn_raw_buffer_store_b64(v2u{v.x, v.y}, rs_ws, g3 ? pubo : OOB, os, SC1);
      keep(v);
    }
    if (!isA) __builtin_amdgcn_s_setprio(0);
    STAMP(8 + layer * 12);
    if (first) {
      const unsigned ptB = ptA + 16 * 86016;
#pragma unroll
      for (int j = 0; j < 8; ++j) park[j] = __builtin_amdgcn_raw_buffer_load_b128(rs_ws, vl16 + (j & 1) * 1024, (j < 6 || w < 4) ? ptB + (j >> 1) * 16384 : W_END, 0);
      VMKNOWN(0);
    }
    if (more) { swap_block(0); swap_block(1); }
    if (w == 7) *(LAS int*)(csg + 768) = lateness(oh, h, srdW, vrow, soh, budget);
    wg_bar();
    pause(__builtin_amdgcn_readfirstlane(*(LAS int*)(csg + 768)) + (isA ? T_OA : T_OB));
    int w2 = w;
    {
      const unsigned op = os - tw + tp + w * 1024, op3 = w < 4 ? op + 24576 : W_END;
      v4u of[4];
#pragma unroll
      for (int i = 0; i < 3; ++i) of[i] = ld16(rs_ws, vl16, op + i * 8192);
      of[3] = ld16(rs_ws, vl16, op3);
      if (first) {
        const unsigned ptB = ptA + 16 * 86016;
        dma2(L_WQB + w * 2048, vl16, vl16, srdW, ptB + L_WQ, ptB + L_WQ);
        if (w < 6) dma2(L_WQB + 16384 + w * 2048, vl16, vl16, srdW, ptB + L_WQ + 16384, ptB + L_WQ + 16384);
      }
      if (more) { swap_block(2); if (w < 4) swap_block(3); }
      STAMP(9 + layer * 12);
      const lptr sw = sl16 + w * 1024;
#pragma unroll
      for (int i = 0; i < 4; i += 2) {
        if (__builtin_expect(__any(has_sent2(of[i], of[i + 1])), 0)) respin2(of[i], of[i + 1], srdW, vl16, op + i * 8192, i ? op3 : op + 8192, budget);
        lwr(sw + i * 8192, of[i]); lwr(sw + (i + 1) * 8192, of[i + 1]);
      }
      wg_bar();
      if (w == 7) {
        if (more) {
          hint_put(8 + layer);
          __builtin_amdgcn_s_sleep(P_X);
          const unsigned sx = W_HINT + (8 + layer) * 32768 + t * 2048;
          *(LAS int*)(csg + 768) = lateness(__builtin_amdgcn_raw_buffer_load_b32(rs_ws, vrow, sx, SC1), h, srdW, vrow, sx, budget);
        }
      }
      asm volatile("" : "+s"(w2));
      if (w2 != 7) {
        v4u bq[8];
#pragma unroll
        for (int i = 0; i < 4; ++i) { bq[2 * i] = lrd(sl16 + i * 1024); SB(); bq[2 * i + 1] = lrd(sl16 + 14336 + i * 1024); SB(); }
#pragma unroll
        for (int s = 0; s < 14; ++s) {
          const int i = (2 * s) & 7;
          if (!(s & 1)) { if (s < 12) LGKNOWN(4); else LGKNOWN(0); SB(); }
          res0 = mfma(wof[s], bq[i], res0); SB(); if (s + 4 < 14) { bq[i] = lrd(sl16 + (s + 4) * 1024); SB(); }
          res1 = mfma(wof[s], bq[i + 1], res1); SB(); if (s + 4 < 14) { bq[i + 1] = lrd(sl16 + 14336 + (s + 4) * 1024); SB(); }
          if (s >= 1 && s - 1 < WO1) load_wo(s - 1, !isA, more);
        }
      }
    }
    if (more) {
      const v4u xo = swap16(v4u{pk(res0.x, res0.y), pk(res0.z, res0.w), pk(res1.x, res1.y), pk(res1.z, res1.w)});
      const unsigned xd = (isA ? (rep ? x_buf(2) : x_buf(0)) : x_buf(1)) + tp;
      __builtin_amdgcn_raw_buffer_store_b128(xo, rs_ws, pubx, w < 7 ? xd : W_END, SC1);
      keep(xo);
      STAMP(10 + layer * 12);
      *(LAS float*)(lbase(L_SCR + 32768 + w * 256 + (vl16 >> 2))) = sumsq8(xo);
      wg_bar();
      const int late = __builtin_amdgcn_readfirstlane(*(LAS int*)(csg + 768));
      if (w == 7) {
        const lptr sp = lbase(L_SCR + 32768 + (vl16 >> 2));
        float q = 0.f;
#pragma unroll
        for (int i = 0; i < 7; ++i) q += *(LAS float*)(sp + i * 256);
        auto r = __builtin_amdgcn_permlane32_swap(asu(q), asu(q), false, false);
        const unsigned r0 = r[0], r1 = r[1], lo = lane_id();
        __builtin_amdgcn_raw_buffer_store_b32(asu(asf(r0) + asf(r1)), rs_ws, lo < 32 ? (lo >> 4) * 256 + (lo & 15) * 4 : OOB,
                                              W_ST + layer * 32768 + (t * 8 + 2 * (h >> 2)) * 256 + (h & 3) * 64, SC1);
      }
      pause(late + T_X - (w2 == 7 ? 8 : 0));
    }
  };
#pragma unroll 1
  for (int rep = 0; rep < 2; ++rep) {
    run_layer(TagA{}, rep);
    run_layer(TagB{}, rep);
  }
  {
    const rsrc_t rs_o = __builtin_amdgcn_make_buffer_rsrc(p.out, 0, SEQ * DM * 2, 0x00020000);
    const unsigned lo = lane_id();
    const unsigned oo = (t * 128 + (2 * (h >> 2) + ((lo >> 4) & 1)) * 16 + (lo & 15)) * (DM * 2) + (2 * ft + (lo >> 5)) * 16;
    const v4u xo = swap16(v4u{pk(res0.x, res0.y), pk(res0.z, res0.w), pk(res1.x, res1.y), pk(res1.z, res1.w)});
    if (w_ < 7) __builtin_amdgcn_raw_buffer_store_b128(xo, rs_o, oo, 0, 0);
    STAMP(46);
  }
}
}

extern "C" void kernel_launch(void* const* d_in, const int* in_sizes, int n_in, void* d_out, int out_size, void* d_ws, size_t ws_size,
                              hipStream_t stream) {
  (void)in_sizes; (void)n_in; (void)out_size; (void)ws_size;
  (void)hipFuncSetAttribute((const void*)fused_layers, hipFuncAttributeMaxDynamicSharedMemorySize, (int)L_TOTAL);
  Params p;
  p.x = d_in[0]; p.wA = d_in[1]; p.wphi = d_in[2]; p.woA = d_in[3]; p.wB = d_in[5]; p.woB = d_in[6]; p.out = d_out; p.ws = d_ws;
  hipLaunchKernelGGL(prepare, dim3(256), dim3(512), 36864, stream, p);
  hipLaunchKernelGGL(fused_layers, dim3(256), dim3(512), L_TOTAL, stream, p);
}
```

```cpp
#include <hip/hip_runtime.h>
#include <stdint.h>

#define LAS __attribute__((address_space(3)))
#define DEV __device__ __forceinline__
typedef unsigned v4u __attribute__((ext_vector_type(4)));
typedef unsigned v2u __attribute__((ext_vector_type(2)));
typedef float v4f __attribute__((ext_vector_type(4)));
typedef float v2f __attribute__((ext_vector_type(2)));
typedef int v4i __attribute__((ext_vector_type(4)));
typedef short v4s __attribute__((ext_vector_type(4)));
typedef __bf16 v8bf __attribute__((ext_vector_type(8)));
typedef __bf16 v2bf __attribute__((ext_vector_type(2)));
typedef __amdgpu_buffer_rsrc_t rsrc_t;

#define TPX 4
#define DLY_T 3
#define WO1 6
#define POLL_OB 2
#define T_KV 4
#define T_H 24
#define T_OA 0
#define T_OB 12
#define P_X 10
#define T_X 8
#define NXW 2
#define XWIN 8
#define P_F 8
#define WIN 4

namespace {
constexpr int SEQ = 2048, DM = 448;
constexpr unsigned L_WK = 0, L_WV = 28672, L_WQ = 57344, L_WQB = 86016, L_SCR = 114688, L_TOTAL = 163840;
constexpr unsigned L_CS = L_SCR + 23 * 2048;
constexpr unsigned L_KVF = L_SCR + 40960;
constexpr unsigned TILEBUF = 16 * 8 * 14 * 1024;
constexpr unsigned W_KEY = 0, W_FLAG = 2048, W_HINT = 4096, W_FLAGC = W_HINT + 11 * 32768, W_ST = W_HINT + 12 * 32768, W_KVP = W_ST + 4 * 131072, W_O = W_KVP + 2 * 1048576,
                   W_HALO = W_O + 4 * 2097152, W_X = W_HALO + 2 * 4194304, W_PT = W_X + 4 * TILEBUF, W_WO = W_PT + 2 * 16 * 86016,
                   W_CS = W_WO + 2 * 28 * 14336, W_PHI = W_CS + 16 * 84 * 128, W_END = W_PHI + 16 * 2048;
constexpr unsigned W_DBG = 64u << 20;
constexpr int SC1 = 16;
constexpr unsigned OOB = 0x40000000u;

DEV v4f mfma(v4u a, v4u b, v4f c) {
  return __builtin_amdgcn_mfma_f32_16x16x32_bf16(__builtin_bit_cast(v8bf, a), __builtin_bit_cast(v8bf, b), c, 0, 0, 0);
}
DEV unsigned pk(float a, float b) { v2f f = {a, b}; v2bf r = __builtin_convertvector(f, v2bf); return __builtin_bit_cast(unsigned, r); }
typedef LAS char* lptr;
DEV lptr lbase(unsigned v) { asm volatile("" : "+v"(v)); return (lptr)(size_t)v; }
DEV v2u trd(lptr a) { v4s r = __builtin_amdgcn_ds_read_tr16_b64_v4i16((LAS v4s*)a); return __builtin_bit_cast(v2u, r); }
DEV v4u cat(v2u a, v2u b) { return v4u{a.x, a.y, b.x, b.y}; }
DEV v4u lrd(lptr a) { return *(LAS v4u*)a; }
DEV void lwr(lptr a, v4u v) { *(LAS v4u*)a = v; }
#define VMWAIT(n) asm volatile("s_waitcnt vmcnt(" #n ")" ::: "memory")
DEV float asf(unsigned u) { return __builtin_bit_cast(float, u); }
DEV unsigned asu(float f) { return __builtin_bit_cast(unsigned, f); }
DEV void wg_bar() { asm volatile("s_waitcnt lgkmcnt(0)\n\ts_barrier" ::: "memory"); }
DEV void drain() { asm volatile("s_waitcnt vmcnt(0)" ::: "memory"); }
DEV void fence() { asm volatile("" ::: "memory"); }
DEV float ex2(float x) { return __builtin_amdgcn_exp2f(x); }

DEV unsigned lane_id() { unsigned r; asm volatile("v_mbcnt_lo_u32_b32 %0, -1, 0\n\tv_mbcnt_hi_u32_b32 %0, -1, %0" : "=v"(r)); __builtin_assume(r < 64); return r; }
DEV float mx(float a, float b) { return __builtin_elementwise_maximum(a, b); }
DEV float mn(float a, float b) { return __builtin_elementwise_minimum(a, b); }
DEV float mx3(float a, float b, float c) { return mx(mx(a, b), c); }
DEV float mn3(float a, float b, float c) { return mn(mn(a, b), c); }
DEV float xrow(float sel, float v) { const v4f z = {0.f, 0.f, 0.f, 0.f}; const v4f r = __builtin_amdgcn_mfma_f32_16x16x4f32(sel, v, z, 0, 0, 0); return r.x; }
DEV v4f xrow4(float sel, float v) { const v4f z = {0.f, 0.f, 0.f, 0.f}; return __builtin_amdgcn_mfma_f32_16x16x4f32(sel, v, z, 0, 0, 0); }
DEV v4u swap16(v4u v) {
  auto r = __builtin_amdgcn_permlane16_swap(v.x, v.z, false, false);
  auto q = __builtin_amdgcn_permlane16_swap(v.y, v.w, false, false);
  const unsigned r0 = r[0], r1 = r[1], q0 = q[0], q1 = q[1];
  return v4u{r0, q0, r1, q1};
}
DEV float sumsq8(v4u v) {
  const v4f lo = __builtin_bit_cast(v4f, v << 16), hi = __builtin_bit_cast(v4f, v & 0xffff0000u), q = lo * lo + hi * hi;
  return (q.x + q.y) + (q.z + q.w);
}
DEV void xmax2(float& a, float& b) {
  auto r = __builtin_amdgcn_permlane32_swap(asu(a), asu(b), false, false);
  unsigned r0 = r[0], r1 = r[1];
  float z = mx(asf(r0), asf(r1));
  auto q = __builtin_amdgcn_permlane16_swap(asu(z), asu(z), false, false);
  unsigned q0 = q[0], q1 = q[1];
  z = mx(asf(q0), asf(q1));
  auto p = __builtin_amdgcn_permlane32_swap(asu(z), asu(z), false, false);
  unsigned p0 = p[0], p1 = p[1];
  a = asf(p0); b = asf(p1);
}
DEV float xmax1(float a) {
  auto r = __builtin_amdgcn_permlane32_swap(asu(a), asu(a), false, false);
  unsigned r0 = r[0], r1 = r[1];
  float z = mx(asf(r0), asf(r1));
  auto q = __builtin_amdgcn_permlane16_swap(asu(z), asu(z), false, false);
  unsigned q0 = q[0], q1 = q[1];
  return mx(asf(q0), asf(q1));
}
DEV void dma2(unsigned ldsdst, unsigned va, unsigned vb, v4u srd, unsigned sa, unsigned sb) {
  asm volatile("s_mov_b32 m0, %0\n\ts_nop 3\n\tbuffer_load_dwordx4 %1, %3, %4 offen lds\n\tbuffer_load_dwordx4 %2, %3, %5 offen offset:1024 lds"
               :: "s"(ldsdst), "v"(va), "v"(vb), "s"(srd), "s"(sa), "s"(sb) : "memory");
}
DEV void keep(v4u v) { asm volatile("s_nop 1" :: "v"(v) : "memory"); }
DEV void keep(v4u v, v4u u) { asm volatile("s_nop 1" :: "v"(v), "v"(u) : "memory"); }
DEV void dma2s(unsigned ldsdst, unsigned v, v4u srd, unsigned so) {
  asm volatile("s_mov_b32 m0, %0\n\ts_nop 3\n\tbuffer_load_dwordx4 %1, %2, %3 offen sc1 lds\n\tbuffer_load_dwordx4 %1, %2, %3 offen offset:1024 sc1 lds"
               :: "s"(ldsdst), "v"(v), "s"(srd), "s"(so) : "memory");
}
DEV v4u ld16(rsrc_t rs, unsigned v, unsigned so) {
  const v4u r = __builtin_amdgcn_raw_buffer_load_b128(rs, v, so, SC1);
  __builtin_amdgcn_sched_barrier(0);
  return r;
}
DEV v4u mk_srd(const void* p, unsigned bytes) {
  unsigned long long a = (unsigned long long)p;
  return v4u{(unsigned)a, (unsigned)(a >> 32) & 0xffffu, bytes, 0x00020000u};
}
DEV v4u imma(v4u a, v4u b, v4u c) {
  const v4i r = __builtin_amdgcn_mfma_i32_16x16x64_i8(__builtin_bit_cast(v4i, a), __builtin_bit_cast(v4i, b), __builtin_bit_cast(v4i, c), 0, 0, 0);
  return __builtin_bit_cast(v4u, r);
}
DEV v4u weights(unsigned lo) {
  const unsigned a = (lo + 1) * 0x9e3779b1u, b = (a ^ (a >> 15)) * 0x85ebca77u, c = (b ^ (b >> 13)) * 0xc2b2ae3du, d = (c ^ (c >> 16)) * 0x27d4eb2fu;
  return v4u{(a ^ (a >> 16)) | 0x01010101u, (b ^ (b >> 16)) | 0x01010101u, (c ^ (c >> 16)) | 0x01010101u, (d ^ (d >> 16)) | 0x01010101u};
}
DEV void keys(unsigned e, unsigned& k0, unsigned& k1) { k0 = e * 0x9e3779b1u + 0x7f4a7c15u; k1 = e * 0x85ebca77u + 0x165667b1u; }
DEV unsigned xsum2u(unsigned a, unsigned b) {
  auto r = __builtin_amdgcn_permlane32_swap(a, b, false, false);
  const unsigned r0 = r[0], r1 = r[1], z = r0 + r1;
  auto q = __builtin_amdgcn_permlane16_swap(z, z, false, false);
  const unsigned q0 = q[0], q1 = q[1];
  return q0 + q1;
}
DEV void reload4(v4u& a, v4u& b, v4u& c, v4u& d, v4u srd, unsigned voff, unsigned sa, unsigned sb, unsigned sc, unsigned sd) {
  asm volatile("s_sleep 2\n\tbuffer_load_dwordx4 %0, %4, %5, %6 offen sc1\n\tbuffer_load_dwordx4 %1, %4, %5, %7 offen sc1\n\t"
               "buffer_load_dwordx4 %2, %4, %5, %8 offen sc1\n\tbuffer_load_dwordx4 %3, %4, %5, %9 offen sc1\n\ts_waitcnt vmcnt(0)"
               : "+v"(a), "+v"(b), "+v"(c), "+v"(d) : "v"(voff), "s"(srd), "s"(sa), "s"(sb), "s"(sc), "s"(sd) : "memory");
}
DEV bool retry(bool bad, int& budget) {
  if (__builtin_expect(!__any(bad), 1)) return false;
  budget = max(budget - 1, 0);
  return __any(budget != 0);
}

DEV unsigned clock32() { unsigned long long t; asm volatile("s_memrealtime %0\n\ts_waitcnt lgkmcnt(0)" : "=s"(t) :: "memory"); return (unsigned)t; }
DEV int lateness(v2u v, int own, unsigned key, v4u srd, unsigned voff, unsigned soff, int& budget) {
  asm volatile("" : "+v"(v) :: "memory");
  int waited = 0;
  if (__builtin_expect(__any(v.y != key && voff < OOB), 0)) {
    waited = clock32();
    do {
      asm volatile("s_sleep 1\n\tbuffer_load_dwordx2 %0, %1, %2, %3 offen sc1\n\ts_waitcnt vmcnt(0)" : "+v"(v) : "v"(voff), "s"(srd), "s"(soff) : "memory");
    } while (retry(v.y != key && voff < OOB, budget));
    waited = clock32() - waited;
  }
  const unsigned tv = v.x;
  int d = (int)(tv - __builtin_amdgcn_readlane(tv, own));
  d = max(d, __builtin_amdgcn_update_dpp(0, d, 0x128, 0xf, 0xf, false));
  d = max(d, __builtin_amdgcn_update_dpp(0, d, 0x124, 0xf, 0xf, false));
  d = max(d, __builtin_amdgcn_update_dpp(0, d, 0x122, 0xf, 0xf, false));
  d = max(d, __builtin_amdgcn_update_dpp(0, d, 0x121, 0xf, 0xf, false));
  return min(__builtin_amdgcn_readfirstlane(d), 1000) - waited;
}
DEV void pause(int ticks) {
  asm volatile("s_cmp_lt_i32 %0, 1\n\ts_cbranch_scc1 2f\n1:\n\ts_sleep 1\n\ts_sub_i32 %0, %0, 4\n\ts_cmp_gt_i32 %0, 0\n\ts_cbranch_scc1 1b\n2:" : "+s"(ticks) :: "memory", "scc");
}

template <class F>
DEV void feature_map(v4u vf, const v4u (&wphi)[2], v4u& plus, v4u& minus, F mid) {
  const v4f z4 = {0.f, 0.f, 0.f, 0.f};
  const v4f p0 = mfma(wphi[0], vf, z4), p1 = mfma(wphi[1], vf, z4);
  float hi = mx3(mx3(mx3(p0.x, p0.y, p0.z), p0.w, p1.x), p1.y, p1.z);
  float lo = -mn3(mn3(mn3(p0.x, p0.y, p0.z), p0.w, p1.x), p1.y, p1.z);
  xmax2(hi, lo);
  mid();
  const v2f p01 = {p0.x, p0.y}, p23 = {p0.z, p0.w}, p45 = {p1.x, p1.y}, h2 = {hi, hi}, l2 = {lo, lo};
  v2f a01 = p01 - h2, a23 = p23 - h2, a45 = p45 - h2, b01 = p01 + l2, b23 = p23 + l2, b45 = p45 + l2;
  asm volatile("" : "+v"(a01), "+v"(a23), "+v"(a45), "+v"(b01), "+v"(b23), "+v"(b45));
  const v2f e01 = {ex2(a01.x), ex2(a01.y)}, e23 = {ex2(a23.x), ex2(a23.y)}, e45 = {ex2(a45.x), ex2(a45.y)};
  const float e6 = ex2(p1.z - hi);
  const v2f se = (e01 + e23) + e45;
  v4f te = xrow4(1.f, (se.x + e6) + se.y);
  __builtin_amdgcn_sched_barrier(0);
  const v2f f01 = {ex2(-b01.x), ex2(-b01.y)}, f23 = {ex2(-b23.x), ex2(-b23.y)}, f45 = {ex2(-b45.x), ex2(-b45.y)};
  const float f6 = ex2(-(p1.z + lo));
  const v2f sf = (f01 + f23) + f45;
  v4f tf = xrow4(1.f, (sf.x + f6) + sf.y);
  __builtin_amdgcn_sched_barrier(0);
  const float re = __builtin_amdgcn_rcpf(te.x);
  asm volatile("" :: "v"(te));
  const v2f x01 = e01 * re, x23 = e23 * re, x45 = e45 * re;
  plus = v4u{pk(x01.x, x01.y), pk(x23.x, x23.y), pk(x45.x, x45.y), pk(e6 * re, 0.f)};
  __builtin_amdgcn_sched_barrier(0);
  const float rf = __builtin_amdgcn_rcpf(tf.x);
  asm volatile("" :: "v"(tf));
  const v2f y01 = f01 * rf, y23 = f23 * rf, y45 = f45 * rf;
  minus = v4u{pk(y01.x, y01.y), pk(y23.x, y23.y), pk(y45.x, y45.y), pk(f6 * rf, 0.f)};
}

DEV v4u pexp(v4f a, v4f b) {
  return v4u{pk(ex2(a.x), ex2(a.y)), pk(ex2(a.z), ex2(a.w)), pk(ex2(b.x), ex2(b.y)), pk(ex2(b.z), ex2(b.w))};
}
#define VMKNOWN(n) __builtin_amdgcn_s_waitcnt(((n) & 15) | (((n) >> 4) << 14) | 0xf70)
#define LGKNOWN(n) __builtin_amdgcn_s_waitcnt(0xc07f | ((n) << 8))
#define SB() __builtin_amdgcn_sched_barrier(0)
#define INL __attribute__((always_inline))

#define STAMP(i) do {} while (0)

template <int N> struct Layer { static constexpr int value = N; };
struct Params {
  const void* x; const void* wA; const void* wphi; const void* woA; const void* wB; const void* woB; void* out; void* ws;
};

__global__ void __launch_bounds__(512, 2) fused_layers(Params p) {
  const int b = blockIdx.x, xg = b & 7, xi = b >> 3;
  constexpr int HPX = 32 / TPX, NGT = 16 / TPX;
  const int t = (xg % NGT) * TPX + xi / HPX;
  const int h = (xg / NGT) * HPX + xi % HPX;
  const int wid = __builtin_amdgcn_readfirstlane(threadIdx.x >> 6) & 7;
  const int w_ = wid < 4 ? 7 - wid : wid - 4;
  const int l = threadIdx.x & 63, c = l & 15, g = l >> 4;
  const bool g3 = g == 3;
  unsigned key0, key1;
  const unsigned epoch = *(const unsigned*)p.ws + 1u;
  keys(epoch, key0, key1);
  STAMP(0);
  constexpr unsigned L_XS = 49152;
  const unsigned at = w_ * 2048, chunk16 = ((l & 3) ^ ((l >> 5) << 1)) * 16;
  const int job = t + 16 * w_, wj = (job - 85) * 16 + h;
  const int jt = job < 84 ? job / 42 : wj / 196, jr = job < 84 ? job - 42 * jt : wj - 196 * jt, jp = jr / 14, js = jr - 14 * jp;
  if (job < 84) {
    const unsigned v = (l >> 2) * (3 * DM * 2) + chunk16, so = js * (32 * 3 * DM * 2) + (jp == 2 ? 0 : (jp + 1) * DM * 2) + 56 * h;
    dma2(at, v, v, mk_srd(jt ? p.wB : p.wA, DM * 3 * DM * 2), so, so + 16 * 3 * DM * 2 - 1024);
  } else if (job == 84) {
    const unsigned e = (l >> 2) & 7, g0 = l >> 5, d0 = e < 4 ? 4 * g0 + e : 12 + 3 * g0 + e, d1 = d0 + (e < 4 ? 8 : 6);
    dma2(at, e < 7 ? d0 * 56 + chunk16 + 1024 : OOB, e < 7 ? d1 * 56 + chunk16 : OOB,
         mk_srd((const char*)p.wphi - 1024, 16 * 28 * 28 * 2 + 1024), h * 28 * 28 * 2, h * 28 * 28 * 2);
  } else if (wj < 392) {
    unsigned v[2];
#pragma unroll
    for (int j = 0; j < 2; ++j) {
      const int n = 4 * js + 2 * j + (l >> 5), e = (l >> 2) & 7, hh = n / 3, gg = n - 3 * hh;
      const unsigned ra = 28 * hh + 4 * gg + 16 * (e >> 2), rb = 28 * (2 * (n - 48) + (e >> 2)) + 12;
      v[j] = ((js < 12 ? ra : rb) + (e & 3)) * (DM * 2) + chunk16 + 1024 - j * 1024;
    }
    dma2(at, v[0], v[1], mk_srd((const char*)(jt ? p.woB : p.woA) - 1024, DM * DM * 2 + 1024), 64 * jp, 64 * jp);
  }
  STAMP(51);
  *(LAS unsigned*)(lbase(16384)) = 0u;
  const unsigned xsb = L_XS + w_ * 14336;
  const v4u srdX = mk_srd(p.x, SEQ * DM * 2);
  if (w_ == 7) {
    constexpr int PER = 896 / HPX;
    unsigned t0, t1;
    asm volatile("buffer_load_dword %0, %2, %4, %5 offen\n\tbuffer_load_dword %1, %3, %4, %5 offen\n\ts_waitcnt vmcnt(0)"
                 : "=&v"(t0), "=&v"(t1) : "v"(l < PER ? l * 128 : OOB), "v"(l + 64 < PER ? (l + 64) * 128 : OOB), "s"(srdX), "s"(t * 114688 + (h % HPX) * PER * 128) : "memory");
  }
  const rsrc_t rs_ws = __builtin_amdgcn_make_buffer_rsrc(p.ws, 0, W_END, 0x00020000);
  const v4u srdW = mk_srd(p.ws, W_END);
  unsigned vl16 = l * 16;
  asm volatile("" : "+v"(vl16));
  auto done = [&]() INL {
    unsigned n = 0;
    if (l == 0) n = __hip_atomic_fetch_add((LAS unsigned*)lbase(16384), 1u, __ATOMIC_RELAXED, __HIP_MEMORY_SCOPE_WORKGROUP);
    if (__builtin_amdgcn_readfirstlane(n) == 6)
      __builtin_amdgcn_raw_buffer_store_b64(v2u{key0, key1}, rs_ws, l < 16 ? W_FLAGC + (t * 16 + h) * 128 + l * 8 : l == 16 ? W_FLAG + b * 8 : OOB, 0, SC1);
    STAMP(48);
  };
  if (w_ != 7) {
    const unsigned a0 = at + (8 * g + (c >> 2)) * 64 + ((((l & 3) >> 1) ^ ((g & 1) << 1)) * 16) + (l & 1) * 8;
    const lptr r0 = lbase(a0), r1 = lbase(a0 ^ 32);
    const bool spread = job == 84 || (job < 84 && jp != 1), pad = spread && c >= 12;
    const unsigned slot = !spread ? c : c >= 12 ? 4 * c - 45 : c + ((c * 11) >> 5), one = 0x3f803f80u;
    const unsigned o1 = (job > 84 ? 14336 : 1024) + ((l & 48) + slot) * 16;
    const unsigned dst = job < 84 ? W_PT + (jt * 16 + h) * 86016 + jr * 2048 : job == 84 ? W_PHI + h * 2048
                         : wj < 392 ? W_WO + (jt * 28 + 2 * jp) * 14336 + js * 1024 : W_END;
    const v4u one8 = {one, one, one, one};
    const v4f z4 = {0.f, 0.f, 0.f, 0.f};
    drain();
    STAMP(47);
    const v4u f0 = cat(trd(r0), trd(r0 + 256));
    v4u f1 = cat(trd(r1), trd(r1 + 256));
    f1 = v4u{pad ? one : f1.x, pad ? one : f1.y, pad ? one : f1.z, pad ? one : f1.w};
    __builtin_amdgcn_raw_buffer_store_b128(f0, rs_ws, vl16, dst, SC1);
    __builtin_amdgcn_raw_buffer_store_b128(f1, rs_ws, o1, dst, SC1);
    keep(f0, f1);
    const v4f c0 = mfma(one8, f0, z4), c1 = mfma(one8, f1, z4);
    __builtin_amdgcn_raw_buffer_store_b32(asu(g ? c1.x : c0.x), rs_ws, g == 0 ? c * 4 : g == 1 ? 64 + slot * 4 : OOB,
                                          job < 84 ? W_CS + (h * 84 + job) * 128 : W_END, SC1);
  }
  STAMP(52);
  wg_bar();
  STAMP(53);
  if (w_ >= 8 - NXW) {
    const unsigned vx0 = (l >> 3) * (DM * 2) + ((l & 7) ^ (l >> 4)) * 16, vx1 = ((l >> 3) + 8) * (DM * 2) + ((l & 7) ^ (4 + (l >> 4))) * 16 - 1024;
#pragma unroll 1
    for (int u = 7 - w_; u < 8; u += NXW) {
#pragma unroll
      for (int j = 0; j < 7; ++j) {
        dma2(L_XS + u * 14336 + j * 2048, vx0, vx1, srdX, (t * 128 + u * 16) * (DM * 2) + j * 128, (t * 128 + u * 16) * (DM * 2) + j * 128);
        asm volatile("s_waitcnt vmcnt(%0)" :: "n"(XWIN) : "memory");
        if (NXW > 1 && j == XWIN / 2 && u == 1) done();
      }
    }
  }
  if (w_ < 8 - NXW) { drain(); done(); }
  const int ft = 7 * (h & 3) + min(w_, 6), nx = 2 * ft + (g >> 1);
  const unsigned pubx = (g & 1) * 14336 + (nx >> 2) * 1024 + ((nx & 3) * 16 + c) * 16;
  const unsigned tw = (t * 8 + w_) * 14336, tp = (t * 8 + 2 * (h >> 2)) * 14336;
  const unsigned ptA = W_PT + h * 86016 + w_ * 2048;
  int budget = 1 << 16;
  v4u wphi[2];
  v4u park[8];
  v4u xf[14];
  v4f k0, k1, v0, v1;
  float var;
  v4f res0, res1;
  {
    if (w_ == 0) {
      v2u f;
      __builtin_amdgcn_s_sleep(P_F);
      do {
        asm volatile("s_sleep 2\n\tbuffer_load_dwordx2 %0, %1, %2, %3 offen sc1\n\ts_waitcnt vmcnt(0)"
                     : "=&v"(f) : "v"(l < 16 ? l * 2048 : OOB), "s"(srdW), "s"(W_FLAGC + h * 128) : "memory");
      } while (retry(l < 16 && (f.x != key0 || f.y != key1), budget));
    }
    drain();
    STAMP(49);
    wg_bar();
    STAMP(50);
#pragma unroll
    for (int j = 0; j < 3; ++j) {
      const unsigned o = (w_ >> 2) * L_WV + (4 * j + (w_ & 3)) * 2048;
      if (j < 2 || w_ < 6) dma2s(o, vl16, srdW, ptA - at + o);
    }
    const unsigned xa = xsb + c * 128 + (g ^ (c >> 1)) * 16;
    const lptr xa0 = lbase(xa), xa1 = lbase(xa ^ 64);
#pragma unroll
    for (int j = 0; j < 7; ++j) { xf[2 * j] = lrd(xa0 + j * 2048); xf[2 * j + 1] = lrd(xa1 + j * 2048); }
    const v4u xr = swap16(lrd(lbase(L_XS + (2 * (h >> 2) + (g & 1)) * 14336 + (nx >> 3) * 2048 + c * 128 + ((nx & 7) ^ (c >> 1)) * 16)));
    STAMP(54);
    wg_bar();
    {
      const unsigned o = w_ < 2 ? (12 + w_) * 2048 : L_WV + (8 + w_) * 2048;
      if (w_ < 6) dma2s(o, vl16, srdW, ptA - at + o);
    }
    v4u cp0, cp1;
    cp0 = ld16(rs_ws, vl16, w_ < 6 ? W_CS + (h * 84 + 14 * w_) * 128 : W_END);
    cp1 = ld16(rs_ws, vl16 < 768 ? vl16 + 1024 : OOB, w_ < 6 ? W_CS + (h * 84 + 14 * w_) * 128 : W_END);
    wphi[0] = ld16(rs_ws, vl16, W_PHI + h * 2048);
    wphi[1] = ld16(rs_ws, vl16 + 1024, W_PHI + h * 2048);
    res0 = v4f{asf(xr.x << 16), asf(xr.x & 0xffff0000u), asf(xr.y << 16), asf(xr.y & 0xffff0000u)};
    res1 = v4f{asf(xr.z << 16), asf(xr.z & 0xffff0000u), asf(xr.w << 16), asf(xr.w & 0xffff0000u)};
    v4f gram = {0.f, 0.f, 0.f, 0.f};
#pragma unroll
    for (int s = 0; s < 14; ++s) gram = mfma(xf[s], xf[s], gram);
    var = xrow(1.f / DM, c == 4 * g ? gram.x : c == 4 * g + 1 ? gram.y : c == 4 * g + 2 ? gram.z : c == 4 * g + 3 ? gram.w : 0.f);
    asm volatile("" : "+v"(var));
    STAMP(55);
    v4f cs = __builtin_bit_cast(v4f, cp0) + __builtin_bit_cast(v4f, cp1);
#pragma unroll
    for (int i = 0; i < 4; ++i) {
      float u = cs[i];
      u += asf(__builtin_amdgcn_update_dpp(0, asu(u), 0x128, 0xf, 0xf, false));
      cs[i] = xrow(1.f, u);
    }
    if (w_ < 6) *(LAS v4f*)(lbase(L_CS + w_ * 128 + (vl16 & 127))) = cs;
    if (w_ == 7) {
      v4u f0, f1;
      do {
        asm volatile("s_nop 4\n\tbuffer_load_dwordx4 %0, %2, %3, %4 offen sc1\n\tbuffer_load_dwordx4 %1, %2, %3, %4 offen offset:1024 sc1\n\ts_waitcnt vmcnt(0)"
                     : "=&v"(f0), "=&v"(f1) : "v"(vl16), "s"(srdW), "s"(W_FLAG) : "memory");
      } while (retry(f0.x != key0 || f0.z != key0 || f1.x != key0 || f1.z != key0 || f0.y != key1 || f0.w != key1 || f1.y != key1 || f1.w != key1, budget));
    }
    VMKNOWN(0);
    wg_bar();
    STAMP(2);
  }

  auto hint_put = [&](int slot) INL {
    __builtin_amdgcn_raw_buffer_store_b64(v2u{clock32(), key0}, rs_ws, vl16 < 256 ? vl16 >> 1 : OOB, W_HINT + slot * 32768 + (t * 16 + h) * 128, SC1);
  };
  auto hint_get = [&](unsigned v, unsigned so) INL { SB(); const v2u r = __builtin_amdgcn_raw_buffer_load_b64(rs_ws, v, so, SC1); SB(); return r; };
  const unsigned q4 = (l & 15) >> 2, p4 = l & 3;
  const lptr l16 = (lptr)(size_t)vl16, lq16 = lbase(L_WQ + l * 16);
  const lptr stw = lbase(L_SCR + c * 32 + ((g ^ (c >> 2)) * 8));
  const lptr str = lbase(L_SCR + (4 * g + q4) * 32 + ((p4 ^ g) * 8));
  const lptr sl16 = lbase(L_SCR + l * 16), csg = lbase(L_CS + g * 16);
  const unsigned vcol = l < 16 ? l * 2048 : OOB, vrow = l < 16 ? l * 128 : OOB;
  const float sel3 = g3 ? 1.f : 0.f;
  v4u hwt = weights(l);
  if (g3) { hwt.z = 0u; hwt.w = 0u; }
  const v4u zu = {0u, 0u, 0u, 0u};

  v4u wof[14];
  const unsigned wos = W_WO + ft * 14336;
  auto kv_gemm = [&](auto step) INL {
    const v4f z4 = {0.f, 0.f, 0.f, 0.f};
    k0 = z4; k1 = z4; v0 = z4; v1 = z4;
    v4u a[8];
#pragma unroll
    for (int i = 0; i < 2; ++i) { a[4 * i] = lrd(l16 + i * 2048); a[4 * i + 1] = lrd(l16 + i * 2048 + 1024); a[4 * i + 2] = lrd(l16 + L_WV + i * 2048); a[4 * i + 3] = lrd(l16 + L_WV + i * 2048 + 1024); SB(); }
#pragma unroll
    for (int s = 0; s < 14; ++s) {
      const int i = 4 * (s & 1), o = (s + 2) * 2048;
      if (s < 13) LGKNOWN(4); else LGKNOWN(0);
      SB();
      k1 = mfma(a[i + 1], xf[s], k1); SB();
      step(s);
      if (s + 2 < 14) { a[i + 1] = lrd(l16 + o + 1024); SB(); }
      k0 = mfma(a[i], xf[s], k0); SB(); if (s + 2 < 14) { a[i] = lrd(l16 + o); SB(); }
      v0 = mfma(a[i + 2], xf[s], v0); SB(); if (s + 2 < 14) { a[i + 2] = lrd(l16 + L_WV + o); SB(); }
      v1 = mfma(a[i + 3], xf[s], v1); SB(); if (s + 2 < 14) { a[i + 3] = lrd(l16 + L_WV + o + 1024); SB(); }
    }
  };
  kv_gemm([&](int s) INL {
    if (s == 0) dma2s(L_WQ + at, vl16, srdW, ptA + L_WQ);
    if (s == 3 && w_ < 6) dma2s(L_WQ + 16384 + at, vl16, srdW, ptA + L_WQ + 16384);
    if (s >= 6 && !(s & 1)) wof[(s - 6) >> 1] = ld16(rs_ws, vl16, w_ < 7 ? wos + ((s - 6) >> 1) * 1024 : W_END);
  });
  STAMP(56);
  auto run_layer = [&](auto kind) INL {
    constexpr int layer = decltype(kind)::value, rep = layer >> 1;
    constexpr bool isA = !(layer & 1), first = layer == 0, more = layer < 3;
    int w = w_;
    asm volatile("" : "+s"(w));
    __builtin_assume(w >= 0 && w < 8);
    const bool lead = w >= 4;
    const v4f z4 = {0.f, 0.f, 0.f, 0.f};
    const unsigned xs = W_X + layer * TILEBUF + tw;
    auto load_wo = [&](int s, bool typeA, bool on) INL {
      wof[s] = __builtin_amdgcn_raw_buffer_load_b128(rs_ws, vl16 + (s & 3) * 1024, on ? wos + (typeA ? 0 : 28 * 14336) + (s >> 2) * 4096 : W_END, SC1); SB();
    };
    auto load_wo_rest = [&](int s0, int s1) INL {
#pragma unroll
      for (int s = s0; s < s1; ++s) load_wo(s, isA, w < 7);
    };
    const unsigned sts = W_ST + layer * 131072 + (t * 8 + w) * 1024;
    v2u hh;
    unsigned vh = OOB;
    const unsigned shh = W_HINT + 2 * layer * 32768 + (t - 2) * 2048 + h * 128;
    if (!isA) {
      const unsigned lo = lane_id();
      vh = (lo >= 16 || lead) ? OOB : (lo < 2 && (int)lo < t) ? (1 - lo) * 2048 : 4096;
      if (w == 7) hint_put(2 * layer);
    }
    auto ldx = [&](int s) INL { xf[s] = ld16(rs_ws, vl16 + (s & 3) * 1024, xs + (s >> 2) * 4096); };
    if (!first) {
      for (;;) {
#pragma unroll
        for (int s = 0; s < WIN; ++s) ldx(s);
        STAMP(2 + layer * 12);
        v4u st;
        unsigned c0 = 0u - key0, c1 = 0u - key1;
        kv_gemm([&](int s) INL {
          if (s + WIN < 14) ldx(s + WIN);
          if (s + WIN == 14) st = ld16(rs_ws, vl16, sts);
          if (!isA && s + WIN == 15) hh = hint_get(vh, shh);
          c0 += xf[s].x + xf[s].z; c1 += xf[s].y + xf[s].w;
          if (s == 13) { var = xrow(1.f / DM, asf(st.x)); c0 = xsum2u(c0 + st.x + st.y, c1 + st.z); }
          SB();
        });
        if (!retry(c0 != 0u, budget)) break;
        __builtin_amdgcn_s_sleep(4);
      }
    }
    const float mean = k1.w * (1.f / DM);
    var = fmaf(-mean, mean, var);
    if (isA) { if (w == 4) hint_put(2 * layer); load_wo_rest(first ? 4 : WO1, first ? 8 : (WO1 + 14) / 2); }
    STAMP(3 + layer * 12);
    const lptr cs = csg + (isA ? 0 : 384);
    const v4f ck0 = *(LAS v4f*)(cs), ck1 = *(LAS v4f*)(cs + 64), cv0 = *(LAS v4f*)(cs + 128), cv1 = *(LAS v4f*)(cs + 192);
    float rstd, nmr;
    {
      rstd = __builtin_amdgcn_rsqf(mx(var, 0.f) + 1e-5f);
      nmr = -mean * rstd;
    }
    auto ln7 = [&](v4f a0, v4f a1, v4f c0, v4f c1, float sc, float sh, float pad) INL {
      a0 = a0 * sc + c0 * sh;
      const v2f u = v2f{a1.x, a1.y} * sc + v2f{c1.x, c1.y} * sh;
      return v4u{pk(a0.x, a0.y), pk(a0.z, a0.w), pk(u.x, u.y), pk(fmaf(a1.z, sc, c1.z * sh), pad)};
    };
    auto q_gemm = [&](float sc, float pad) INL {
      v4f q0 = z4, q1 = z4;
      const lptr lq = lq16 + (isA ? 0 : L_WQB - L_WQ);
      v4u a[8];
#pragma unroll
      for (int i = 0; i < 8; ++i) { a[i] = lrd(lq + i * 1024); SB(); }
#pragma unroll
      for (int s = 0; s < 14; ++s) {
        const int i = (2 * s) & 7;
        if (!(s & 1)) { if (s < 12) LGKNOWN(4); else LGKNOWN(0); SB(); }
        q0 = mfma(a[i], xf[s], q0); SB(); if (s + 4 < 14) { a[i] = lrd(lq + (s + 4) * 2048); SB(); }
        q1 = mfma(a[i + 1], xf[s], q1); SB(); if (s + 4 < 14) { a[i + 1] = lrd(lq + (s + 4) * 2048 + 1024); SB(); }
      }
      return ln7(q0, q1, *(LAS v4f*)(cs + 256), *(LAS v4f*)(cs + 320), rstd * sc, nmr * sc, pad);
    };
    auto swap_block = [&](int j) INL {
      const lptr a = lbase(w * 2048 + j * 16384 + vl16);
      const v4u t0 = lrd(a), t1 = lrd(a + 1024);
      lwr(a, park[2 * j]); lwr(a + 1024, park[2 * j + 1]);
      park[2 * j] = t0; park[2 * j + 1] = t1;
    };
    constexpr float L2E = 1.4426950408889634f;
    const v4u kf = isA ? ln7(k0, k1, ck0, ck1, rstd * L2E, nmr * L2E, 0.f) : ln7(k0, k1, ck0, ck1, rstd, nmr, 0.f);
    v4u vf;
    {
      const v4f a0 = v0 * rstd + cv0 * nmr, a1 = v1 * rstd + cv1 * nmr;
      vf = v4u{pk(a0.x, a0.y), pk(a0.z, a0.w), g3 ? 0x3f80u : pk(a1.x, a1.y), g3 ? 0u : pk(a1.z, a1.w)};
    }
    v4f o0, o1;
    float den;
    const unsigned soh = W_HINT + (2 * layer + 1) * 32768 + t * 2048;
    v2u oh;
    auto o_poll = [&]() INL { oh = hint_get(vrow, w == (isA ? 7 : 3) ? soh : W_END); };

    if constexpr (isA) {
      v4u pp, pm;
      feature_map(kf, wphi, pp, pm, [&]() INL { SB(); load_wo_rest(first ? 8 : (WO1 + 14) / 2, first ? 12 : 14); });
      {
        const lptr tb = stw + w * 3072;
        *(LAS v2u*)(tb) = v2u{pp.x, pp.y}; *(LAS v2u*)(tb + 512) = v2u{pp.z, pp.w};
        *(LAS v2u*)(tb + 1024) = v2u{pm.x, pm.y}; *(LAS v2u*)(tb + 1536) = v2u{pm.z, pm.w};
        *(LAS v2u*)(tb + 2048) = v2u{vf.x, vf.y}; *(LAS v2u*)(tb + 2560) = v2u{vf.z, vf.w};
      }
      v4u hwk = weights(lane_id());
      hwk.w &= 0xffffu;
      unsigned keyk = ((vl16 & 512) ? key1 : key0) >> ((vl16 & 256) ? 14 : 0);
      asm volatile("" : "+v"(hwk), "+v"(keyk));
      if (first) { VMWAIT(8); load_wo_rest(12, 14); }
      wg_bar();
      STAMP(4 + layer * 12);
      const unsigned kvp = W_KVP + rep * 1048576 + h * 65536 + (w & 3) * 1024, sh = W_HINT + 2 * layer * 32768 + h * 128;
      if (!lead) { __builtin_amdgcn_s_setprio(1); __builtin_amdgcn_s_sleep(DLY_T); }
      const v2u hv = hint_get(vcol, lead ? W_END : sh);
      if (lead) {
        __builtin_amdgcn_s_setprio(2);
        const lptr pa = str + (w & 2) * 512, pb = str + (2048 + (w & 1) * 512);
        v4u bv[4], x0[4], x1[4];
#pragma unroll
        for (int kk = 0; kk < 4; ++kk) {
          bv[kk] = cat(trd(pb + kk * 6144), trd(pb + kk * 6144 + 3072));
          x0[kk] = cat(trd(pa + kk * 6144), trd(pa + kk * 6144 + 3072));
          x1[kk] = cat(trd(pa + kk * 6144 + 512), trd(pa + kk * 6144 + 3072 + 512));
        }
        v4f d0 = z4, d1 = z4;
#pragma unroll
        for (int kk = 0; kk < 4; ++kk) { d0 = mfma(x0[kk], bv[kk], d0); d1 = mfma(x1[kk], bv[kk], d1); }
        v4u dv = {pk(d0.x, d0.y), pk(d0.z, d0.w), pk(d1.x, d1.y), pk(d1.z, 0.f)};
        const v4u hs = imma(hwk, dv, zu);
        dv.w |= ((hs.x + keyk) & 0x3fffu) << 16;
        __builtin_amdgcn_raw_buffer_store_b128(dv, rs_ws, vl16, kvp + t * 4096, SC1);
        keep(dv);
        __builtin_amdgcn_s_setprio(0);
        STAMP(5 + layer * 12);
      }
      v4u qf = q_gemm(L2E, 0.f);
      asm volatile("" : "+v"(qf) :: "memory");
      if (lead) {
        feature_map(qf, wphi, pp, pm, []() {});
        if (first) {
          asm volatile("" : "+v"(pp), "+v"(pm) :: "memory");
          const unsigned qb = ptA - w * 2048 + 16 * 86016 + L_WQ + (w & 3) * 2048;
#pragma unroll
          for (int j = 0; j < 4; ++j) if (j < 3 || w < 6) dma2s(L_WQB + (w & 3) * 2048 + j * 8192, vl16, srdW, qb + j * 8192);
        }
      } else {
        pause(lateness(hv, t, key0, srdW, vcol, sh, budget) + T_KV);
        if (w == 0) hint_put(2 * layer + 1);
        fence();
        STAMP(6 + layer * 12);
        v4u kp[16];
#pragma unroll
        for (int i = 0; i < 8; ++i) kp[i] = ld16(rs_ws, vl16, kvp + i * 4096);
        feature_map(qf, wphi, pp, pm, [&]() INL {
          SB();
#pragma unroll
          for (int i = 8; i < 16; ++i) kp[i] = ld16(rs_ws, vl16, kvp + i * 4096);
        });
        const unsigned lo = lane_id();
        const unsigned bit = ((lo >> 4) == ((lo >> 2) & 3)) ? ((lo & 1) ? 0x3f800000u : 0x3f80u) : 0u;
        const unsigned u0 = (lo & 2) ? 0u : bit, u1 = (lo & 2) ? bit : 0u;
        const v4u sa = {u0, u1, 0u, 0u}, sb = {0u, 0u, u0, u1};
        v4f lo4, hi4;
        SB();
        for (;;) {
          lo4 = z4; hi4 = z4;
          v4u hs = zu;
          unsigned ck = 0u - 16u * keyk;
#pragma unroll
          for (int i = 0; i < 16; ++i) {
            hs = imma(hwk, kp[i], hs); lo4 = mfma(sa, kp[i], lo4); hi4 = mfma(sb, kp[i], hi4);
            ck += kp[i].w >> 16;
            SB();
          }
          if (!retry(((hs.x - ck) & 0x3fffu) != 0u, budget)) break;
#pragma unroll
          for (int i = 0; i < 16; i += 4) reload4(kp[i], kp[i + 1], kp[i + 2], kp[i + 3], srdW, vl16, kvp + i * 4096, kvp + (i + 1) * 4096, kvp + (i + 2) * 4096, kvp + (i + 3) * 4096);
        }
        lwr(sl16 + (L_KVF - L_SCR) + (w & 3) * 1024, v4u{pk(lo4.x, lo4.y), pk(lo4.z, lo4.w), pk(hi4.x, hi4.y), pk(hi4.z, 0.f)});
      }
      wg_bar();
      __builtin_amdgcn_s_setprio(0);
      STAMP(7 + layer * 12);
      o_poll();
      v4u kv[4];
#pragma unroll
      for (int i = 0; i < 4; ++i) kv[i] = lrd(sl16 + (L_KVF - L_SCR) + (i ^ 1) * 1024);
      o1 = mfma(kv[0], pp, z4); o0 = mfma(kv[1], pp, z4);
      o1 = mfma(kv[2], pm, o1); o0 = mfma(kv[3], pm, o0);
      den = xrow(sel3, o1.x) + 1e-6f;
    } else {
      const unsigned hb = W_HALO + rep * 4194304 + h * 262144;
      const unsigned hp = t < 15 ? hb + (t * 8 + w) * 2048 : W_END;
      __builtin_amdgcn_raw_buffer_store_b128(kf, rs_ws, vl16, hp, SC1);
      {
        const v4u hs = imma(hwt, vf, imma(hwt, kf, zu));
        const unsigned e0 = (hs.y << 13) + hs.x + kf.z + key0, e1 = (hs.w << 13) + hs.z + kf.w + key1;
        const v4u vp = {vf.x, vf.y, g3 ? e0 : vf.z, g3 ? e1 : vf.w};
        __builtin_amdgcn_raw_buffer_store_b128(vp, rs_ws, vl16 + 1024, hp, SC1);
        keep(vp);
      }
      const lptr kw = sl16 + w * 2048, vw = stw + w * 2048, vr = str + w * 2048;
      if (w < 7) lwr(kw + 32768, kf);
      *(LAS v2u*)(vw + 32768 + 1024) = v2u{vf.x, vf.y}; *(LAS v2u*)(vw + 32768 + 1536) = v2u{vf.z, vf.w};
      STAMP(4 + layer * 12);
      wg_bar();
      v4u hk[4], hv[4];
      unsigned hs[2];
#pragma unroll
      for (int i = 0; i < 2; ++i) hs[i] = (t - 2 + i >= 0 && !lead) ? hb + ((t - 2 + i) * 8 + w) * 2048 : W_END;
      if (!lead) pause(lateness(hh, 2, key0, srdW, vh, shh, budget) + T_H);
      fence();
#pragma unroll
      for (int i = 0; i < 4; ++i) {
        const unsigned wb = w < 7 ? wos + 28 * 14336 + (WO1 + 2 * i) * 1024 : W_END;
        hk[i] = ld16(rs_ws, vl16, lead ? wb : min(hs[i >> 1] + (i & 1) * 8192, W_END));
        hv[i] = ld16(rs_ws, vl16 + 1024, lead ? wb : min(hs[i >> 1] + (i & 1) * 8192, W_END));
      }
      static_assert(WO1 == 6, "eight fragments follow");
      const v4u qf = q_gemm(0.18898223650461357f * L2E, 1.f);
      const float NEG = -3.0e38f;
      v4f sd = mfma(kf, qf, z4);
#pragma unroll
      for (int r = 0; r < 4; ++r) sd[r] = (4 * g + r <= c) ? sd[r] : NEG;
      float m = xmax1(mx(mx3(sd.x, sd.y, sd.z), sd.w));
      auto diag = [&]() INL {
        const v2u a0 = trd(vr + 32768 + 1024), a1 = trd(vr + 32768 + 1536);
        const v4f e = sd - m;
        const v2u pd = {pk(ex2(e.x), ex2(e.y)), pk(ex2(e.z), ex2(e.w))};
        typedef short v4h __attribute__((ext_vector_type(4)));
        o0 = __builtin_amdgcn_mfma_f32_16x16x16bf16_1k(__builtin_bit_cast(v4h, a0), __builtin_bit_cast(v4h, pd), z4, 0, 0, 0);
        o1 = __builtin_amdgcn_mfma_f32_16x16x16bf16_1k(__builtin_bit_cast(v4h, a1), __builtin_bit_cast(v4h, pd), z4, 0, 0, 0);
      };
      diag();
      auto take_halo = [&](bool ld) INL {
        STAMP(6 + layer * 12);
        if (ld) {
          const unsigned nv = 2u * min(t, 2);
          for (;;) {
            v4u hs_ = zu;
            unsigned e0 = nv * key0, e1 = nv * key1;
#pragma unroll
            for (int i = 0; i < 4; ++i) {
              hs_ = imma(hwt, hv[i], imma(hwt, hk[i], hs_));
              e0 += hk[i].z - hv[i].z; e1 += hk[i].w - hv[i].w;
              const bool none = t - 2 + (i >> 1) < 0;
              lwr(kw + i * 8192, v4u{hk[i].x, hk[i].y, hk[i].z, none ? 0xfe000000u : hk[i].w});
              *(LAS v2u*)(vw + i * 8192 + 1024) = v2u{hv[i].x, hv[i].y};
              *(LAS v2u*)(vw + i * 8192 + 1536) = v2u{g3 ? 0x3f80u : hv[i].z, g3 ? 0u : hv[i].w};
            }
            if (!retry(g3 && (((hs_.y << 13) + hs_.x + e0) | ((hs_.w << 13) + hs_.z + e1)) != 0u, budget)) break;
            const unsigned h1 = min(hs[0] + 8192, W_END), h3 = min(hs[1] + 8192, W_END);
            reload4(hk[0], hk[1], hk[2], hk[3], srdW, vl16, hs[0], h1, hs[1], h3);
            reload4(hv[0], hv[1], hv[2], hv[3], srdW, vl16 + 1024, hs[0], h1, hs[1], h3);
          }
        }
        wg_bar();
        if (ld) {
          __builtin_amdgcn_s_setprio(1);
          if (w == 3) hint_put(2 * layer + 1);
#pragma unroll
          for (int i = 0; i < 4; ++i) {
            hk[i] = __builtin_amdgcn_raw_buffer_load_b128(rs_ws, vl16, wos + 28 * 14336 + (WO1 + 2 * i) * 1024, SC1); SB();
            hv[i] = __builtin_amdgcn_raw_buffer_load_b128(rs_ws, vl16 + 1024, wos + 28 * 14336 + (WO1 + 2 * i) * 1024, SC1); SB();
          }
        }
        STAMP(7 + layer * 12);
      };
      {
        const v4f cm = {-m, -m, -m, -m};
        v4f sa, sb; v4u a0, a1;
#pragma unroll
        for (int j = 7; j >= 0; --j) {
          if (j >= 4 && (w >> 1) == 7 - j) take_halo(j >= 6);
          if (j == POLL_OB) o_poll();
          const v4u ka = lrd(kw + j * 4096), kb = lrd(kw + j * 4096 + 2048);
          const v4u n0 = cat(trd(vr + j * 4096 + 1024), trd(vr + j * 4096 + 3072)), n1 = cat(trd(vr + j * 4096 + 1536), trd(vr + j * 4096 + 3584));
          SB();
          if (j < 7) { const v4u pf = pexp(sa, sb); o0 = mfma(a0, pf, o0); o1 = mfma(a1, pf, o1); }
          sa = mfma(ka, qf, cm); sb = mfma(kb, qf, cm);
          a0 = n0; a1 = n1;
          SB();
        }
#pragma unroll
        for (int r = 0; r < 4; ++r) sa[r] = (4 * g + r > c) ? sa[r] : NEG;
        const v4u pf = pexp(sa, sb);
        o1 = mfma(a1, pf, o1); o0 = mfma(a0, pf, o0);
      }
      den = xrow(sel3, o1.x);
      if (__builtin_expect(__any(!(den < 1.0e18f)), 0)) {
        float mt = m;
#pragma unroll 1
        for (int k = 0; k < 16; ++k) {
          v4f s = mfma(lrd(kw + k * 2048), qf, z4);
#pragma unroll
          for (int r = 0; r < 4; ++r) s[r] = (k > 0 || 4 * g + r > c) ? s[r] : NEG;
          mt = mx3(mt, mx(s.x, s.y), mx(s.z, s.w));
        }
        m = xmax1(mt);
        diag();
        const v4f cm = {-m, -m, -m, -m};
#pragma unroll 1
        for (int j = 7; j >= 0; --j) {
          v4f sa = mfma(lrd(kw + j * 4096), qf, cm);
          const v4f sb = mfma(lrd(kw + j * 4096 + 2048), qf, cm);
#pragma unroll
          for (int r = 0; r < 4; ++r) sa[r] = (j > 0 || 4 * g + r > c) ? sa[r] : NEG;
          const v4u pf = pexp(sa, sb);
          o0 = mfma(cat(trd(vr + j * 4096 + 1024), trd(vr + j * 4096 + 3072)), pf, o0);
          o1 = mfma(cat(trd(vr + j * 4096 + 1536), trd(vr + j * 4096 + 3584)), pf, o1);
        }
        den = xrow(sel3, o1.x);
      }
#pragma unroll
      for (int i = 0; i < 4; ++i) { wof[WO1 + 2 * i] = hk[i]; wof[WO1 + 2 * i + 1] = hv[i]; }
    }
    const unsigned ob = W_O + layer * 2097152 + t * 131072;
    {
      const float rd = __builtin_amdgcn_rcpf(den);
      o0 *= rd; o1 *= rd;
      v4u v = {pk(o0.x, o0.y), pk(o0.z, o0.w), pk(o1.x, o1.y), pk(o1.z, o1.w)};
      const v4u hs = imma(hwt, v, zu);
      const unsigned e0 = (hs.y << 13) + hs.x + key0, e1 = (hs.w << 13) + hs.z + key1;
      v.z = g3 ? e0 : v.z; v.w = g3 ? e1 : v.w;
      __builtin_amdgcn_raw_buffer_store_b128(v, rs_ws, vl16, ob + (w * 16 + h) * 1024, SC1);
      keep(v);
    }
    if (!isA) __builtin_amdgcn_s_setprio(0);
    STAMP(8 + layer * 12);
    if (first) {
#pragma unroll
      for (int j = 0; j < 4; ++j) {
        if (j < 3 || w < 4) {
          const lptr a = lbase(w * 2048 + j * 16384 + vl16);
          park[2 * j] = lrd(a); park[2 * j + 1] = lrd(a + 1024);
        }
      }
      asm volatile("s_waitcnt lgkmcnt(0)" ::: "memory");
      dma2s(w * 2048, vl16, srdW, ptA + 16 * 86016); dma2s(w * 2048 + 16384, vl16, srdW, ptA + 16 * 86016 + 16384);
    }
    if (isA && !first && w != 7) { swap_block(0); swap_block(1); }
    if (w == (isA ? 7 : 3)) *(LAS int*)(csg + 768) = lateness(oh, h, key0, srdW, vrow, soh, budget);
    wg_bar();
    const int olate = __builtin_amdgcn_readfirstlane(*(LAS int*)(csg + 768));
    if (!isA && more) { swap_block(0); swap_block(1); }
    pause(olate + (isA ? T_OA : T_OB) + (more ? 0 : 8));
    int w2 = w;
    {
      const unsigned op = ob + (h >> 2) * 32768 + w * 1024;
      v4u of[4];
#pragma unroll
      for (int i = 0; i < 4; ++i) of[i] = ld16(rs_ws, vl16, op + i * 8192);
      if (first) {
        const unsigned ptB = ptA + 16 * 86016;
        dma2s(w * 2048 + 32768, vl16, srdW, ptB + 32768);
        if (w < 4) dma2s(w * 2048 + 49152, vl16, srdW, ptB + 49152);
      }
      if (isA && !first && w == 7) { swap_block(0); swap_block(1); }
      if (more && !first) { swap_block(2); if (w < 4) swap_block(3); }
      STAMP(9 + layer * 12);
      const lptr s16 = sl16 + w * 768, s8 = sl16 + (12288 - 768 + (w >> 1) * 256 + (w & 1) * 8);
      for (;;) {
        v4u hs = zu;
        unsigned e0 = 4u * key0, e1 = 4u * key1;
#pragma unroll
        for (int i = 0; i < 4; ++i) { hs = imma(hwt, of[i], hs); e0 -= of[i].z; e1 -= of[i].w; }
        if (!g3) {
#pragma unroll
          for (int i = 0; i < 4; ++i) lwr(s16 + (i >> 1) * 14336 + (i & 1) * 6144, of[i]);
        } else {
#pragma unroll
          for (int i = 0; i < 4; ++i) *(LAS v2u*)(s8 + (i >> 1) * 14336 + (i & 1) * 1024) = v2u{of[i].x, of[i].y};
        }
        if (!retry(g3 && (((hs.y << 13) + hs.x + e0) | ((hs.w << 13) + hs.z + e1)) != 0u, budget)) break;
        reload4(of[0], of[1], of[2], of[3], srdW, vl16, op, op + 8192, op + 16384, op + 24576);
      }
      wg_bar();
      if (w == 7) {
        if (more) {
          hint_put(8 + layer);
          __builtin_amdgcn_s_sleep(P_X);
          const unsigned sx = W_HINT + (8 + layer) * 32768 + t * 2048;
          *(LAS int*)(csg + 832) = lateness(hint_get(vrow, sx), h, key0, srdW, vrow, sx, budget);
        }
      }
      asm volatile("" : "+s"(w2));
      if (w2 != 7) {
        v4u bq[8];
#pragma unroll
        for (int i = 0; i < 4; ++i) { bq[2 * i] = lrd(sl16 + i * 1024); SB(); bq[2 * i + 1] = lrd(sl16 + 14336 + i * 1024); SB(); }
#pragma unroll
        for (int s = 0; s < 14; ++s) {
          const int i = (2 * s) & 7;
          if (!(s & 1)) { if (s < 12) LGKNOWN(4); else LGKNOWN(0); SB(); }
          res0 = mfma(wof[s], bq[i], res0); SB(); if (s + 4 < 14) { bq[i] = lrd(sl16 + (s + 4) * 1024); SB(); }
          res1 = mfma(wof[s], bq[i + 1], res1); SB(); if (s + 4 < 14) { bq[i + 1] = lrd(sl16 + 14336 + (s + 4) * 1024); SB(); }
          if (more && s >= 1 && s - 1 < WO1) load_wo(s - 1, !isA, true);
        }
      }
    }
    if (more) {
      const v4u xo = swap16(v4u{pk(res0.x, res0.y), pk(res0.z, res0.w), pk(res1.x, res1.y), pk(res1.z, res1.w)});
      const unsigned xd = W_X + (layer + 1) * TILEBUF + tp;
      __builtin_amdgcn_raw_buffer_store_b128(xo, rs_ws, pubx, w < 7 ? xd : W_END, SC1);
      keep(xo);
      STAMP(10 + layer * 12);
      lwr(sl16 + 32768 + w * 1024, v4u{asu(sumsq8(xo)), xo.x + xo.z, xo.y + xo.w, 0u});
      wg_bar();
      const int late = __builtin_amdgcn_readfirstlane(*(LAS int*)(csg + 832));
      if (w == 7) {
        float q = 0.f;
        unsigned a0 = 0u, a1 = 0u;
#pragma unroll
        for (int i = 0; i < 7; ++i) { const v4u u = lrd(sl16 + 32768 + i * 1024); q += asf(u.x); a0 += u.y; a1 += u.z; }
        auto r = __builtin_amdgcn_permlane32_swap(asu(q), asu(q), false, false);
        auto ra = __builtin_amdgcn_permlane32_swap(a0, a0, false, false);
        auto rb = __builtin_amdgcn_permlane32_swap(a1, a1, false, false);
        const unsigned r0 = r[0], r1 = r[1], a00 = ra[0], a01 = ra[1], a10 = rb[0], a11 = rb[1], qs = asu(asf(r0) + asf(r1));
        const v4u rec = {qs, key0 - qs - a00 - a01, key1 - a10 - a11, 0u};
        __builtin_amdgcn_raw_buffer_store_b128(rec, rs_ws, vl16 < 512 ? (vl16 >> 8) * 1024 + (vl16 & 255) : OOB,
                                               W_ST + (layer + 1) * 131072 + (t * 8 + 2 * (h >> 2)) * 1024 + (h & 3) * 256, SC1);
        keep(rec);
      }
      pause(late + T_X - (w2 == 7 ? 8 : 0));
    }
  };
  run_layer(Layer<0>{}); run_layer(Layer<1>{}); run_layer(Layer<2>{}); run_layer(Layer<3>{});
  {
    const rsrc_t rs_o = __builtin_amdgcn_make_buffer_rsrc(p.out, 0, SEQ * DM * 2, 0x00020000);
    const unsigned lo = lane_id();
    const unsigned oo = (t * 128 + (2 * (h >> 2) + ((lo >> 4) & 1)) * 16 + (lo & 15)) * (DM * 2) + (2 * ft + (lo >> 5)) * 16;
    const v4u xo = swap16(v4u{pk(res0.x, res0.y), pk(res0.z, res0.w), pk(res1.x, res1.y), pk(res1.z, res1.w)});
    if (w_ < 7) __builtin_amdgcn_raw_buffer_store_b128(xo, rs_o, oo, 0, 0);
    else if (b == 0) __builtin_amdgcn_raw_buffer_store_b32(epoch, rs_ws, 0, W_KEY, 0);
    STAMP(46);
  }
}
}

extern "C" void kernel_launch(void* const* d_in, const int* in_sizes, int n_in, void* d_out, int out_size, void* d_ws, size_t ws_size,
                              hipStream_t stream) {
  (void)in_sizes; (void)n_in; (void)out_size; (void)ws_size;
  (void)hipFuncSetAttribute((const void*)fused_layers, hipFuncAttributeMaxDynamicSharedMemorySize, (int)L_TOTAL);
  Params p;
  p.x = d_in[0]; p.wA = d_in[1]; p.wphi = d_in[2]; p.woA = d_in[3]; p.wB = d_in[5]; p.woB = d_in[6]; p.out = d_out; p.ws = d_ws;
  hipLaunchKernelGGL(fused_layers, dim3(256), dim3(512), L_TOTAL, stream, p);
}
```

```cpp
#include <hip/hip_runtime.h>
#include <stdint.h>

#define LAS __attribute__((address_space(3)))
#define DEV __device__ __forceinline__
typedef unsigned v4u __attribute__((ext_vector_type(4)));
typedef unsigned v2u __attribute__((ext_vector_type(2)));
typedef float v4f __attribute__((ext_vector_type(4)));
typedef float v2f __attribute__((ext_vector_type(2)));
typedef int v4i __attribute__((ext_vector_type(4)));
typedef short v4s __attribute__((ext_vector_type(4)));
typedef __bf16 v8bf __attribute__((ext_vector_type(8)));
typedef __bf16 v2bf __attribute__((ext_vector_type(2)));
typedef __amdgpu_buffer_rsrc_t rsrc_t;

#define TPX 4
#define DLY_T 3
#define WO1 6
#define POLL_OB 2
#define T_KV 4
#define T_H 20
#define T_OA 0
#define T_OB 12
#define P_X 10
#define T_X 8
#define T_XL 0
#define T_OT 20
#define REUSE 0
#define WIN 4

namespace {
constexpr int SEQ = 2048, DM = 448;
constexpr unsigned L_WK = 0, L_WV = 28672, L_WQ = 57344, L_WQB = 86016, L_SCR = 114688, L_TOTAL = 163840;
constexpr unsigned L_CS = L_SCR + 23 * 2048;
constexpr unsigned L_KVF = L_SCR + 40960;
constexpr unsigned TILEBUF = 16 * 8 * 14 * 1024;
constexpr unsigned W_KEY = 0, W_FLAG = 2048, W_HINT = 4096, W_ST = W_HINT + 12 * 32768, W_KVP = W_ST + 4 * 131072, W_O = W_KVP + 2 * 1048576,
                   W_HALO = W_O + 4 * 2097152, W_X = W_HALO + 2 * 4194304, W_PT = W_X + 4 * TILEBUF, W_WO = W_PT + 2 * 16 * 86016,
                   W_CS = W_WO + 2 * 28 * 14336, W_END = W_CS + 16 * 56 * 128;
constexpr unsigned W_DBG = 64u << 20;
constexpr int SC1 = 16;
constexpr unsigned OOB = 0x40000000u;

DEV v4f mfma(v4u a, v4u b, v4f c) {
  return __builtin_amdgcn_mfma_f32_16x16x32_bf16(__builtin_bit_cast(v8bf, a), __builtin_bit_cast(v8bf, b), c, 0, 0, 0);
}
DEV unsigned pk(float a, float b) { v2f f = {a, b}; v2bf r = __builtin_convertvector(f, v2bf); return __builtin_bit_cast(unsigned, r); }
typedef LAS char* lptr;
DEV lptr lbase(unsigned v) { asm volatile("" : "+v"(v)); return (lptr)(size_t)v; }
DEV v2u trd(lptr a) { v4s r = __builtin_amdgcn_ds_read_tr16_b64_v4i16((LAS v4s*)a); return __builtin_bit_cast(v2u, r); }
DEV v4u cat(v2u a, v2u b) { return v4u{a.x, a.y, b.x, b.y}; }
DEV v4u lrd(lptr a) { return *(LAS v4u*)a; }
DEV void lwr(lptr a, v4u v) { *(LAS v4u*)a = v; }
#define VMWAIT(n) asm volatile("s_waitcnt vmcnt(" #n ")" ::: "memory")
DEV float asf(unsigned u) { return __builtin_bit_cast(float, u); }
DEV unsigned asu(float f) { return __builtin_bit_cast(unsigned, f); }
DEV void wg_bar() { asm volatile("s_waitcnt lgkmcnt(0)\n\ts_barrier" ::: "memory"); }
DEV void drain() { asm volatile("s_waitcnt vmcnt(0)" ::: "memory"); }
DEV void fence() { asm volatile("" ::: "memory"); }
DEV float ex2(float x) { return __builtin_amdgcn_exp2f(x); }

DEV unsigned lane_id() { unsigned r; asm volatile("v_mbcnt_lo_u32_b32 %0, -1, 0\n\tv_mbcnt_hi_u32_b32 %0, -1, %0" : "=v"(r)); __builtin_assume(r < 64); return r; }
DEV float mx(float a, float b) { return __builtin_elementwise_maximum(a, b); }
DEV float mn(float a, float b) { return __builtin_elementwise_minimum(a, b); }
DEV float mx3(float a, float b, float c) { return mx(mx(a, b), c); }
DEV float mn3(float a, float b, float c) { return mn(mn(a, b), c); }
DEV float xrow(float sel, float v) { const v4f z = {0.f, 0.f, 0.f, 0.f}; const v4f r = __builtin_amdgcn_mfma_f32_16x16x4f32(sel, v, z, 0, 0, 0); return r.x; }
DEV v4f xrow4(float sel, float v) { const v4f z = {0.f, 0.f, 0.f, 0.f}; return __builtin_amdgcn_mfma_f32_16x16x4f32(sel, v, z, 0, 0, 0); }
DEV v4u swap16(v4u v) {
  auto r = __builtin_amdgcn_permlane16_swap(v.x, v.z, false, false);
  auto q = __builtin_amdgcn_permlane16_swap(v.y, v.w, false, false);
  const unsigned r0 = r[0], r1 = r[1], q0 = q[0], q1 = q[1];
  return v4u{r0, q0, r1, q1};
}
DEV float sumsq8(v4u v) {
  const v4f lo = __builtin_bit_cast(v4f, v << 16), hi = __builtin_bit_cast(v4f, v & 0xffff0000u), q = lo * lo + hi * hi;
  return (q.x + q.y) + (q.z + q.w);
}
DEV void xmax2(float& a, float& b) {
  auto r = __builtin_amdgcn_permlane32_swap(asu(a), asu(b), false, false);
  unsigned r0 = r[0], r1 = r[1];
  float z = mx(asf(r0), asf(r1));
  auto q = __builtin_amdgcn_permlane16_swap(asu(z), asu(z), false, false);
  unsigned q0 = q[0], q1 = q[1];
  z = mx(asf(q0), asf(q1));
  auto p = __builtin_amdgcn_permlane32_swap(asu(z), asu(z), false, false);
  unsigned p0 = p[0], p1 = p[1];
  a = asf(p0); b = asf(p1);
}
DEV float xmax1(float a) {
  auto r = __builtin_amdgcn_permlane32_swap(asu(a), asu(a), false, false);
  unsigned r0 = r[0], r1 = r[1];
  float z = mx(asf(r0), asf(r1));
  auto q = __builtin_amdgcn_permlane16_swap(asu(z), asu(z), false, false);
  unsigned q0 = q[0], q1 = q[1];
  return mx(asf(q0), asf(q1));
}
DEV void dma2(unsigned ldsdst, unsigned va, unsigned vb, v4u srd, unsigned sa, unsigned sb) {
  asm volatile("s_mov_b32 m0, %0\n\ts_nop 3\n\tbuffer_load_dwordx4 %1, %3, %4 offen lds\n\tbuffer_load_dwordx4 %2, %3, %5 offen offset:1024 lds"
               :: "s"(ldsdst), "v"(va), "v"(vb), "s"(srd), "s"(sa), "s"(sb) : "memory");
}
DEV void keep(v4u v) { asm volatile("s_nop 1" :: "v"(v) : "memory"); }
DEV void keep(v4u v, v4u u) { asm volatile("s_nop 1" :: "v"(v), "v"(u) : "memory"); }
DEV void dma2s(unsigned ldsdst, unsigned v, v4u srd, unsigned so) {
  asm volatile("s_mov_b32 m0, %0\n\ts_nop 3\n\tbuffer_load_dwordx4 %1, %2, %3 offen sc1 lds\n\tbuffer_load_dwordx4 %1, %2, %3 offen offset:1024 sc1 lds"
               :: "s"(ldsdst), "v"(v), "s"(srd), "s"(so) : "memory");
}
DEV v4u ld16(rsrc_t rs, unsigned v, unsigned so) {
  const v4u r = __builtin_amdgcn_raw_buffer_load_b128(rs, v, so, SC1);
  __builtin_amdgcn_sched_barrier(0);
  return r;
}
DEV v4u mk_srd(const void* p, unsigned bytes) {
  unsigned long long a = (unsigned long long)p;
  return v4u{(unsigned)a, (unsigned)(a >> 32) & 0xffffu, bytes, 0x00020000u};
}
DEV v4u imma(v4u a, v4u b, v4u c) {
  const v4i r = __builtin_amdgcn_mfma_i32_16x16x64_i8(__builtin_bit_cast(v4i, a), __builtin_bit_cast(v4i, b), __builtin_bit_cast(v4i, c), 0, 0, 0);
  return __builtin_bit_cast(v4u, r);
}
DEV v4u weights(unsigned lo) {
  const unsigned a = (lo + 1) * 0x9e3779b1u, b = (a ^ (a >> 15)) * 0x85ebca77u, c = (b ^ (b >> 13)) * 0xc2b2ae3du, d = (c ^ (c >> 16)) * 0x27d4eb2fu;
  return v4u{(a ^ (a >> 16)) | 0x01010101u, (b ^ (b >> 16)) | 0x01010101u, (c ^ (c >> 16)) | 0x01010101u, (d ^ (d >> 16)) | 0x01010101u};
}
DEV void keys(unsigned e, unsigned& k0, unsigned& k1) { k0 = e * 0x9e3779b1u + 0x7f4a7c15u; k1 = e * 0x85ebca77u + 0x165667b1u; }
DEV unsigned xsum2u(unsigned a, unsigned b) {
  auto r = __builtin_amdgcn_permlane32_swap(a, b, false, false);
  const unsigned r0 = r[0], r1 = r[1], z = r0 + r1;
  auto q = __builtin_amdgcn_permlane16_swap(z, z, false, false);
  const unsigned q0 = q[0], q1 = q[1];
  return q0 + q1;
}
DEV void reload4(v4u& a, v4u& b, v4u& c, v4u& d, v4u srd, unsigned voff, unsigned sa, unsigned sb, unsigned sc, unsigned sd) {
  asm volatile("s_sleep 2\n\tbuffer_load_dwordx4 %0, %4, %5, %6 offen sc1\n\tbuffer_load_dwordx4 %1, %4, %5, %7 offen sc1\n\t"
               "buffer_load_dwordx4 %2, %4, %5, %8 offen sc1\n\tbuffer_load_dwordx4 %3, %4, %5, %9 offen sc1\n\ts_waitcnt vmcnt(0)"
               : "+v"(a), "+v"(b), "+v"(c), "+v"(d) : "v"(voff), "s"(srd), "s"(sa), "s"(sb), "s"(sc), "s"(sd) : "memory");
}
DEV bool spend(int& budget) { budget = max(budget - 1, 0); return budget != 0; }
DEV bool nonzero(unsigned v, unsigned long long lanes = ~0ull) { return (__builtin_amdgcn_uicmp(v, 0u, 33) & lanes) != 0ull; }
DEV bool retry(bool bad, int& budget) {
  if (__builtin_expect(!__any(bad), 1)) return false;
  budget = max(budget - 1, 0);
  return __any(budget != 0);
}

DEV unsigned clock32() { unsigned long long t; asm volatile("s_memrealtime %0\n\ts_waitcnt lgkmcnt(0)" : "=s"(t) :: "memory"); return (unsigned)t; }
DEV int lateness(v2u v, int own, int seg, unsigned key, v4u srd, unsigned voff, unsigned soff, int& budget) {
  asm volatile("" : "+v"(v) :: "memory");
  int over = 0;
  if (__builtin_expect(nonzero(v.y - key, 0xffffull), 0)) {
    do {
      asm volatile("s_sleep 1\n\tbuffer_load_dwordx2 %0, %1, %2, %3 offen sc1\n\ts_waitcnt vmcnt(0)" : "+v"(v) : "v"(voff), "s"(srd), "s"(soff) : "memory");
    } while (nonzero(v.y - key, 0xffffull) && spend(budget));
    const unsigned tv = v.x;
    over = max((int)(clock32() - __builtin_amdgcn_readlane(tv, own)) - seg, 0);
  }
  const unsigned tv = v.x;
  int d = (int)(tv - __builtin_amdgcn_readlane(tv, own));
  d = max(d, __builtin_amdgcn_update_dpp(0, d, 0x128, 0xf, 0xf, false));
  d = max(d, __builtin_amdgcn_update_dpp(0, d, 0x124, 0xf, 0xf, false));
  d = max(d, __builtin_amdgcn_update_dpp(0, d, 0x122, 0xf, 0xf, false));
  d = max(d, __builtin_amdgcn_update_dpp(0, d, 0x121, 0xf, 0xf, false));
  return min(__builtin_amdgcn_readfirstlane(d), 1000) - over;
}
DEV void pause(int ticks) {
  asm volatile("s_cmp_lt_i32 %0, 1\n\ts_cbranch_scc1 2f\n1:\n\ts_sleep 1\n\ts_sub_i32 %0, %0, 4\n\ts_cmp_gt_i32 %0, 0\n\ts_cbranch_scc1 1b\n2:" : "+s"(ticks) :: "memory", "scc");
}

template <class F>
DEV void feature_map(v4u vf, const v4u (&wphi)[2], v4u& plus, v4u& minus, F mid) {
  const v4f z4 = {0.f, 0.f, 0.f, 0.f};
  const v4f p0 = mfma(wphi[0], vf, z4), p1 = mfma(wphi[1], vf, z4);
  float hi = mx3(mx3(mx3(p0.x, p0.y, p0.z), p0.w, p1.x), p1.y, p1.z);
  float lo = -mn3(mn3(mn3(p0.x, p0.y, p0.z), p0.w, p1.x), p1.y, p1.z);
  xmax2(hi, lo);
  mid();
  const v2f p01 = {p0.x, p0.y}, p23 = {p0.z, p0.w}, p45 = {p1.x, p1.y}, h2 = {hi, hi}, l2 = {lo, lo};
  v2f a01 = p01 - h2, a23 = p23 - h2, a45 = p45 - h2, b01 = p01 + l2, b23 = p23 + l2, b45 = p45 + l2;
  asm volatile("" : "+v"(a01), "+v"(a23), "+v"(a45), "+v"(b01), "+v"(b23), "+v"(b45));
  const v2f e01 = {ex2(a01.x), ex2(a01.y)}, e23 = {ex2(a23.x), ex2(a23.y)}, e45 = {ex2(a45.x), ex2(a45.y)};
  const float e6 = ex2(p1.z - hi);
  const v2f se = (e01 + e23) + e45;
  v4f te = xrow4(1.f, (se.x + e6) + se.y);
  __builtin_amdgcn_sched_barrier(0);
  const v2f f01 = {ex2(-b01.x), ex2(-b01.y)}, f23 = {ex2(-b23.x), ex2(-b23.y)}, f45 = {ex2(-b45.x), ex2(-b45.y)};
  const float f6 = ex2(-(p1.z + lo));
  const v2f sf = (f01 + f23) + f45;
  v4f tf = xrow4(1.f, (sf.x + f6) + sf.y);
  __builtin_amdgcn_sched_barrier(0);
  const float re = __builtin_amdgcn_rcpf(te.x);
  asm volatile("" :: "v"(te));
  const v2f x01 = e01 * re, x23 = e23 * re, x45 = e45 * re;
  plus = v4u{pk(x01.x, x01.y), pk(x23.x, x23.y), pk(x45.x, x45.y), pk(e6 * re, 0.f)};
  __builtin_amdgcn_sched_barrier(0);
  const float rf = __builtin_amdgcn_rcpf(tf.x);
  asm volatile("" :: "v"(tf));
  const v2f y01 = f01 * rf, y23 = f23 * rf, y45 = f45 * rf;
  minus = v4u{pk(y01.x, y01.y), pk(y23.x, y23.y), pk(y45.x, y45.y), pk(f6 * rf, 0.f)};
}

DEV v4u pexp(v4f a, v4f b) {
  return v4u{pk(ex2(a.x), ex2(a.y)), pk(ex2(a.z), ex2(a.w)), pk(ex2(b.x), ex2(b.y)), pk(ex2(b.z), ex2(b.w))};
}
#define VMKNOWN(n) __builtin_amdgcn_s_waitcnt(((n) & 15) | (((n) >> 4) << 14) | 0xf70)
#define LGKNOWN(n) __builtin_amdgcn_s_waitcnt(0xc07f | ((n) << 8))
#define SB() __builtin_amdgcn_sched_barrier(0)
#define INL __attribute__((always_inline))

#define STAMP(i) do {} while (0)

template <int N> struct Layer { static constexpr int value = N; };
struct Params {
  const void* x; const void* wA; const void* wphi; const void* woA; const void* wB; const void* woB; void* out; void* ws;
};

__global__ void __launch_bounds__(512, 2) fused_layers(Params p) {
  const int b = blockIdx.x, xg = b & 7, xi = b >> 3;
  constexpr int HPX = 32 / TPX, NGT = 16 / TPX;
  const int t = (xg % NGT) * TPX + xi / HPX;
  const int h = (xg / NGT) * HPX + xi % HPX;
  const int wid = __builtin_amdgcn_readfirstlane(threadIdx.x >> 6) & 7;
  const int w_ = wid < 4 ? 7 - wid : wid - 4;
  const int l = threadIdx.x & 63, c = l & 15, g = l >> 4;
  const bool g3 = g == 3;
  unsigned key0, key1;
  const unsigned epoch = *(const unsigned*)p.ws + 1u;
  keys(epoch, key0, key1);
  STAMP(0);
  constexpr unsigned L_XS = 73728, L_JOB = L_WQ, L_CSP = L_XS + 81920;
  const unsigned chunk16 = ((l & 3) ^ ((l >> 5) << 1)) * 16, vqkv = (l >> 2) * (3 * DM * 2) + chunk16;
  const int kvp_ = (w_ >> 1) & 1, kvs = w_ & 1;
  const unsigned kvat = kvp_ * L_WV + kvs * 2048, kvso = kvs * (32 * 3 * DM * 2) + (kvp_ + 1) * DM * 2 + 56 * h;
  const v4u srdA = mk_srd(p.wA, DM * 3 * DM * 2), srdX = mk_srd(p.x, SEQ * DM * 2);
  auto kvblock = [&](int j) INL { const unsigned so = kvso + j * (64 * 3 * DM * 2); dma2(kvat + j * 4096, vqkv, vqkv, srdA, so, so + 16 * 3 * DM * 2 - 1024); };
  const unsigned xsb = L_XS + w_ * 10240, xso = (t * 128 + w_ * 16) * (DM * 2);
  const unsigned vx0 = (l >> 3) * (DM * 2) + ((l & 7) ^ (l >> 4)) * 16, vx1 = ((l >> 3) + 8) * (DM * 2) + ((l & 7) ^ (4 + (l >> 4))) * 16 - 1024;
  auto xline = [&](int j, int up) INL {
    const unsigned so = xso + up * (64 * DM * 2) + j * 128;
    dma2(xsb + up * 40960 + (j % 5) * 2048, vx0, vx1, srdX, so, so);
  };
  auto job_of = [&](int u, int& job, int& wj, int& jt, int& jr, int& jp, int& js) INL {
    job = t + 16 * u; wj = (job - 56) * 16 + h;
    jt = job < 56 ? job >= 14 : wj / 196; jr = job < 56 ? job + 28 - 42 * jt : wj - 196 * jt; jp = jr / 14; js = jr - 14 * jp;
  };
  if (w_ >= 4) {
    kvblock(0); kvblock(1); kvblock(2); kvblock(3);
  } else {
    xline(0, 0); xline(0, 1);
#pragma unroll 1
    for (int u = w_ & 3; u < 8; u += 4) {
      int job, wj, jt, jr, jp, js;
      job_of(u, job, wj, jt, jr, jp, js);
      const unsigned jat = L_JOB + u * 2048;
      if (job < 56) {
        const unsigned so = js * (32 * 3 * DM * 2) + (jp == 2 ? 0 : (jp + 1) * DM * 2) + 56 * h;
        dma2(jat, vqkv, vqkv, mk_srd(jt ? p.wB : p.wA, DM * 3 * DM * 2), so, so + 16 * 3 * DM * 2 - 1024);
      } else if (u == 7) {
        const unsigned e = (l >> 2) & 7, g0 = l >> 5, d0 = e < 4 ? 4 * g0 + e : 12 + 3 * g0 + e, d1 = d0 + (e < 4 ? 8 : 6);
        dma2(jat, e < 7 ? d0 * 56 + chunk16 + 1024 : OOB, e < 7 ? d1 * 56 + chunk16 : OOB,
             mk_srd((const char*)p.wphi - 1024, 16 * 28 * 28 * 2 + 1024), h * 28 * 28 * 2, h * 28 * 28 * 2);
      } else if (wj < 392) {
        unsigned v[2];
#pragma unroll
        for (int j = 0; j < 2; ++j) {
          const int n = 4 * js + 2 * j + (l >> 5), e = (l >> 2) & 7, hh = n / 3, gg = n - 3 * hh;
          const unsigned ra = 28 * hh + 4 * gg + 16 * (e >> 2), rb = 28 * (2 * (n - 48) + (e >> 2)) + 12;
          v[j] = ((js < 12 ? ra : rb) + (e & 3)) * (DM * 2) + chunk16 + 1024 - j * 1024;
        }
        dma2(jat, v[0], v[1], mk_srd((const char*)(jt ? p.woB : p.woA) - 1024, DM * DM * 2 + 1024), 64 * jp, 64 * jp);
      }
    }
    xline(1, 0); xline(1, 1);
  }
  const rsrc_t rs_ws = __builtin_amdgcn_make_buffer_rsrc(p.ws, 0, W_END, 0x00020000);
  const v4u srdW = mk_srd(p.ws, W_END);
  unsigned vl16 = l * 16;
  asm volatile("" : "+v"(vl16));
  const int ft = 7 * (h & 3) + min(w_, 6), nx = 2 * ft + (g >> 1);
  const unsigned pubx = w_ == 7 ? OOB : (g & 1) * 14336 + (nx >> 2) * 1024 + ((nx & 3) * 16 + c) * 16;
  const unsigned tw = (t * 8 + w_) * 14336, tp = (t * 8 + 2 * (h >> 2)) * 14336;
  const unsigned ptA = W_PT + h * 86016 + w_ * 2048;
  int budget = 1 << 16;
  v4u wphi[2];
  v4u park[8];
  v4u xf[14];
  v4f k0, k1, v0, v1, ck0, ck1, cv0, cv1;
  float var;
  v4f res0, res1;
  auto hint_put = [&](int slot, int ahead = 0) INL {
    __builtin_amdgcn_raw_buffer_store_b64(v2u{clock32() + ahead, key0}, rs_ws, vl16 < 256 ? vl16 >> 1 : OOB, W_HINT + slot * 32768 + (t * 16 + h) * 128, SC1);
  };
  auto hint_get = [&](unsigned v, unsigned so) INL { SB(); const v2u r = __builtin_amdgcn_raw_buffer_load_b64(rs_ws, v, so, SC1); SB(); return r; };
  const unsigned q4 = (l & 15) >> 2, p4 = l & 3;
  const lptr l16 = (lptr)(size_t)vl16, lq16 = lbase(L_WQ + l * 16);
  const lptr stw = lbase(L_SCR + c * 32 + ((g ^ (c >> 2)) * 8));
  const lptr str = lbase(L_SCR + (4 * g + q4) * 32 + ((p4 ^ g) * 8));
  const lptr sl16 = lbase(L_SCR + l * 16), csg = lbase(L_CS + g * 16);
  const unsigned vcol = l < 16 ? l * 2048 : OOB, vrow = l < 16 ? l * 128 : OOB;
  const float sel3 = g3 ? 1.f : 0.f;
  v4u hwt = weights(l);
  if (g3) { hwt.z = 0u; hwt.w = 0u; }
  const v4u zu = {0u, 0u, 0u, 0u};

  v4u wof[14];
  const unsigned wos = W_WO + ft * 14336;
  auto kv_gemm = [&](const int s0, const int s1, auto step) INL {
    const v4f z4 = {0.f, 0.f, 0.f, 0.f};
    if (s0 == 0) { k0 = z4; k1 = z4; v0 = z4; v1 = z4; }
    v4u a[8];
#pragma unroll
    for (int i = 0; i < 2; ++i) { const int o = (s0 + i) * 2048; a[4 * i] = lrd(l16 + o); a[4 * i + 1] = lrd(l16 + o + 1024); a[4 * i + 2] = lrd(l16 + L_WV + o); a[4 * i + 3] = lrd(l16 + L_WV + o + 1024); SB(); }
#pragma unroll
    for (int s = s0; s < s1; ++s) {
      const int i = 4 * (s & 1), o = (s + 2) * 2048;
      if (s < s1 - 1) LGKNOWN(4); else LGKNOWN(0);
      SB();
      k1 = mfma(a[i + 1], xf[s], k1); SB();
      step(s);
      if (s + 2 < s1) { a[i + 1] = lrd(l16 + o + 1024); SB(); }
      k0 = mfma(a[i], xf[s], k0); SB(); if (s + 2 < s1) { a[i] = lrd(l16 + o); SB(); }
      v0 = mfma(a[i + 2], xf[s], v0); SB(); if (s + 2 < s1) { a[i + 2] = lrd(l16 + L_WV + o); SB(); }
      v1 = mfma(a[i + 3], xf[s], v1); SB(); if (s + 2 < s1) { a[i + 3] = lrd(l16 + L_WV + o + 1024); SB(); }
    }
  };
  v4u hwk = weights(l);
  hwk.w &= 0xffffu;
  unsigned keyk = ((l & 32) ? key1 : key0) >> ((l & 16) ? 14 : 0);
  asm volatile("" : "+v"(hwk.z), "+v"(hwk.w), "+v"(keyk), "+v"(hwt));
  v4u fl0, fl1;
  unsigned vres0;
  {
    unsigned a0 = (8 * g + (c >> 2)) * 64 + ((((l & 3) >> 1) ^ ((g & 1) << 1)) * 16) + (l & 1) * 8;
    unsigned sl3 = c >= 12 ? 4 * c - 45 : c + ((c * 11) >> 5);
    const unsigned one = 0x3f803f80u;
    v4u one8 = {one, one, one, one};
    const v4f z4 = {0.f, 0.f, 0.f, 0.f};
    auto turn = [&](lptr r0, lptr r1, bool pad, v4u& f0, v4u& f1) INL {
      f0 = cat(trd(r0), trd(r0 + 256)); f1 = cat(trd(r1), trd(r1 + 256));
      f1 = v4u{pad ? one : f1.x, pad ? one : f1.y, pad ? one : f1.z, pad ? one : f1.w};
    };
    const unsigned xa = xsb + c * 128 + (g ^ (c >> 1)) * 16;
    const lptr xa0 = lbase(xa), xa1 = lbase(xa ^ 64), cp = lbase(L_CSP + g * 16);
    unsigned vres = (t * 128 + (2 * (h >> 2) + (g & 1)) * 16 + c) * (DM * 2) + nx * 16;
    unsigned pubx_ = pubx, vcol_ = vcol, vrow_ = vrow;
    float dg0 = c == 4 * g ? 1.f : 0.f, dg1 = c == 4 * g + 1 ? 1.f : 0.f, dg2 = c == 4 * g + 2 ? 1.f : 0.f, dg3 = c == 4 * g + 3 ? 1.f : 0.f;
    asm volatile("" : "+v"(vres), "+v"(a0), "+v"(sl3), "+v"(one8), "+v"(dg0), "+v"(dg1), "+v"(dg2), "+v"(dg3) : "v"(pubx_), "v"(vcol_), "v"(vrow_), "v"(sel3));
    STAMP(51);
    if (w_ >= 4) {
      const unsigned kslot = kvp_ ? c : sl3;
      const lptr kr0 = lbase(kvat + a0), kr1 = lbase(kvat + (a0 ^ 32)), kw0 = lbase(kvat + vl16), kw1 = lbase(kvat + 1024 + (vl16 & 768) + kslot * 16);
      const lptr cw0 = lbase(L_CSP + (w_ & 3) * 128 + c * 4), cw1 = lbase(L_CSP + (w_ & 3) * 128 + 64 + kslot * 4);
      const bool kpad = !kvp_ && c >= 12;
      v4f cs0 = z4, cs1 = z4;
#pragma unroll
      for (int j = 0; j < 7; ++j) {
        if (j + 4 < 7) kvblock(j + 4);
        if (j < 3) VMWAIT(8); else if (j == 3) VMWAIT(6); else if (j == 4) VMWAIT(4); else if (j == 5) VMWAIT(2); else VMWAIT(0);
        v4u f0, f1;
        turn(kr0 + j * 4096, kr1 + j * 4096, kpad, f0, f1);
        lwr(kw0 + j * 4096, f0); lwr(kw1 + j * 4096, f1);
        cs0 = mfma(one8, f0, cs0); cs1 = mfma(one8, f1, cs1);
      }
      *(LAS float*)(cw0) = cs0.x; *(LAS float*)(cw1) = cs1.x;
    } else {
      xline(2, 0); xline(2, 1);
      VMWAIT(8);
      STAMP(47);
#pragma unroll 1
      for (int u = w_; u < 8; u += 4) {
        int job, wj, jt, jr, jp, js;
        job_of(u, job, wj, jt, jr, jp, js);
        if (u != 7 && wj >= 392) break;
        const bool spread = u == 7 || (job < 56 && jp != 1);
        const unsigned cc = (vl16 >> 4) & 15, slot = spread ? sl3 : cc;
        v4u f0, f1;
        turn(lbase(L_JOB + u * 2048 + a0), lbase(L_JOB + u * 2048 + (a0 ^ 32)), spread && cc >= 12, f0, f1);
        if (u == 7) { lwr(lq16 + 7 * 2048, f0); lwr(lbase(L_JOB + 7 * 2048 + 1024 + (vl16 & 768) + slot * 16), f1); break; }
        const unsigned dst = job < 56 ? W_PT + (jt * 16 + h) * 86016 + jr * 2048 : W_WO + (jt * 28 + 2 * jp) * 14336 + js * 1024;
        __builtin_amdgcn_raw_buffer_store_b128(f0, rs_ws, vl16, dst, SC1);
        __builtin_amdgcn_raw_buffer_store_b128(f1, rs_ws, (job >= 56 ? 14336 : 1024) + (vl16 & 768) + slot * 16, dst, SC1);
        keep(f0, f1);
        if (job < 56) {
          const v4f c0 = mfma(one8, f0, z4), c1 = mfma(one8, f1, z4);
          __builtin_amdgcn_raw_buffer_store_b32(asu(vl16 >= 256 ? c1.x : c0.x), rs_ws, vl16 < 256 ? cc * 4 : vl16 < 512 ? 64 + slot * 4 : OOB,
                                                W_CS + (h * 56 + job) * 128, SC1);
        }
      }
      STAMP(52);
      xline(3, 0); xline(3, 1); xline(4, 0); xline(4, 1);
      drain();
    }
    STAMP(48);
    wg_bar();
    STAMP(49);
#pragma unroll
    for (int j = 0; j < 2; ++j) { xf[2 * j] = lrd(xa0 + j * 2048); xf[2 * j + 1] = lrd(xa1 + j * 2048); }
    if (w_ == 6) __builtin_amdgcn_raw_buffer_store_b64(v2u{key0, key1}, rs_ws, 0, W_FLAG + b * 8, SC1);
    asm volatile("s_waitcnt lgkmcnt(0)" : "+v"(xf[0]), "+v"(xf[1]), "+v"(xf[2]), "+v"(xf[3]) :: "memory");
    xline(5, 0); xline(6, 0);
    v4f gram = z4, pa, pb;
    kv_gemm(0, 10, [&](int s) INL {
      gram = mfma(xf[s], xf[s], gram);
      if (s < 3) { xf[4 + 2 * s] = lrd(xa0 + (2 + s) * 2048); xf[5 + 2 * s] = lrd(xa1 + (2 + s) * 2048); }
      if (s >= 4 && s < 8) { const v4f u = pa + pb; if (s == 4) ck0 = u; else if (s == 5) ck1 = u; else if (s == 6) cv0 = u; else cv1 = u; }
      if (s >= 3 && s < 7) {
        const int o = ((s - 3) >> 1) * 256 + ((s - 3) & 1) * 64;
        pa = *(LAS v4f*)(cp + o); pb = *(LAS v4f*)(cp + o + 128);
      }
      if (s == 7) { wphi[0] = lrd(lq16 + 7 * 2048); wphi[1] = lrd(lq16 + 7 * 2048 + 1024); }
      if (s == 8) drain();
      if (s >= 8) { xf[2 * s - 6] = lrd(xa0 + (s - 8) * 2048); xf[2 * s - 5] = lrd(xa1 + (s - 8) * 2048); }
      SB();
    });
    STAMP(50);
    if (w_ == 7) {
      do {
        asm volatile("s_nop 4\n\tbuffer_load_dwordx4 %0, %2, %3, %4 offen sc1\n\tbuffer_load_dwordx4 %1, %2, %3, %4 offen offset:1024 sc1\n\ts_waitcnt vmcnt(0)"
                     : "=&v"(fl0), "=&v"(fl1) : "v"(vl16), "s"(srdW), "s"(W_FLAG) : "memory");
      } while (retry(fl0.x != key0 || fl0.z != key0 || fl1.x != key0 || fl1.z != key0 || fl0.y != key1 || fl0.w != key1 || fl1.y != key1 || fl1.w != key1, budget));
    }
    if (w_ == 6) { *(LAS v4f*)(csg) = ck0; *(LAS v4f*)(csg + 64) = ck1; *(LAS v4f*)(csg + 128) = cv0; *(LAS v4f*)(csg + 192) = cv1; }
    wg_bar();
    STAMP(54);
    kv_gemm(10, 14, [&](int s) INL {
      if (s < 12) { gram = mfma(xf[2 * s - 10], xf[2 * s - 10], gram); gram = mfma(xf[2 * s - 9], xf[2 * s - 9], gram); }
      if (s == 10) dma2s(L_WQ + w_ * 2048, vl16, srdW, ptA + L_WQ);
      if (s == 12 && w_ < 6) dma2s(L_WQ + 16384 + w_ * 2048, vl16, srdW, ptA + L_WQ + 16384);
      if (s == 13) var = xrow(1.f / DM, (gram.x * dg0 + gram.y * dg1) + (gram.z * dg2 + gram.w * dg3));
      SB();
    });
    vres0 = vres;
  }
  STAMP(56);
  constexpr unsigned long long ROW3 = 0xffffull << 48;
  auto run_layer = [&](auto kind, auto role) INL {
    constexpr int layer = decltype(kind)::value, rep = layer >> 1;
    constexpr bool isA = !(layer & 1), first = layer == 0, more = layer < 3, lead = decltype(role)::value;
    int w = w_;
    asm volatile("" : "+s"(w));
    __builtin_assume(lead ? (w >= 4 && w < 8) : (w >= 0 && w < 4));
    const v4f z4 = {0.f, 0.f, 0.f, 0.f};
    constexpr unsigned SALT = REUSE ? 0x0badf00du : 0u, LS = REUSE ? 0 : 1;
    const unsigned ka = key0 + layer * SALT, kb_ = key1 + layer * SALT, kk_ = keyk + layer * SALT;
    const unsigned wob = (lead && w == 7) ? W_END - 12288 : wos + (isA ? 0 : 28 * 14336), wov = (lead && w == 7) ? OOB : vl16;
    auto load_wo = [&](int s0, int s1) INL {
#pragma unroll
      for (int s = s0; s < s1; ++s) wof[s] = ld16(rs_ws, wov + (s & 3) * 1024, wob + (s >> 2) * 4096);
    };
    v2u hh;
    unsigned vh;
    const unsigned shh = W_HINT + 2 * layer * 32768 + (t - 2) * 2048 + h * 128;
    if (!isA && !lead) { const unsigned lo = lane_id(); vh = lo >= 16 ? OOB : (lo < 2 && (int)lo < t) ? (1 - lo) * 2048 : 4096; }
    unsigned touched;
    auto touch = [&]() INL {
    };
    const lptr cs = csg + (isA ? 0 : 384);
    if (!first) {
      const unsigned xs = W_X + LS * layer * TILEBUF + tw, sts = W_ST + LS * layer * 131072 + (t * 8 + w) * 1024;
      auto ldx = [&](int s) INL { xf[s] = ld16(rs_ws, vl16 + (s & 3) * 1024, xs + (s >> 2) * 4096); };
      for (;;) {
#pragma unroll
        for (int s = 0; s < WIN; ++s) ldx(s);
        STAMP(2 + layer * 12);
        v4u st;
        unsigned c0 = 0u - ka, c1 = 0u - kb_;
        kv_gemm(0, 14, [&](int s) INL {
          if (s + WIN < 14) ldx(s + WIN);
          if (s + WIN == 14) st = ld16(rs_ws, vl16, sts);
          if (!isA && !lead && s + WIN == 15) hh = hint_get(vh, shh);
          c0 += xf[s].x + xf[s].z; c1 += xf[s].y + xf[s].w;
          if (s == 13) { ck0 = *(LAS v4f*)(cs); ck1 = *(LAS v4f*)(cs + 64); cv0 = *(LAS v4f*)(cs + 128); cv1 = *(LAS v4f*)(cs + 192); }
          if (s == 13) { var = xrow(1.f / DM, asf(st.x)); c0 = xsum2u(c0 + st.x + st.y, c1 + st.z); }
          SB();
        });
        if (__builtin_expect(!nonzero(c0), 1) || !spend(budget)) break;
        __builtin_amdgcn_s_sleep(4);
      }
    }
    STAMP(3 + layer * 12);
    const float mean = k1.w * (1.f / DM);
    const float rstd = __builtin_amdgcn_rsqf(mx(fmaf(-mean, mean, var), 0.f) + 1e-5f), nmr = -mean * rstd;
    auto ln7 = [&](v4f a0, v4f a1, v4f c0, v4f c1, float sc, float sh, float pad) INL {
      a0 = a0 * sc + c0 * sh;
      const v2f u = v2f{a1.x, a1.y} * sc + v2f{c1.x, c1.y} * sh;
      return v4u{pk(a0.x, a0.y), pk(a0.z, a0.w), pk(u.x, u.y), pk(fmaf(a1.z, sc, c1.z * sh), pad)};
    };
    auto q_gemm = [&](float sc, float pad) INL {
      v4f q0 = z4, q1 = z4, c0, c1;
      const lptr lq = lq16 + (isA ? 0 : L_WQB - L_WQ);
      v4u a[8];
#pragma unroll
      for (int i = 0; i < 8; ++i) { a[i] = lrd(lq + i * 1024); SB(); }
#pragma unroll
      for (int s = 0; s < 14; ++s) {
        const int i = (2 * s) & 7;
        if (!(s & 1)) { if (s < 12) LGKNOWN(4); else LGKNOWN(0); SB(); }
        q0 = mfma(a[i], xf[s], q0); SB(); if (s + 4 < 14) { a[i] = lrd(lq + (s + 4) * 2048); SB(); }
        q1 = mfma(a[i + 1], xf[s], q1); SB(); if (s + 4 < 14) { a[i + 1] = lrd(lq + (s + 4) * 2048 + 1024); SB(); }
        if (s == 12) { c0 = *(LAS v4f*)(cs + 256); c1 = *(LAS v4f*)(cs + 320); SB(); }
      }
      v4u r = ln7(q0, q1, c0, c1, rstd * sc, nmr * sc, pad);
      asm volatile("" : "+v"(r) :: "memory");
      return r;
    };
    constexpr float L2E = 1.4426950408889634f;
    const v4u kf = isA ? ln7(k0, k1, ck0, ck1, rstd * L2E, nmr * L2E, 0.f) : ln7(k0, k1, ck0, ck1, rstd, nmr, 0.f);
    auto v_pack = [&]() INL {
      const v4f a0 = v0 * rstd + cv0 * nmr, a1 = v1 * rstd + cv1 * nmr;
      return v4u{pk(a0.x, a0.y), pk(a0.z, a0.w), g3 ? 0x3f80u : pk(a1.x, a1.y), g3 ? 0u : pk(a1.z, a1.w)};
    };
    v4f o0, o1;
    float den;
    const unsigned soh = W_HINT + (2 * layer + 1) * 32768 + t * 2048;
    v2u oh;

    if constexpr (isA) {
      if (lead && w == 7) hint_put(2 * layer);
      if (!first) load_wo(6, 10);
      else if (lead) { fl0 = ld16(rs_ws, vl16, W_CS + (h * 56 + 14 * (w & 3)) * 128); fl1 = ld16(rs_ws, vl16 < 768 ? vl16 + 1024 : OOB, W_CS + (h * 56 + 14 * (w & 3)) * 128); }
      const v4u vf = v_pack();
      v4u pp, pm, qf;
      feature_map(kf, wphi, pp, pm, [&]() INL { SB(); if (first) load_wo(0, 4); else load_wo(10, 14); touch(); });
      {
        const lptr tb = stw + w * 3072;
        *(LAS v2u*)(tb) = v2u{pp.x, pp.y}; *(LAS v2u*)(tb + 512) = v2u{pp.z, pp.w};
        *(LAS v2u*)(tb + 1024) = v2u{pm.x, pm.y}; *(LAS v2u*)(tb + 1536) = v2u{pm.z, pm.w};
        *(LAS v2u*)(tb + 2048) = v2u{vf.x, vf.y}; *(LAS v2u*)(tb + 2560) = v2u{vf.z, vf.w};
      }
      if (first) {
        VMWAIT(4);
        if (lead) {
          v4f cq = __builtin_bit_cast(v4f, fl0) + __builtin_bit_cast(v4f, fl1);
#pragma unroll
          for (int i = 0; i < 4; ++i) {
            float u = cq[i];
            u += asf(__builtin_amdgcn_update_dpp(0, asu(u), 0x128, 0xf, 0xf, false));
            cq[i] = xrow(1.f, u);
          }
          *(LAS v4f*)(lbase(L_CS + 256 + (w & 3) * 128 + (vl16 & 127))) = cq;
        }
      }
      v4u hwk_ = {hwt.x, hwt.y, hwk.z, hwk.w};
      unsigned kvp = W_KVP + LS * rep * 1048576 + h * 65536 + (w & 3) * 1024 + (lead ? t * 4096 : 0);
      const lptr pa = lbase((size_t)str + (w & 2) * 512), pb = lbase((size_t)str + 2048 + (w & 1) * 512);
      asm volatile("" : "+v"(hwk_));
      asm volatile("" : "+s"(kvp));
      wg_bar();
      STAMP(4 + layer * 12);
      auto residual = [&]() INL {
        park[0] = __builtin_amdgcn_raw_buffer_load_b128(__builtin_amdgcn_make_buffer_rsrc((void*)p.x, 0, SEQ * DM * 2, 0x00020000), vres0, 0, 0); SB();
      };
      if (lead) {
        __builtin_amdgcn_s_setprio(2);
        v4u bv[4], x0[4], x1[4];
#pragma unroll
        for (int kk = 0; kk < 4; ++kk) {
          bv[kk] = cat(trd(pb + kk * 6144), trd(pb + kk * 6144 + 3072));
          x0[kk] = cat(trd(pa + kk * 6144), trd(pa + kk * 6144 + 3072));
          x1[kk] = cat(trd(pa + kk * 6144 + 512), trd(pa + kk * 6144 + 3072 + 512));
          SB();
        }
        v4f d0 = z4, d1 = z4;
#pragma unroll
        for (int kk = 0; kk < 4; ++kk) { d0 = mfma(x0[kk], bv[kk], d0); d1 = mfma(x1[kk], bv[kk], d1); }
        v4u dv = {pk(d0.x, d0.y), pk(d0.z, d0.w), pk(d1.x, d1.y), pk(d1.z, 0.f)};
        const v4u hs = imma(hwk_, dv, zu);
        dv.w |= ((hs.x + kk_) & 0x3fffu) << 16;
        __builtin_amdgcn_raw_buffer_store_b128(dv, rs_ws, vl16, kvp, SC1);
        keep(dv);
        __builtin_amdgcn_s_setprio(0);
        STAMP(5 + layer * 12);
        if (first) { load_wo(4, 9); residual(); }
        qf = q_gemm(L2E, 0.f);
        feature_map(qf, wphi, pp, pm, []() {});
        if (first) {
          asm volatile("" : "+v"(pp), "+v"(pm) :: "memory");
          load_wo(9, 14);
#pragma unroll
          for (int j = 0; j < 4; ++j) if (j < 3 || w < 6) dma2s(L_WQB + (w & 3) * 2048 + j * 8192, vl16, srdW, ptA - 8192 + 16 * 86016 + L_WQ + j * 8192);
        }
      } else {
        const unsigned sh = W_HINT + 2 * layer * 32768 + h * 128;
        __builtin_amdgcn_s_setprio(1);
        __builtin_amdgcn_s_sleep(DLY_T);
        const v2u hv = hint_get(vcol, sh);
        if (first) { load_wo(4, 6); residual(); }
        qf = q_gemm(L2E, 0.f);
        pause(lateness(hv, t, 150, key0, srdW, vcol, sh, budget) + T_KV);
        if (w == 0) hint_put(2 * layer + 1);
        fence();
        STAMP(6 + layer * 12);
        v4u kp[16];
#pragma unroll
        for (int i = 0; i < 8; ++i) kp[i] = ld16(rs_ws, vl16, kvp + i * 4096);
        feature_map(qf, wphi, pp, pm, [&]() INL {
          SB();
#pragma unroll
          for (int i = 8; i < 16; ++i) kp[i] = ld16(rs_ws, vl16, kvp + i * 4096);
        });
        const unsigned lo = lane_id();
        const unsigned bit = ((lo >> 4) == ((lo >> 2) & 3)) ? ((lo & 1) ? 0x3f800000u : 0x3f80u) : 0u;
        const unsigned u0 = (lo & 2) ? 0u : bit, u1 = (lo & 2) ? bit : 0u;
        const v4u sa = {u0, u1, 0u, 0u}, sb = {0u, 0u, u0, u1};
        const lptr kd = lbase((size_t)sl16 + (L_KVF - L_SCR) + w * 1024);
        auto add_up = [&](bool once) INL {
          v4f lo4 = z4, hi4 = z4;
          v4u hs = zu;
          unsigned ck = 0u - 16u * kk_;
          SB();
#pragma unroll
          for (int i = 0; i < 16; ++i) {
            hs = imma(hwk_, kp[i], hs); lo4 = mfma(sa, kp[i], lo4); hi4 = mfma(sb, kp[i], hi4);
            ck += kp[i].w >> 16;
            SB();
            if (first && once && i < 8) load_wo(6 + i, 7 + i);
          }
          lwr(kd, v4u{pk(lo4.x, lo4.y), pk(lo4.z, lo4.w), pk(hi4.x, hi4.y), pk(hi4.z, 0.f)});
          return nonzero((hs.x - ck) & 0x3fffu);
        };
        if (__builtin_expect(add_up(true), 0)) {
          do {
#pragma unroll
            for (int i = 0; i < 16; i += 4) reload4(kp[i], kp[i + 1], kp[i + 2], kp[i + 3], srdW, vl16, kvp + i * 4096, kvp + (i + 1) * 4096, kvp + (i + 2) * 4096, kvp + (i + 3) * 4096);
          } while (add_up(false) && spend(budget));
        }
      }
      __builtin_amdgcn_s_setprio(0);
      wg_bar();
      STAMP(7 + layer * 12);
      if (lead) oh = hint_get(vrow, w == 7 ? soh : W_END);
      v4u kv[4];
      kv[0] = lrd(sl16 + (L_KVF - L_SCR) + 1024); kv[1] = lrd(sl16 + (L_KVF - L_SCR) + 3072);
      kv[2] = lrd(sl16 + (L_KVF - L_SCR)); kv[3] = lrd(sl16 + (L_KVF - L_SCR) + 2048);
      SB();
      o1 = mfma(kv[0], pp, z4); o1 = mfma(kv[1], pm, o1); SB();
      o0 = mfma(kv[2], pp, z4); o0 = mfma(kv[3], pm, o0); SB();
      den = xrow(sel3, o1.x) + 1e-6f;
    } else {
      const unsigned hb = W_HALO + LS * rep * 4194304 + h * 262144;
      const unsigned hp = t < 15 ? hb + (t * 8 + w) * 2048 : W_END;
      __builtin_amdgcn_raw_buffer_store_b128(kf, rs_ws, vl16, hp, SC1); SB();
      const v4u vf = v_pack();
      v4u vp;
      {
        const v4u hs = imma(hwt, vf, imma(hwt, kf, zu));
        const unsigned e0 = (hs.y << 13) + hs.x + kf.z + ka, e1 = (hs.w << 13) + hs.z + kf.w + kb_;
        vp = v4u{vf.x, vf.y, g3 ? e0 : vf.z, g3 ? e1 : vf.w};
      }
      __builtin_amdgcn_raw_buffer_store_b128(vp, rs_ws, vl16 + 1024, hp, SC1); keep(vp);
      const lptr kw = sl16 + w * 2048, vw = stw + w * 2048, vr = str + w * 2048;
      if (!lead || w < 7) lwr(kw + 32768, kf);
      *(LAS v2u*)(vw + 32768 + 1024) = v2u{vf.x, vf.y}; *(LAS v2u*)(vw + 32768 + 1536) = v2u{vf.z, vf.w};
      STAMP(4 + layer * 12);
      v4u hk[4], hv[4];
      unsigned hs[4];
      if (lead) {
        load_wo(6, 14);
        wg_bar();
      } else {
#pragma unroll
        for (int i = 0; i < 4; ++i) hs[i] = t - 2 + (i >> 1) >= 0 ? hb + ((t - 2) * 8 + w + 4 * i) * 2048 : W_END;
        wg_bar();
        pause(lateness(hh, 2, 200, key0, srdW, vh, shh, budget) + T_H);
#pragma unroll
        for (int i = 0; i < 4; ++i) { hk[i] = ld16(rs_ws, vl16, hs[i]); hv[i] = ld16(rs_ws, vl16 + 1024, hs[i]); }
        __builtin_amdgcn_s_setprio(1);
      }
      const v4u qf = q_gemm(0.18898223650461357f * L2E, 1.f);
      const float NEG = -3.0e38f;
      v4f sd = mfma(kf, qf, z4);
#pragma unroll
      for (int r = 0; r < 4; ++r) sd[r] = (4 * g + r <= c) ? sd[r] : NEG;
      float m = xmax1(mx(mx3(sd.x, sd.y, sd.z), sd.w));
      auto diag = [&]() INL {
        const v2u a0 = trd(vr + 32768 + 1024), a1 = trd(vr + 32768 + 1536);
        const v4f e = sd - m;
        const v2u pd = {pk(ex2(e.x), ex2(e.y)), pk(ex2(e.z), ex2(e.w))};
        typedef short v4h __attribute__((ext_vector_type(4)));
        o0 = __builtin_amdgcn_mfma_f32_16x16x16bf16_1k(__builtin_bit_cast(v4h, a0), __builtin_bit_cast(v4h, pd), z4, 0, 0, 0);
        o1 = __builtin_amdgcn_mfma_f32_16x16x16bf16_1k(__builtin_bit_cast(v4h, a1), __builtin_bit_cast(v4h, pd), z4, 0, 0, 0);
      };
      diag();
      auto take_halo = [&]() INL {
        STAMP(6 + layer * 12);
        if (!lead) {
          const unsigned nv = 2u * min(t, 2);
          const lptr kd = kw, vd = vw;
          auto stage = [&]() INL {
            v4u hs_ = zu;
            unsigned e0 = nv * ka, e1 = nv * kb_;
#pragma unroll
            for (int i = 0; i < 4; ++i) {
              const unsigned none = t - 2 + (i >> 1) < 0 ? 0xfe000000u : 0u;
              hs_ = imma(hwt, hk[i], hs_);
              lwr(kd + i * 8192, v4u{hk[i].x, hk[i].y, hk[i].z, hk[i].w | none});
              SB();
              hs_ = imma(hwt, hv[i], hs_);
              *(LAS v2u*)(vd + i * 8192 + 1024) = v2u{hv[i].x, hv[i].y};
              *(LAS v2u*)(vd + i * 8192 + 1536) = v2u{g3 ? 0x3f80u : hv[i].z, g3 ? 0u : hv[i].w};
              e0 += hk[i].z - hv[i].z; e1 += hk[i].w - hv[i].w;
              SB();
            }
            return nonzero(((hs_.y << 13) + hs_.x + e0) | ((hs_.w << 13) + hs_.z + e1), ROW3);
          };
          if (__builtin_expect(stage(), 0)) {
            do {
              reload4(hk[0], hk[1], hk[2], hk[3], srdW, vl16, hs[0], hs[1], hs[2], hs[3]);
              reload4(hv[0], hv[1], hv[2], hv[3], srdW, vl16 + 1024, hs[0], hs[1], hs[2], hs[3]);
            } while (stage() && spend(budget));
          }
        }
        wg_bar();
        STAMP(7 + layer * 12);
      };
      {
        const v4f cm = {-m, -m, -m, -m};
        v4f sa, sb; v4u a0, a1;
#pragma unroll
        for (int j = 7; j >= 0; --j) {
          if (j >= 4 && (j < 6) == lead && (w >> 1) == 7 - j) take_halo();
          if (!lead && j == 6) { VMKNOWN(0); if (w == 3) hint_put(2 * layer + 1); }
          if (j == (lead ? 4 : 6)) touch();
          if (!lead && j <= 6 && j >= 3) load_wo(6 + 2 * (6 - j), 8 + 2 * (6 - j));
          if (!lead && j == POLL_OB) oh = hint_get(vrow, w == 3 ? soh : W_END);
          const v4u ka = lrd(kw + j * 4096), kb = lrd(kw + j * 4096 + 2048);
          const v4u n0 = cat(trd(vr + j * 4096 + 1024), trd(vr + j * 4096 + 3072)), n1 = cat(trd(vr + j * 4096 + 1536), trd(vr + j * 4096 + 3584));
          SB();
          if (j < 7) { const v4u pf = pexp(sa, sb); o0 = mfma(a0, pf, o0); o1 = mfma(a1, pf, o1); }
          sa = mfma(ka, qf, cm); sb = mfma(kb, qf, cm);
          a0 = n0; a1 = n1;
          SB();
        }
#pragma unroll
        for (int r = 0; r < 4; ++r) sa[r] = (4 * g + r > c) ? sa[r] : NEG;
        const v4u pf = pexp(sa, sb);
        o1 = mfma(a1, pf, o1); o0 = mfma(a0, pf, o0);
      }
      den = xrow(sel3, o1.x);
      if (__builtin_expect(__any(!(den < 1.0e18f)), 0)) {
        float mt = m;
#pragma unroll 1
        for (int k = 0; k < 16; ++k) {
          v4f s = mfma(lrd(kw + k * 2048), qf, z4);
#pragma unroll
          for (int r = 0; r < 4; ++r) s[r] = (k > 0 || 4 * g + r > c) ? s[r] : NEG;
          mt = mx3(mt, mx(s.x, s.y), mx(s.z, s.w));
        }
        m = xmax1(mt);
        diag();
        const v4f cm = {-m, -m, -m, -m};
#pragma unroll 1
        for (int j = 7; j >= 0; --j) {
          v4f sa = mfma(lrd(kw + j * 4096), qf, cm);
          const v4f sb = mfma(lrd(kw + j * 4096 + 2048), qf, cm);
#pragma unroll
          for (int r = 0; r < 4; ++r) sa[r] = (j > 0 || 4 * g + r > c) ? sa[r] : NEG;
          const v4u pf = pexp(sa, sb);
          o0 = mfma(cat(trd(vr + j * 4096 + 1024), trd(vr + j * 4096 + 3072)), pf, o0);
          o1 = mfma(cat(trd(vr + j * 4096 + 1536), trd(vr + j * 4096 + 3584)), pf, o1);
        }
        den = xrow(sel3, o1.x);
      }
    }
    const unsigned ob = W_O + LS * layer * 2097152 + t * 131072;
    {
      const float rd = __builtin_amdgcn_rcpf(den);
      o0 *= rd; o1 *= rd;
      v4u v = {pk(o0.x, o0.y), pk(o0.z, o0.w), pk(o1.x, o1.y), pk(o1.z, o1.w)};
      const v4u hs = imma(hwt, v, zu);
      const unsigned e0 = (hs.y << 13) + hs.x + ka, e1 = (hs.w << 13) + hs.z + kb_;
      v.z = g3 ? e0 : v.z; v.w = g3 ? e1 : v.w;
      __builtin_amdgcn_raw_buffer_store_b128(v, rs_ws, vl16, ob + (w * 16 + h) * 1024, SC1);
      keep(v);
    }
    __builtin_amdgcn_s_setprio(0);
    STAMP(8 + layer * 12);
    auto swap_kv = [&](int j0, int j1) INL {
#pragma unroll
      for (int j = j0; j < j1 && j < (lead ? 3 : 4); ++j) {
        const lptr a = lbase(w * 2048 + j * 16384 + vl16);
        v4u t0, t1;
        if (layer < 2) { t0 = lrd(a); t1 = lrd(a + 1024); }
        if (!first) { lwr(a, park[2 * j]); lwr(a + 1024, park[2 * j + 1]); }
        if (layer < 2) { park[2 * j] = t0; park[2 * j + 1] = t1; }
      }
      if (first) {
        asm volatile("s_waitcnt lgkmcnt(0)" ::: "memory");
#pragma unroll
        for (int j = j0; j < j1 && j < (lead ? 3 : 4); ++j) dma2s(w * 2048 + j * 16384, vl16, srdW, ptA + 16 * 86016 + j * 16384);
      }
    };
    if (first) {
      const v4u xr = swap16(park[0]);
      res0 = v4f{asf(xr.x << 16), asf(xr.x & 0xffff0000u), asf(xr.y << 16), asf(xr.y & 0xffff0000u)};
      res1 = v4f{asf(xr.z << 16), asf(xr.z & 0xffff0000u), asf(xr.w << 16), asf(xr.w & 0xffff0000u)};
    }
    unsigned op = ob + (2 * (h >> 2) + (lead ? 0 : 1)) * 16384 + (w & 3) * 1024;
    asm volatile("" : "+s"(op));
    v4u of[4];
    if (isA) swap_kv(0, 2);
    if (lead == isA && w == (isA ? 7 : 3)) *(LAS int*)(csg + 768) = lateness(oh, h, isA ? 165 : 125, key0, srdW, vrow, soh, budget);
    wg_bar();
    const int olate = __builtin_amdgcn_readfirstlane(*(LAS int*)(csg + 768));
    if (!isA && more) swap_kv(0, 2);
    pause(olate + (isA ? T_OA : T_OB) + (more ? 0 : 6));
#pragma unroll
    for (int i = 0; i < 4; ++i) of[i] = ld16(rs_ws, vl16, op + i * 4096);
    if (more && !first) swap_kv(2, 4);
    lptr lo[4], hi[4];
#pragma unroll
    for (int i = 0; i < 4; ++i) {
      const unsigned sb_ = L_SCR + (lead ? 0 : 14336) + vl16, full = sb_ + ((w & 3) + 4 * i) * 768;
      lo[i] = lbase(g3 ? sb_ + 12288 - 768 + (((w & 3) >> 1) + 2 * i) * 256 + (w & 1) * 8 : full);
      hi[i] = lbase(g3 ? L_SCR + 45056 + vl16 : full + 8);
    }
    STAMP(9 + layer * 12);
    auto stage_o = [&]() INL {
      auto put = [&]() INL {
        v4u hs = zu;
        unsigned e0 = 4u * ka, e1 = 4u * kb_;
#pragma unroll
        for (int i = 0; i < 4; ++i) {
          hs = imma(hwt, of[i], hs);
          *(LAS v2u*)(lo[i]) = v2u{of[i].x, of[i].y};
          *(LAS v2u*)(hi[i]) = v2u{of[i].z, of[i].w};
          e0 -= of[i].z; e1 -= of[i].w;
          SB();
        }
        return nonzero(((hs.y << 13) + hs.x + e0) | ((hs.w << 13) + hs.z + e1), ROW3);
      };
      if (__builtin_expect(put(), 0)) {
        do reload4(of[0], of[1], of[2], of[3], srdW, vl16, op, op + 4096, op + 8192, op + 12288);
        while (put() && spend(budget));
      }
    };
    stage_o();
    wg_bar();
    if (first) swap_kv(2, 4);
    const unsigned sx = W_HINT + (8 + layer) * 32768 + t * 2048;
    if (lead && w == 7) {
      if (more) {
        hint_put(8 + layer);
        __builtin_amdgcn_s_sleep(P_X);
        *(LAS int*)(csg + 832) = lateness(hint_get(vrow, sx), h, 70, key0, srdW, vrow, sx, budget);
      }
    }
    int w2 = w;
    asm volatile("" : "+s"(w2));
    if (!lead || w2 != 7) {
      const unsigned won = wos + (isA ? 28 * 14336 : 0);
      v4u bq[8];
#pragma unroll
      for (int i = 0; i < 4; ++i) { bq[2 * i] = lrd(sl16 + i * 1024); SB(); bq[2 * i + 1] = lrd(sl16 + 14336 + i * 1024); SB(); }
#pragma unroll
      for (int s = 0; s < 14; ++s) {
        const int i = (2 * s) & 7;
        if (!(s & 1)) { if (s < 12) LGKNOWN(4); else LGKNOWN(0); SB(); }
        res0 = mfma(wof[s], bq[i], res0); SB(); if (s + 4 < 14) { bq[i] = lrd(sl16 + (s + 4) * 1024); SB(); }
        res1 = mfma(wof[s], bq[i + 1], res1); SB(); if (s + 4 < 14) { bq[i + 1] = lrd(sl16 + 14336 + (s + 4) * 1024); SB(); }
        if (more && s >= 1 && s < 7) wof[s - 1] = ld16(rs_ws, vl16 + ((s - 1) & 3) * 1024, won + ((s - 1) >> 2) * 4096);
      }
      const v4u xo = swap16(v4u{pk(res0.x, res0.y), pk(res0.z, res0.w), pk(res1.x, res1.y), pk(res1.z, res1.w)});
      if (more) {
        __builtin_amdgcn_raw_buffer_store_b128(xo, rs_ws, pubx, W_X + LS * (layer + 1) * TILEBUF + tp, SC1);
        keep(xo);
        auto r = __builtin_amdgcn_permlane32_swap(xo.x + xo.z, xo.y + xo.w, false, false);
        const unsigned r0 = r[0], r1 = r[1];
        *(LAS v2u*)(lbase(L_SCR + 28672 + w * 512 + (vl16 >> 1))) = v2u{r0 + r1, asu(sumsq8(xo))};
      } else {
        const unsigned lo = lane_id();
        __builtin_amdgcn_raw_buffer_store_b128(xo, __builtin_amdgcn_make_buffer_rsrc(p.out, 0, SEQ * DM * 2, 0x00020000),
                                               (lo & 31) * (DM * 2) + (2 * ft + (lo >> 5)) * 16, (t * 128 + (h >> 2) * 32) * (DM * 2), 0);
        keep(xo);
      }
    }
    STAMP(10 + layer * 12);
    if (more) {
      if (first) VMWAIT(7);
      wg_bar();
      const int late = __builtin_amdgcn_readfirstlane(*(LAS int*)(csg + 832));
      if (lead && w2 == 7) {
        float q = 0.f;
        unsigned a = 0u;
        const lptr sp = lbase(L_SCR + 28672 + (vl16 >> 1));
#pragma unroll
        for (int i = 0; i < 7; ++i) { const v2u v = *(LAS v2u*)(sp + i * 512); a += v.x; q += asf(v.y); }
        auto r = __builtin_amdgcn_permlane32_swap(asu(q), asu(q), false, false);
        auto ra = __builtin_amdgcn_permlane32_swap(a, a, false, false);
        const unsigned r0 = r[0], r1 = r[1], a0 = ra[0], a1 = ra[1], qs = asu(asf(r0) + asf(r1));
        const v4u rec = {qs, ka + SALT - qs - a0, kb_ + SALT - a1, 0u};
        __builtin_amdgcn_raw_buffer_store_b128(rec, rs_ws, vl16 < 512 ? (vl16 >> 8) * 1024 + (vl16 & 255) : OOB,
                                               W_ST + LS * (layer + 1) * 131072 + (t * 8 + 2 * (h >> 2)) * 1024 + (h & 3) * 256, SC1);
        keep(rec);
        if (isA) hint_put(2 * layer + 2, max(late, 0));
        pause(late + T_X - 12);
      } else {
        pause(late + T_X);
      }
    }
  };
  if (w_ >= 4) {
    run_layer(Layer<0>{}, Layer<1>{}); run_layer(Layer<1>{}, Layer<1>{}); run_layer(Layer<2>{}, Layer<1>{}); run_layer(Layer<3>{}, Layer<1>{});
    if (w_ == 7 && b == 0) __builtin_amdgcn_raw_buffer_store_b32(epoch, rs_ws, 0, W_KEY, 0);
  }
  int wz = w_;
  asm volatile("" : "+s"(wz));
  if (wz < 4) {
    run_layer(Layer<0>{}, Layer<0>{}); run_layer(Layer<1>{}, Layer<0>{}); run_layer(Layer<2>{}, Layer<0>{}); run_layer(Layer<3>{}, Layer<0>{});
  }
}
}

extern "C" void kernel_launch(void* const* d_in, const int* in_sizes, int n_in, void* d_out, int out_size, void* d_ws, size_t ws_size,
                              hipStream_t stream) {
  (void)in_sizes; (void)n_in; (void)out_size; (void)ws_size;
  (void)hipFuncSetAttribute((const void*)fused_layers, hipFuncAttributeMaxDynamicSharedMemorySize, (int)L_TOTAL);
  Params p;
  p.x = d_in[0]; p.wA = d_in[1]; p.wphi = d_in[2]; p.woA = d_in[3]; p.wB = d_in[5]; p.woB = d_in[6]; p.out = d_out; p.ws = d_ws;
  hipLaunchKernelGGL(fused_layers, dim3(256), dim3(512), L_TOTAL, stream, p);
}
```

```cpp
#include <hip/hip_runtime.h>
#include <stdint.h>

#define LAS __attribute__((address_space(3)))
#define DEV __device__ __forceinline__
typedef unsigned v4u __attribute__((ext_vector_type(4)));
typedef unsigned v2u __attribute__((ext_vector_type(2)));
typedef float v4f __attribute__((ext_vector_type(4)));
typedef float v2f __attribute__((ext_vector_type(2)));
typedef int v4i __attribute__((ext_vector_type(4)));
typedef short v4s __attribute__((ext_vector_type(4)));
typedef __bf16 v8bf __attribute__((ext_vector_type(8)));
typedef __bf16 v2bf __attribute__((ext_vector_type(2)));
typedef __amdgpu_buffer_rsrc_t rsrc_t;

#define TPX 4
#define DLY_T 3
#define WO1 6
#define POLL_OB 2
#define T_KV 4
#define T_H 20
#define T_OA 0
#define T_OB 12
#define P_X 8
#define T_X 8
#define T_XL 0
#define T_OT 20
#define PRI0 1
#define RAMP 0
#define WOS 1
#define T_HL -8
#define ILV 1
#define WIN 4

namespace {
constexpr int SEQ = 2048, DM = 448;
constexpr unsigned L_WK = 0, L_WV = 28672, L_WQ = 57344, L_WQB = 86016, L_SCR = 114688, L_TOTAL = 163840;
constexpr unsigned L_CS = L_SCR + 23 * 2048;
constexpr unsigned L_KVF = L_SCR + 40960;
constexpr unsigned TILEBUF = 16 * 8 * 14 * 1024;
constexpr unsigned W_KEY = 0, W_FLAG = 2048, W_HINT = 4096, W_ST = W_HINT + 12 * 32768, W_KVP = W_ST + 4 * 131072, W_O = W_KVP + 2 * 1048576,
                   W_HALO = W_O + 4 * 2097152, W_X = W_HALO + 2 * 4194304, W_PT = W_X + 4 * TILEBUF, W_WO = W_PT + 2 * 16 * 86016,
                   W_CS = W_WO + 2 * 28 * 14336, W_END = W_CS + 16 * 56 * 128;
constexpr unsigned W_DBG = 64u << 20;
constexpr int SC1 = 16;
constexpr unsigned OOB = 0x40000000u;

DEV v4f mfma(v4u a, v4u b, v4f c) {
  return __builtin_amdgcn_mfma_f32_16x16x32_bf16(__builtin_bit_cast(v8bf, a), __builtin_bit_cast(v8bf, b), c, 0, 0, 0);
}
DEV unsigned pk(float a, float b) { v2f f = {a, b}; v2bf r = __builtin_convertvector(f, v2bf); return __builtin_bit_cast(unsigned, r); }
typedef LAS char* lptr;
DEV lptr lbase(unsigned v) { asm volatile("" : "+v"(v)); return (lptr)(size_t)v; }
DEV v2u trd(lptr a) { v4s r = __builtin_amdgcn_ds_read_tr16_b64_v4i16((LAS v4s*)a); return __builtin_bit_cast(v2u, r); }
DEV v4u cat(v2u a, v2u b) { return v4u{a.x, a.y, b.x, b.y}; }
DEV v4u lrd(lptr a) { return *(LAS v4u*)a; }
DEV void lwr(lptr a, v4u v) { *(LAS v4u*)a = v; }
#define VMWAIT(n) asm volatile("s_waitcnt vmcnt(" #n ")" ::: "memory")
DEV float asf(unsigned u) { return __builtin_bit_cast(float, u); }
DEV unsigned asu(float f) { return __builtin_bit_cast(unsigned, f); }
DEV void wg_bar() { asm volatile("s_waitcnt lgkmcnt(0)\n\ts_barrier" ::: "memory"); }
DEV void drain() { asm volatile("s_waitcnt vmcnt(0)" ::: "memory"); }
DEV void fence() { asm volatile("" ::: "memory"); }
DEV float ex2(float x) { return __builtin_amdgcn_exp2f(x); }

DEV unsigned lane_id() { unsigned r; asm volatile("v_mbcnt_lo_u32_b32 %0, -1, 0\n\tv_mbcnt_hi_u32_b32 %0, -1, %0" : "=v"(r)); __builtin_assume(r < 64); return r; }
DEV float mx(float a, float b) { return __builtin_elementwise_maximum(a, b); }
DEV float mn(float a, float b) { return __builtin_elementwise_minimum(a, b); }
DEV float mx3(float a, float b, float c) { return mx(mx(a, b), c); }
DEV float mn3(float a, float b, float c) { return mn(mn(a, b), c); }
DEV float xrow(float sel, float v) { const v4f z = {0.f, 0.f, 0.f, 0.f}; const v4f r = __builtin_amdgcn_mfma_f32_16x16x4f32(sel, v, z, 0, 0, 0); return r.x; }
DEV v4f xrow4(float sel, float v) { const v4f z = {0.f, 0.f, 0.f, 0.f}; return __builtin_amdgcn_mfma_f32_16x16x4f32(sel, v, z, 0, 0, 0); }
DEV v4u swap16(v4u v) {
  auto r = __builtin_amdgcn_permlane16_swap(v.x, v.z, false, false);
  auto q = __builtin_amdgcn_permlane16_swap(v.y, v.w, false, false);
  const unsigned r0 = r[0], r1 = r[1], q0 = q[0], q1 = q[1];
  return v4u{r0, q0, r1, q1};
}
DEV float sumsq8(v4u v) {
  const v4f lo = __builtin_bit_cast(v4f, v << 16), hi = __builtin_bit_cast(v4f, v & 0xffff0000u), q = lo * lo + hi * hi;
  return (q.x + q.y) + (q.z + q.w);
}
DEV void xmax2(float& a, float& b) {
  auto r = __builtin_amdgcn_permlane32_swap(asu(a), asu(b), false, false);
  unsigned r0 = r[0], r1 = r[1];
  float z = mx(asf(r0), asf(r1));
  auto q = __builtin_amdgcn_permlane16_swap(asu(z), asu(z), false, false);
  unsigned q0 = q[0], q1 = q[1];
  z = mx(asf(q0), asf(q1));
  auto p = __builtin_amdgcn_permlane32_swap(asu(z), asu(z), false, false);
  unsigned p0 = p[0], p1 = p[1];
  a = asf(p0); b = asf(p1);
}
DEV float xmax1(float a) {
  auto r = __builtin_amdgcn_permlane32_swap(asu(a), asu(a), false, false);
  unsigned r0 = r[0], r1 = r[1];
  float z = mx(asf(r0), asf(r1));
  auto q = __builtin_amdgcn_permlane16_swap(asu(z), asu(z), false, false);
  unsigned q0 = q[0], q1 = q[1];
  return mx(asf(q0), asf(q1));
}
DEV void dma2(unsigned ldsdst, unsigned va, unsigned vb, v4u srd, unsigned sa, unsigned sb) {
  asm volatile("s_mov_b32 m0, %0\n\ts_nop 3\n\tbuffer_load_dwordx4 %1, %3, %4 offen lds\n\tbuffer_load_dwordx4 %2, %3, %5 offen offset:1024 lds"
               :: "s"(ldsdst), "v"(va), "v"(vb), "s"(srd), "s"(sa), "s"(sb) : "memory");
}
DEV void keep(v4u v) { asm volatile("s_nop 1" :: "v"(v) : "memory"); }
DEV void keep(v4u v, v4u u) { asm volatile("s_nop 1" :: "v"(v), "v"(u) : "memory"); }
DEV void dma2s(unsigned ldsdst, unsigned v, v4u srd, unsigned so) {
  asm volatile("s_mov_b32 m0, %0\n\ts_nop 3\n\tbuffer_load_dwordx4 %1, %2, %3 offen sc1 lds\n\tbuffer_load_dwordx4 %1, %2, %3 offen offset:1024 sc1 lds"
               :: "s"(ldsdst), "v"(v), "s"(srd), "s"(so) : "memory");
}
DEV v4u ld16(rsrc_t rs, unsigned v, unsigned so) {
  const v4u r = __builtin_amdgcn_raw_buffer_load_b128(rs, v, so, SC1);
  __builtin_amdgcn_sched_barrier(0);
  return r;
}
DEV v4u mk_srd(const void* p, unsigned bytes) {
  unsigned long long a = (unsigned long long)p;
  return v4u{(unsigned)a, (unsigned)(a >> 32) & 0xffffu, bytes, 0x00020000u};
}
DEV v4u imma(v4u a, v4u b, v4u c) {
  const v4i r = __builtin_amdgcn_mfma_i32_16x16x64_i8(__builtin_bit_cast(v4i, a), __builtin_bit_cast(v4i, b), __builtin_bit_cast(v4i, c), 0, 0, 0);
  return __builtin_bit_cast(v4u, r);
}
DEV v4u weights(unsigned lo) {
  const unsigned a = (lo + 1) * 0x9e3779b1u, b = (a ^ (a >> 15)) * 0x85ebca77u, c = (b ^ (b >> 13)) * 0xc2b2ae3du, d = (c ^ (c >> 16)) * 0x27d4eb2fu;
  return v4u{(a ^ (a >> 16)) | 0x01010101u, (b ^ (b >> 16)) | 0x01010101u, (c ^ (c >> 16)) | 0x01010101u, (d ^ (d >> 16)) | 0x01010101u};
}
DEV void keys(unsigned e, unsigned& k0, unsigned& k1) { k0 = e * 0x9e3779b1u + 0x7f4a7c15u; k1 = e * 0x85ebca77u + 0x165667b1u; }
DEV unsigned xsum2u(unsigned a, unsigned b) {
  auto r = __builtin_amdgcn_permlane32_swap(a, b, false, false);
  const unsigned r0 = r[0], r1 = r[1], z = r0 + r1;
  auto q = __builtin_amdgcn_permlane16_swap(z, z, false, false);
  const unsigned q0 = q[0], q1 = q[1];
  return q0 + q1;
}
DEV void reload4(v4u& a, v4u& b, v4u& c, v4u& d, v4u srd, unsigned voff, unsigned sa, unsigned sb, unsigned sc, unsigned sd) {
  asm volatile("s_sleep 2\n\tbuffer_load_dwordx4 %0, %4, %5, %6 offen sc1\n\tbuffer_load_dwordx4 %1, %4, %5, %7 offen sc1\n\t"
               "buffer_load_dwordx4 %2, %4, %5, %8 offen sc1\n\tbuffer_load_dwordx4 %3, %4, %5, %9 offen sc1\n\ts_waitcnt vmcnt(0)"
               : "+v"(a), "+v"(b), "+v"(c), "+v"(d) : "v"(voff), "s"(srd), "s"(sa), "s"(sb), "s"(sc), "s"(sd) : "memory");
}
DEV bool spend(int& budget) { budget = max(budget - 1, 0); return budget != 0; }
DEV bool nonzero(unsigned v, unsigned long long lanes = ~0ull) { return (__builtin_amdgcn_uicmp(v, 0u, 33) & lanes) != 0ull; }
DEV bool row3_differs(v4u hs, unsigned e0, unsigned e1) {
  const unsigned long long m = __builtin_amdgcn_uicmp((hs.y << 13) + hs.x, e0, 33) | __builtin_amdgcn_uicmp((hs.w << 13) + hs.z, e1, 33);
  unsigned hi = (unsigned)(m >> 32);
  asm volatile("" : "+s"(hi));
  return (hi >> 16) != 0u;
}
DEV bool retry(bool bad, int& budget) {
  if (__builtin_expect(!__any(bad), 1)) return false;
  budget = max(budget - 1, 0);
  return __any(budget != 0);
}

DEV unsigned clock32() { unsigned long long t; asm volatile("s_memrealtime %0\n\ts_waitcnt lgkmcnt(0)" : "=s"(t) :: "memory"); return (unsigned)t; }
DEV int lateness(v2u v, int own, int seg, unsigned key, v4u srd, unsigned voff, unsigned soff, int& budget) {
  asm volatile("" : "+v"(v) :: "memory");
  int over = 0;
  if (__builtin_expect(nonzero(v.y - key, 0xffffull), 0)) {
    do {
      asm volatile("s_sleep 1\n\tbuffer_load_dwordx2 %0, %1, %2, %3 offen sc1\n\ts_waitcnt vmcnt(0)" : "+v"(v) : "v"(voff), "s"(srd), "s"(soff) : "memory");
    } while (nonzero(v.y - key, 0xffffull) && spend(budget));
    const unsigned tv = v.x;
    over = max((int)(clock32() - __builtin_amdgcn_readlane(tv, own)) - seg, 0);
  }
  const unsigned tv = v.x;
  int d = (int)(tv - __builtin_amdgcn_readlane(tv, own));
  d = max(d, __builtin_amdgcn_update_dpp(0, d, 0x128, 0xf, 0xf, false));
  d = max(d, __builtin_amdgcn_update_dpp(0, d, 0x124, 0xf, 0xf, false));
  d = max(d, __builtin_amdgcn_update_dpp(0, d, 0x122, 0xf, 0xf, false));
  d = max(d, __builtin_amdgcn_update_dpp(0, d, 0x121, 0xf, 0xf, false));
  return min(__builtin_amdgcn_readfirstlane(d), 1000) - over;
}
DEV void pause(int ticks) {
  asm volatile("s_cmp_lt_i32 %0, 1\n\ts_cbranch_scc1 2f\n1:\n\ts_sleep 1\n\ts_sub_i32 %0, %0, 4\n\ts_cmp_gt_i32 %0, 0\n\ts_cbranch_scc1 1b\n2:" : "+s"(ticks) :: "memory", "scc");
}

template <class F>
DEV void feature_map(v4u vf, const v4u (&wphi)[2], v4u& plus, v4u& minus, F mid) {
  const v4f z4 = {0.f, 0.f, 0.f, 0.f};
  const v4f p0 = mfma(wphi[0], vf, z4), p1 = mfma(wphi[1], vf, z4);
  float hi = mx3(mx3(mx3(p0.x, p0.y, p0.z), p0.w, p1.x), p1.y, p1.z);
  float lo = -mn3(mn3(mn3(p0.x, p0.y, p0.z), p0.w, p1.x), p1.y, p1.z);
  xmax2(hi, lo);
  mid();
  const v2f p01 = {p0.x, p0.y}, p23 = {p0.z, p0.w}, p45 = {p1.x, p1.y}, h2 = {hi, hi}, l2 = {lo, lo};
  v2f a01 = p01 - h2, a23 = p23 - h2, a45 = p45 - h2, b01 = p01 + l2, b23 = p23 + l2, b45 = p45 + l2;
  asm volatile("" : "+v"(a01), "+v"(a23), "+v"(a45), "+v"(b01), "+v"(b23), "+v"(b45));
  const v2f e01 = {ex2(a01.x), ex2(a01.y)}, e23 = {ex2(a23.x), ex2(a23.y)}, e45 = {ex2(a45.x), ex2(a45.y)};
  const float e6 = ex2(p1.z - hi);
  const v2f se = (e01 + e23) + e45;
  v4f te = xrow4(1.f, (se.x + e6) + se.y);
  __builtin_amdgcn_sched_barrier(0);
  const v2f f01 = {ex2(-b01.x), ex2(-b01.y)}, f23 = {ex2(-b23.x), ex2(-b23.y)}, f45 = {ex2(-b45.x), ex2(-b45.y)};
  const float f6 = ex2(-(p1.z + lo));
  const v2f sf = (f01 + f23) + f45;
  v4f tf = xrow4(1.f, (sf.x + f6) + sf.y);
  __builtin_amdgcn_sched_barrier(0);
  const float re = __builtin_amdgcn_rcpf(te.x);
  asm volatile("" :: "v"(te));
  const v2f x01 = e01 * re, x23 = e23 * re, x45 = e45 * re;
  plus = v4u{pk(x01.x, x01.y), pk(x23.x, x23.y), pk(x45.x, x45.y), pk(e6 * re, 0.f)};
  __builtin_amdgcn_sched_barrier(0);
  const float rf = __builtin_amdgcn_rcpf(tf.x);
  asm volatile("" :: "v"(tf));
  const v2f y01 = f01 * rf, y23 = f23 * rf, y45 = f45 * rf;
  minus = v4u{pk(y01.x, y01.y), pk(y23.x, y23.y), pk(y45.x, y45.y), pk(f6 * rf, 0.f)};
}

DEV v4u pexp(v4f a, v4f b) {
  return v4u{pk(ex2(a.x), ex2(a.y)), pk(ex2(a.z), ex2(a.w)), pk(ex2(b.x), ex2(b.y)), pk(ex2(b.z), ex2(b.w))};
}
#define VMKNOWN(n) __builtin_amdgcn_s_waitcnt(((n) & 15) | (((n) >> 4) << 14) | 0xf70)
#define LGKNOWN(n) __builtin_amdgcn_s_waitcnt(0xc07f | ((n) << 8))
#define PIN(x) asm volatile("" : "+v"(x))
#define SB() __builtin_amdgcn_sched_barrier(0)
#define INL __attribute__((always_inline))

#define STAMP(i) do {} while (0)

template <int N> struct Layer { static constexpr int value = N; };
struct Params {
  const void* x; const void* wA; const void* wphi; const void* woA; const void* wB; const void* woB; void* out; void* ws;
};

__global__ void __launch_bounds__(512, 2) fused_layers(Params) {
  typedef unsigned long long v8p __attribute__((ext_vector_type(8)));
  v8p args;
  asm volatile("s_load_dwordx16 %0, %1, 0x0" : "=s"(args) : "s"(__builtin_amdgcn_kernarg_segment_ptr()));
  const int b = blockIdx.x, xg = b & 7, xi = b >> 3;
  constexpr int HPX = 32 / TPX, NGT = 16 / TPX;
  const int t = (xg % NGT) * TPX + xi / HPX;
  const int h = (xg / NGT) * HPX + xi % HPX;
  const int wid = __builtin_amdgcn_readfirstlane(threadIdx.x >> 6) & 7;
  const int w_ = wid < 4 ? 7 - wid : wid - 4;
  const int l = threadIdx.x & 63, c = l & 15, g = l >> 4;
  const bool g3 = g == 3;
  STAMP(0);
  constexpr unsigned L_XS = 73728, L_JOB = L_WQ, L_CSP = L_XS + 81920;
  const unsigned chunk16 = ((l & 3) ^ ((l >> 5) << 1)) * 16, vqkv = (l >> 2) * (3 * DM * 2) + chunk16;
  const int kvp_ = (w_ >> 1) & 1, kvs = w_ & 1;
  const unsigned kvat = kvp_ * L_WV + kvs * 2048, kvso = kvs * (32 * 3 * DM * 2) + (kvp_ + 1) * DM * 2 + 56 * h;
  const unsigned xsb = L_XS + w_ * 10240, xso = (t * 128 + w_ * 16) * (DM * 2);
  const unsigned vx0 = (l >> 3) * (DM * 2) + ((l & 7) ^ (l >> 4)) * 16, vx1 = ((l >> 3) + 8) * (DM * 2) + ((l & 7) ^ (4 + (l >> 4))) * 16 - 1024;
  const bool job = t < 14 && (w_ < 4 || (w_ < 6 && h < 14));
  const unsigned jat = L_JOB + w_ * 2048;
  asm volatile("" :: "v"(vqkv), "v"(vx0), "v"(vx1), "s"(kvat), "s"(kvso), "s"(xsb), "s"(xso), "s"(jat));
  asm volatile("s_waitcnt lgkmcnt(0)" : "+s"(args));
  const Params p = {(const void*)args[0], (const void*)args[1], (const void*)args[2], (const void*)args[3], (const void*)args[4], (const void*)args[5], (void*)args[6], (void*)args[7]};
  const v4u srdA = mk_srd(p.wA, DM * 3 * DM * 2), srdX = mk_srd(p.x, SEQ * DM * 2);
  auto kvblock = [&](int j) INL { const unsigned so = kvso + j * (64 * 3 * DM * 2); dma2(kvat + j * 4096, vqkv, vqkv, srdA, so, so + 16 * 3 * DM * 2 - 1024); };
  auto xline = [&](int j, int up) INL {
    const unsigned so = xso + up * (64 * DM * 2) + j * 128;
    dma2(xsb + up * 40960 + (j % 5) * 2048, vx0, vx1, srdX, so, so);
  };
  if (w_ < 4) {
    __builtin_amdgcn_s_setprio(PRI0);
    const unsigned so = t * (32 * 3 * DM * 2) + (w_ == 3 ? 0 : w_ * DM * 2) + 56 * h;
    if (job) dma2(jat, vqkv, vqkv, mk_srd(w_ ? p.wB : p.wA, DM * 3 * DM * 2), so, so + 16 * 3 * DM * 2 - 1024);
    xline(0, 0); xline(0, 1);
  } else {
    if (job) {
      const unsigned v = (l >> 2) * (DM * 2) + chunk16, so = t * (32 * DM * 2) + 64 * h;
      dma2(jat, v, v, mk_srd(w_ == 5 ? p.woB : p.woA, DM * DM * 2), so, so + 16 * DM * 2 - 1024);
    } else if (w_ == 7) {
      const unsigned e = (l >> 2) & 7, g0 = l >> 5, d0 = e < 4 ? 4 * g0 + e : 12 + 3 * g0 + e, d1 = d0 + (e < 4 ? 8 : 6);
      dma2(jat, e < 7 ? d0 * 56 + chunk16 + 1024 : OOB, e < 7 ? d1 * 56 + chunk16 : OOB,
           mk_srd((const char*)p.wphi - 1024, 16 * 28 * 28 * 2 + 1024), h * 28 * 28 * 2, h * 28 * 28 * 2);
    }
    kvblock(0); kvblock(1);
  }
  unsigned epoch, key0, key1, keyk;
  asm volatile("s_load_dword %0, %1, 0x0" : "=s"(epoch) : "s"(p.ws));
  const rsrc_t rs_ws = __builtin_amdgcn_make_buffer_rsrc(p.ws, 0, W_END, 0x00020000);
  const v4u srdW = mk_srd(p.ws, W_END);
  unsigned vl16 = l * 16;
  asm volatile("" : "+v"(vl16));
  const int ft = 7 * (h & 3) + min(w_, 6), nx = 2 * ft + (g >> 1);
  const unsigned pubx = w_ == 7 ? OOB : (g & 1) * 14336 + (nx >> 2) * 1024 + ((nx & 3) * 16 + c) * 16;
  const unsigned tw = (t * 8 + w_) * 14336, tp = (t * 8 + 2 * (h >> 2)) * 14336;
  const unsigned ptA = W_PT + h * 86016 + w_ * 2048;
  int budget = 1 << 16;
  v4u wphi[2];
  v4u park[8];
  v4u xf[14];
  v4f k0, k1, v0, v1, ck0, ck1, cv0, cv1;
  float var;
  v4f res0, res1;
  auto hint_put = [&](int slot, int ahead = 0) INL {
    __builtin_amdgcn_raw_buffer_store_b64(v2u{clock32() + ahead, key0}, rs_ws, vl16 < 256 ? vl16 >> 1 : OOB, W_HINT + slot * 32768 + (t * 16 + h) * 128, SC1);
  };
  auto hint_get = [&](unsigned v, unsigned so) INL { SB(); const v2u r = __builtin_amdgcn_raw_buffer_load_b64(rs_ws, v, so, SC1); SB(); return r; };
  const unsigned q4 = (l & 15) >> 2, p4 = l & 3;
  const lptr l16 = (lptr)(size_t)vl16, lq16 = lbase(L_WQ + l * 16);
  const lptr stw = lbase(L_SCR + c * 32 + ((g ^ (c >> 2)) * 8));
  const lptr str = lbase(L_SCR + (4 * g + q4) * 32 + ((p4 ^ g) * 8));
  const lptr sl16 = lbase(L_SCR + l * 16), csg = lbase(L_CS + g * 16);
  const unsigned vcol = l < 16 ? l * 2048 : OOB, vrow = l < 16 ? l * 128 : OOB;
  const float sel3 = g3 ? 1.f : 0.f;
  v4u hwt = weights(l);
  if (g3) { hwt.z = 0u; hwt.w = 0u; }
  const v4u zu = {0u, 0u, 0u, 0u};

  v4u wof[14];
  const unsigned wos = W_WO + ft * 14336;
  auto kv_gemm = [&](const int s0, const int s1, auto step) INL {
    const v4f z4 = {0.f, 0.f, 0.f, 0.f};
    if (s0 == 0) { k0 = z4; k1 = z4; v0 = z4; v1 = z4; }
    v4u a[8];
#pragma unroll
    for (int i = 0; i < 2; ++i) { const int o = (s0 + i) * 2048; a[4 * i] = lrd(l16 + o); a[4 * i + 1] = lrd(l16 + o + 1024); a[4 * i + 2] = lrd(l16 + L_WV + o); a[4 * i + 3] = lrd(l16 + L_WV + o + 1024); SB(); }
#pragma unroll
    for (int s = s0; s < s1; ++s) {
      const int i = 4 * (s & 1), o = (s + 2) * 2048;
      if (s < s1 - 1) LGKNOWN(4); else LGKNOWN(0);
      SB();
      k1 = mfma(a[i + 1], xf[s], k1); SB();
      step(s);
      if (s + 2 < s1) { a[i + 1] = lrd(l16 + o + 1024); SB(); }
      k0 = mfma(a[i], xf[s], k0); SB(); if (s + 2 < s1) { a[i] = lrd(l16 + o); SB(); }
      v0 = mfma(a[i + 2], xf[s], v0); SB(); if (s + 2 < s1) { a[i + 2] = lrd(l16 + L_WV + o); SB(); }
      v1 = mfma(a[i + 3], xf[s], v1); SB(); if (s + 2 < s1) { a[i + 3] = lrd(l16 + L_WV + o + 1024); SB(); }
    }
  };
  v4u hwk = weights(l);
  hwk.w &= 0xffffu;
  asm volatile("" : "+v"(hwk.z), "+v"(hwk.w), "+v"(hwt));
  v4u fl0, fl1;
  unsigned vres0;
  {
    unsigned a0 = (8 * g + (c >> 2)) * 64 + ((((l & 3) >> 1) ^ ((g & 1) << 1)) * 16) + (l & 1) * 8;
    unsigned sl3 = c >= 12 ? 4 * c - 45 : c + ((c * 11) >> 5);
    const unsigned one = 0x3f803f80u;
    v4u one8 = {one, one, one, one};
    const v4f z4 = {0.f, 0.f, 0.f, 0.f};
    auto turn = [&](lptr r0, lptr r1, bool pad, v4u& f0, v4u& f1) INL {
      f0 = cat(trd(r0), trd(r0 + 256)); f1 = cat(trd(r1), trd(r1 + 256));
      f1 = v4u{pad ? one : f1.x, pad ? one : f1.y, pad ? one : f1.z, pad ? one : f1.w};
    };
    const unsigned xa = xsb + c * 128 + (g ^ (c >> 1)) * 16;
    const lptr xa0 = lbase(xa), xa1 = lbase(xa ^ 64), cp = lbase(L_CSP + g * 16);
    unsigned vres = (t * 128 + (2 * (h >> 2) + (g & 1)) * 16 + c) * (DM * 2) + nx * 16;
    unsigned pubx_ = pubx, vcol_ = vcol, vrow_ = vrow;
    float dg0 = c == 4 * g ? 1.f : 0.f, dg1 = c == 4 * g + 1 ? 1.f : 0.f, dg2 = c == 4 * g + 2 ? 1.f : 0.f, dg3 = c == 4 * g + 3 ? 1.f : 0.f;
    asm volatile("" : "+v"(vres), "+v"(a0), "+v"(sl3), "+v"(one8), "+v"(dg0), "+v"(dg1), "+v"(dg2), "+v"(dg3) : "v"(pubx_), "v"(vcol_), "v"(vrow_), "v"(sel3));
    {
      const bool spread = w_ == 7 || (w_ < 4 && w_ != 2);
      const unsigned slot = spread ? sl3 : c;
      const lptr jr0 = lbase(jat + a0), jr1 = lbase(jat + (a0 ^ 32));
      unsigned vj1 = ((w_ & 6) == 4 ? 14336 : 1024) + (vl16 & 768) + slot * 16, vcs = g == 0 ? c * 4 : g == 1 ? 64 + slot * 4 : OOB;
      unsigned dst = !job ? W_END : w_ < 4 ? W_PT + ((w_ ? 16 : 0) + h) * 86016 + ((w_ ? w_ - 1 : 2) * 14 + t) * 2048 : W_WO + ((w_ - 4) * 28 + 2 * h) * 14336 + t * 1024;
      unsigned dcs = (job && w_ < 4) ? W_CS + (h * 56 + w_ * 14 + t) * 128 : W_END;
      asm volatile("" : "+v"(vj1), "+v"(vcs));
      asm volatile("" : "+s"(dst), "+s"(dcs));
      STAMP(51);
      if (w_ < 4) { xline(1, 0); xline(1, 1); } else { kvblock(2); kvblock(3); }
      asm volatile("s_waitcnt lgkmcnt(0)" : "+s"(epoch) :: "memory");
      epoch += 1u;
      keys(epoch, key0, key1);
      keyk = ((vl16 & 512) ? key1 : key0) >> ((vl16 & 256) ? 14 : 0);
      asm volatile("" : "+v"(keyk));
      VMWAIT(8);
      STAMP(47);
      v4u f0, f1;
      turn(jr0, jr1, spread && c >= 12, f0, f1);
      if (w_ == 7) { lwr(lq16 + 7 * 2048, f0); lwr(lbase(jat + vj1), f1); }
      __builtin_amdgcn_raw_buffer_store_b128(f0, rs_ws, vl16, dst, SC1);
      __builtin_amdgcn_raw_buffer_store_b128(f1, rs_ws, vj1, dst, SC1);
      keep(f0, f1);
      const v4f c0 = mfma(one8, f0, z4), c1 = mfma(one8, f1, z4);
      __builtin_amdgcn_raw_buffer_store_b32(asu(g ? c1.x : c0.x), rs_ws, vcs, dcs, SC1);
      fence();
      STAMP(52);
    }
    if (w_ >= 4) {
      const unsigned kslot = kvp_ ? c : sl3;
      const lptr kr0 = lbase(kvat + a0), kr1 = lbase(kvat + (a0 ^ 32)), kw0 = lbase(kvat + vl16), kw1 = lbase(kvat + 1024 + (vl16 & 768) + kslot * 16);
      const lptr cw0 = lbase(L_CSP + (w_ & 3) * 128 + c * 4), cw1 = lbase(L_CSP + (w_ & 3) * 128 + 64 + kslot * 4);
      const bool kpad = !kvp_ && c >= 12;
      v4f cs0 = z4, cs1 = z4;
#pragma unroll
      for (int j = 0; j < 7; ++j) {
        if (j + 4 < 7) kvblock(j + 4);
        if (j < 3) VMWAIT(11); else if (j == 3) VMWAIT(9); else if (j == 4) VMWAIT(4); else if (j == 5) VMWAIT(2); else VMWAIT(0);
        v4u f0, f1;
        turn(kr0 + j * 4096, kr1 + j * 4096, kpad, f0, f1);
        lwr(kw0 + j * 4096, f0); lwr(kw1 + j * 4096, f1);
        cs0 = mfma(one8, f0, cs0); cs1 = mfma(one8, f1, cs1);
      }
      *(LAS float*)(cw0) = cs0.x; *(LAS float*)(cw1) = cs1.x;
    } else {
      __builtin_amdgcn_s_setprio(0);
#pragma unroll
      for (int j = 2; j < 5; ++j) { xline(j, 0); xline(j, 1); }
      drain();
    }
    STAMP(48);
    wg_bar();
    STAMP(49);
#pragma unroll
    for (int j = 0; j < 2; ++j) { xf[2 * j] = lrd(xa0 + j * 2048); xf[2 * j + 1] = lrd(xa1 + j * 2048); }
    if (w_ == 6) __builtin_amdgcn_raw_buffer_store_b64(v2u{key0, key1}, rs_ws, 0, W_FLAG + b * 8, SC1);
    asm volatile("s_waitcnt lgkmcnt(0)" : "+v"(xf[0]), "+v"(xf[1]), "+v"(xf[2]), "+v"(xf[3]) :: "memory");
    xline(5, 0); xline(6, 0);
    v4f gram = z4, pa, pb;
    kv_gemm(0, 10, [&](int s) INL {
      gram = mfma(xf[s], xf[s], gram);
      if (s < 3) { xf[4 + 2 * s] = lrd(xa0 + (2 + s) * 2048); xf[5 + 2 * s] = lrd(xa1 + (2 + s) * 2048); }
      if (s >= 4 && s < 8) { const v4f u = pa + pb; if (s == 4) ck0 = u; else if (s == 5) ck1 = u; else if (s == 6) cv0 = u; else cv1 = u; }
      if (s >= 3 && s < 7) {
        const int o = ((s - 3) >> 1) * 256 + ((s - 3) & 1) * 64;
        pa = *(LAS v4f*)(cp + o); pb = *(LAS v4f*)(cp + o + 128);
      }
      if (s == 7) { wphi[0] = lrd(lq16 + 7 * 2048); wphi[1] = lrd(lq16 + 7 * 2048 + 1024); }
      if (s == 8) drain();
      if (s >= 8) { xf[2 * s - 6] = lrd(xa0 + (s - 8) * 2048); xf[2 * s - 5] = lrd(xa1 + (s - 8) * 2048); }
      SB();
    });
    STAMP(50);
    if (w_ == 7) {
      do {
        asm volatile("s_nop 4\n\tbuffer_load_dwordx4 %0, %2, %3, %4 offen sc1\n\tbuffer_load_dwordx4 %1, %2, %3, %4 offen offset:1024 sc1\n\ts_waitcnt vmcnt(0)"
                     : "=&v"(fl0), "=&v"(fl1) : "v"(vl16), "s"(srdW), "s"(W_FLAG) : "memory");
      } while (retry(fl0.x != key0 || fl0.z != key0 || fl1.x != key0 || fl1.z != key0 || fl0.y != key1 || fl0.w != key1 || fl1.y != key1 || fl1.w != key1, budget));
    }
    if (w_ == 6) { *(LAS v4f*)(csg) = ck0; *(LAS v4f*)(csg + 64) = ck1; *(LAS v4f*)(csg + 128) = cv0; *(LAS v4f*)(csg + 192) = cv1; }
    wg_bar();
    STAMP(54);
    kv_gemm(10, 14, [&](int s) INL {
      if (s < 12) { gram = mfma(xf[2 * s - 10], xf[2 * s - 10], gram); gram = mfma(xf[2 * s - 9], xf[2 * s - 9], gram); }
      if (s == 10) dma2s(L_WQ + w_ * 2048, vl16, srdW, ptA + L_WQ);
      if (s == 12 && w_ < 6) dma2s(L_WQ + 16384 + w_ * 2048, vl16, srdW, ptA + L_WQ + 16384);
      if (s == 13) var = xrow(1.f / DM, (gram.x * dg0 + gram.y * dg1) + (gram.z * dg2 + gram.w * dg3));
      SB();
    });
    vres0 = vres;
  }
  STAMP(56);
  int xwait;
  v4u kc = {key0, 0u, key1, 0u};
  PIN(kc);
  auto run_layer = [&](auto kind, auto role) INL {
    constexpr int layer = decltype(kind)::value, rep = layer >> 1;
    constexpr bool isA = !(layer & 1), first = layer == 0, more = layer < 3, lead = decltype(role)::value;
    int w = w_;
    asm volatile("" : "+s"(w));
    __builtin_assume(lead ? (w >= 4 && w < 8) : (w >= 0 && w < 4));
    const v4f z4 = {0.f, 0.f, 0.f, 0.f};
    const unsigned wob = (lead && w == 7) ? W_END - 12288 : wos + (isA ? 0 : 28 * 14336), wov = (lead && w == 7) ? OOB : vl16;
    auto load_wo = [&](int s0, int s1) INL {
#pragma unroll
      for (int s = s0; s < s1; ++s) wof[s] = ld16(rs_ws, wov + (s & 3) * 1024, wob + (s >> 2) * 4096);
    };
    v2u hh;
    unsigned vh;
    const unsigned shh = W_HINT + 2 * layer * 32768 + (t - 2) * 2048 + h * 128;
    if (!isA) { const unsigned lo = lane_id(); vh = lo >= 16 ? OOB : (lo < 2 && (int)lo < t) ? (1 - lo) * 2048 : 4096; }
    const lptr cs = csg + (isA ? 0 : 384);
    float rstd, nmr;
    auto stats = [&]() INL {
      const float mean = k1.w * (1.f / DM);
      rstd = __builtin_amdgcn_rsqf(mx(fmaf(-mean, mean, var), 0.f) + 1e-5f); nmr = -mean * rstd;
      PIN(rstd); PIN(nmr);
    };
    unsigned hp = t < 15 ? W_HALO + rep * 4194304 + h * 262144 + (t * 8 + w) * 2048 : W_END;
    if (!isA) asm volatile("" : "+s"(hp));
    if (!first) {
      unsigned xs[4], sts = W_ST + layer * 131072 + (t * 8 + w) * 1024;
#pragma unroll
      for (int i = 0; i < 4; ++i) { xs[i] = W_X + layer * TILEBUF + tw + i * 4096; asm volatile("" : "+s"(xs[i])); }
      asm volatile("" : "+s"(sts));
      if (!isA) PIN(vh);
      pause(xwait);
      auto ldx = [&](int s) INL { xf[s] = ld16(rs_ws, vl16 + (s & 3) * 1024, xs[s >> 2]); };
      for (;;) {
#pragma unroll
        for (int s = 0; s < WIN; ++s) ldx(s);
        STAMP(2 + layer * 12);
        v4u st;
        unsigned c0 = 0u - key0, c1 = 0u - key1;
        kv_gemm(0, 14, [&](int s) INL {
          const int n0 = WIN + s + (s < RAMP ? s : RAMP), n1 = WIN + s + 1 + (s + 1 < RAMP ? s + 1 : RAMP);
#pragma unroll
          for (int n = n0; n < n1; ++n) {
            if (n < 14) ldx(n);
            if (n == 14) st = ld16(rs_ws, vl16, sts);
            if (n == 15 && !isA) hh = hint_get(vh, shh);
          }
          c0 += xf[s].x + xf[s].z; c1 += xf[s].y + xf[s].w; PIN(c0); PIN(c1);
          if (s == 13) { var = xrow(1.f / DM, asf(st.x)); c0 += st.x + st.y; c1 += st.z; PIN(c0); PIN(c1); }
          if (s == 12) { ck0 = *(LAS v4f*)(cs); ck1 = *(LAS v4f*)(cs + 64); cv0 = *(LAS v4f*)(cs + 128); cv1 = *(LAS v4f*)(cs + 192); }
          SB();
        });
        c0 = xsum2u(c0, c1);
        stats();
        if (__builtin_expect(!nonzero(c0), 1) || !spend(budget)) break;
        __builtin_amdgcn_s_sleep(4);
      }
    }
    STAMP(3 + layer * 12);
    if (first) stats();
    auto ln7 = [&](v4f a0, v4f a1, v4f c0, v4f c1, float sc, float sh, float pad) INL {
      a0 = a0 * sc + c0 * sh;
      const v2f u = v2f{a1.x, a1.y} * sc + v2f{c1.x, c1.y} * sh;
      return v4u{pk(a0.x, a0.y), pk(a0.z, a0.w), pk(u.x, u.y), pk(fmaf(a1.z, sc, c1.z * sh), pad)};
    };
    auto q_gemm = [&](float sc, float pad) INL {
      v4f q0 = z4, q1 = z4, c0, c1;
      const lptr lq = lq16 + (isA ? 0 : L_WQB - L_WQ);
      v4u a[8];
#pragma unroll
      for (int i = 0; i < 8; ++i) { a[i] = lrd(lq + i * 1024); SB(); }
#pragma unroll
      for (int s = 0; s < 14; ++s) {
        const int i = (2 * s) & 7;
        if (!(s & 1)) { if (s < 12) LGKNOWN(4); else LGKNOWN(0); SB(); }
        q0 = mfma(a[i], xf[s], q0); SB(); if (s + 4 < 14) { a[i] = lrd(lq + (s + 4) * 2048); SB(); }
        q1 = mfma(a[i + 1], xf[s], q1); SB(); if (s + 4 < 14) { a[i + 1] = lrd(lq + (s + 4) * 2048 + 1024); SB(); }
        if (s == 12) { c0 = *(LAS v4f*)(cs + 256); c1 = *(LAS v4f*)(cs + 320); SB(); }
      }
      v4u r = ln7(q0, q1, c0, c1, rstd * sc, nmr * sc, pad);
      asm volatile("" : "+v"(r) :: "memory");
      return r;
    };
    constexpr float L2E = 1.4426950408889634f;
    const v4u kf = isA ? ln7(k0, k1, ck0, ck1, rstd * L2E, nmr * L2E, 0.f) : ln7(k0, k1, ck0, ck1, rstd, nmr, 0.f);
    auto v_pack = [&]() INL {
      const v4f a0 = v0 * rstd + cv0 * nmr, a1 = v1 * rstd + cv1 * nmr;
      return v4u{pk(a0.x, a0.y), pk(a0.z, a0.w), g3 ? 0x3f80u : pk(a1.x, a1.y), g3 ? 0u : pk(a1.z, a1.w)};
    };
    v4f o0, o1;
    float den;
    const unsigned soh = W_HINT + (2 * layer + 1) * 32768 + t * 2048;
    v2u oh;

    if constexpr (isA) {
      if (lead && w == 7) hint_put(2 * layer);
      if (!first) { if (!lead) load_wo(6, 10); }
      else if (lead) { fl0 = ld16(rs_ws, vl16, W_CS + (h * 56 + 14 * (w & 3)) * 128); fl1 = ld16(rs_ws, vl16 < 768 ? vl16 + 1024 : OOB, W_CS + (h * 56 + 14 * (w & 3)) * 128); }
      const v4u vf = v_pack();
      v4u pp, pm, qf;
      feature_map(kf, wphi, pp, pm, [&]() INL { SB(); if (first) load_wo(0, 4); else if (!lead) load_wo(10, 14); });
      {
        const lptr tb = stw + w * 3072;
        *(LAS v2u*)(tb) = v2u{pp.x, pp.y}; *(LAS v2u*)(tb + 512) = v2u{pp.z, pp.w};
        *(LAS v2u*)(tb + 1024) = v2u{pm.x, pm.y}; *(LAS v2u*)(tb + 1536) = v2u{pm.z, pm.w};
        *(LAS v2u*)(tb + 2048) = v2u{vf.x, vf.y}; *(LAS v2u*)(tb + 2560) = v2u{vf.z, vf.w};
      }
      if (first) {
        VMWAIT(4);
        if (lead) {
          v4f cq = __builtin_bit_cast(v4f, fl0) + __builtin_bit_cast(v4f, fl1);
#pragma unroll
          for (int i = 0; i < 4; ++i) {
            float u = cq[i];
            u += asf(__builtin_amdgcn_update_dpp(0, asu(u), 0x128, 0xf, 0xf, false));
            cq[i] = xrow(1.f, u);
          }
          *(LAS v4f*)(lbase(L_CS + 256 + (w & 3) * 128 + (vl16 & 127))) = cq;
        }
      }
      v4u hwk_ = {hwt.x, hwt.y, hwk.z, hwk.w};
      unsigned kvp = W_KVP + rep * 1048576 + h * 65536 + (w & 3) * 1024 + (lead ? t * 4096 : 0);
      const lptr pa = lbase((size_t)str + (w & 2) * 512), pb = lbase((size_t)str + 2048 + (w & 1) * 512);
      asm volatile("" : "+v"(hwk_));
      asm volatile("" : "+s"(kvp));
      wg_bar();
      STAMP(4 + layer * 12);
      auto residual = [&]() INL {
        park[0] = __builtin_amdgcn_raw_buffer_load_b128(__builtin_amdgcn_make_buffer_rsrc((void*)p.x, 0, SEQ * DM * 2, 0x00020000), vres0, 0, 0); SB();
      };
      if (lead) {
        __builtin_amdgcn_s_setprio(2);
        v4u bv[4], x0[4], x1[4];
#pragma unroll
        for (int kk = 0; kk < 4; ++kk) {
          bv[kk] = cat(trd(pb + kk * 6144), trd(pb + kk * 6144 + 3072));
          x0[kk] = cat(trd(pa + kk * 6144), trd(pa + kk * 6144 + 3072));
          x1[kk] = cat(trd(pa + kk * 6144 + 512), trd(pa + kk * 6144 + 3072 + 512));
          SB();
        }
        v4f d0 = z4, d1 = z4;
#pragma unroll
        for (int kk = 0; kk < 4; ++kk) { d0 = mfma(x0[kk], bv[kk], d0); d1 = mfma(x1[kk], bv[kk], d1); }
        v4u dv = {pk(d0.x, d0.y), pk(d0.z, d0.w), pk(d1.x, d1.y), pk(d1.z, 0.f)};
        const v4u hs = imma(hwk_, dv, zu);
        dv.w |= ((hs.x + keyk) & 0x3fffu) << 16;
        __builtin_amdgcn_raw_buffer_store_b128(dv, rs_ws, vl16, kvp, SC1);
        keep(dv);
        __builtin_amdgcn_s_setprio(0);
        STAMP(5 + layer * 12);
        if (first) { load_wo(4, 9); residual(); } else load_wo(6, 14);
        qf = q_gemm(L2E, 0.f);
        feature_map(qf, wphi, pp, pm, []() {});
        PIN(pp); PIN(pm);
        if (first) {
          asm volatile("" : "+v"(pp), "+v"(pm) :: "memory");
          load_wo(9, 14);
#pragma unroll
          for (int j = 0; j < 4; ++j) if (j < 3 || w < 6) dma2s(L_WQB + (w & 3) * 2048 + j * 8192, vl16, srdW, ptA - 8192 + 16 * 86016 + L_WQ + j * 8192);
        }
      } else {
        const unsigned sh = W_HINT + 2 * layer * 32768 + h * 128;
        __builtin_amdgcn_s_setprio(1);
        __builtin_amdgcn_s_sleep(DLY_T);
        const v2u hv = hint_get(vcol, sh);
        if (first) { load_wo(4, 6); residual(); }
        qf = q_gemm(L2E, 0.f);
        pause(lateness(hv, t, 150, key0, srdW, vcol, sh, budget) + T_KV);
        if (w == 0) hint_put(2 * layer + 1);
        fence();
        STAMP(6 + layer * 12);
        v4u kp[16];
#pragma unroll
        for (int i = 0; i < 8; ++i) kp[i] = ld16(rs_ws, vl16, kvp + i * 4096);
        feature_map(qf, wphi, pp, pm, [&]() INL {
          SB();
#pragma unroll
          for (int i = 8; i < 16; ++i) kp[i] = ld16(rs_ws, vl16, kvp + i * 4096);
        });
        PIN(pp); PIN(pm);
        const unsigned lo = lane_id();
        const unsigned bit = ((lo >> 4) == ((lo >> 2) & 3)) ? ((lo & 1) ? 0x3f800000u : 0x3f80u) : 0u;
        const unsigned u0 = (lo & 2) ? 0u : bit, u1 = (lo & 2) ? bit : 0u;
        const v4u sa = {u0, u1, 0u, 0u}, sb = {0u, 0u, u0, u1};
        const lptr kd = lbase((size_t)sl16 + (L_KVF - L_SCR) + w * 1024);
        auto add_up = [&](bool once) INL {
          v4f lo4 = z4, hi4 = z4;
          v4u hs = zu;
          unsigned ck = 0u - 16u * keyk;
          SB();
#pragma unroll
          for (int i = 0; i < 16; ++i) {
            hs = imma(hwk_, kp[i], hs); lo4 = mfma(sa, kp[i], lo4); hi4 = mfma(sb, kp[i], hi4);
            ck += kp[i].w >> 16; PIN(ck);
            SB();
            if (first && once && i < 8) load_wo(6 + i, 7 + i);
          }
          const bool bad = nonzero((hs.x - ck) & 0x3fffu);
          SB();
          lwr(kd, v4u{pk(lo4.x, lo4.y), pk(lo4.z, lo4.w), pk(hi4.x, hi4.y), pk(hi4.z, 0.f)});
          return bad;
        };
        if (__builtin_expect(add_up(true), 0)) {
          do {
#pragma unroll
            for (int i = 0; i < 16; i += 4) reload4(kp[i], kp[i + 1], kp[i + 2], kp[i + 3], srdW, vl16, kvp + i * 4096, kvp + (i + 1) * 4096, kvp + (i + 2) * 4096, kvp + (i + 3) * 4096);
          } while (add_up(false) && spend(budget));
        }
      }
      __builtin_amdgcn_s_setprio(0);
      wg_bar();
      STAMP(7 + layer * 12);
      v4u kv[4];
      kv[0] = lrd(sl16 + (L_KVF - L_SCR) + 1024); SB(); kv[1] = lrd(sl16 + (L_KVF - L_SCR) + 3072); SB();
      kv[2] = lrd(sl16 + (L_KVF - L_SCR)); SB(); kv[3] = lrd(sl16 + (L_KVF - L_SCR) + 2048);
      SB();
      if (lead) oh = hint_get(vrow, w == 7 ? soh : W_END);
      o1 = mfma(kv[0], pp, z4); o1 = mfma(kv[1], pm, o1); SB();
      o0 = mfma(kv[2], pp, z4); o0 = mfma(kv[3], pm, o0); SB();
      { const v4f e6 = {1e-6f, 1e-6f, 1e-6f, 1e-6f}, r = __builtin_amdgcn_mfma_f32_16x16x4f32(sel3, o1.x, e6, 0, 0, 0); den = r.x; }
    } else {
      const unsigned hb = W_HALO + rep * 4194304 + h * 262144;
      __builtin_amdgcn_raw_buffer_store_b128(kf, rs_ws, vl16, hp, SC1); SB();
      const v4u hsk = imma(hwt, kf, v4u{kf.z + key0, 0u, kf.w + key1, 0u}); SB();
      v4u vf = v_pack();
      const v4u hs_v = imma(hwt, vf, hsk); SB();
      const lptr kw = sl16 + w * 2048, vw = stw + w * 2048, vr = str + w * 2048;
      *(LAS v2u*)(vw + 32768 + 1024) = v2u{vf.x, vf.y}; *(LAS v2u*)(vw + 32768 + 1536) = v2u{vf.z, vf.w};
      if (!lead || w < 7) lwr(kw + 32768, kf);
      SB();
      vf.z = g3 ? (hs_v.y << 13) + hs_v.x : vf.z; vf.w = g3 ? (hs_v.w << 13) + hs_v.z : vf.w;
      __builtin_amdgcn_raw_buffer_store_b128(vf, rs_ws, vl16 + 1024, hp, SC1); keep(vf);
      STAMP(4 + layer * 12);
      v4u hk[2], hv[2];
      unsigned hs[2];
#pragma unroll
      for (int i = 0; i < 2; ++i) hs[i] = t - 2 + i >= 0 ? hb + ((t - 2 + i) * 8 + w) * 2048 : W_END;
      auto fetch = [&](int margin) INL {
        pause(lateness(hh, 2, 200, key0, srdW, vh, shh, budget) + margin);
#pragma unroll
        for (int i = 0; i < 2; ++i) { hk[i] = ld16(rs_ws, vl16, hs[i]); hv[i] = ld16(rs_ws, vl16 + 1024, hs[i]); }
      };
      if (lead) load_wo(6, 10);
      wg_bar();
      if (!lead) { fetch(T_H); __builtin_amdgcn_s_setprio(1); }
      const v4u qf = q_gemm(0.18898223650461357f * L2E, 1.f);
      if (lead) { fetch(T_HL); load_wo(10, 14); } else load_wo(6, 10);
      const float NEG = -3.0e38f;
      v4f sd = mfma(kf, qf, z4);
#pragma unroll
      for (int r = 0; r < 4; ++r) sd[r] = (4 * g + r <= c) ? sd[r] : NEG;
      float m = xmax1(mx(mx3(sd.x, sd.y, sd.z), sd.w));
      auto diag = [&]() INL {
        const v2u a0 = trd(vr + 32768 + 1024), a1 = trd(vr + 32768 + 1536);
        const v4f e = sd - m;
        const v2u pd = {pk(ex2(e.x), ex2(e.y)), pk(ex2(e.z), ex2(e.w))};
        typedef short v4h __attribute__((ext_vector_type(4)));
        o0 = __builtin_amdgcn_mfma_f32_16x16x16bf16_1k(__builtin_bit_cast(v4h, a0), __builtin_bit_cast(v4h, pd), z4, 0, 0, 0);
        o1 = __builtin_amdgcn_mfma_f32_16x16x16bf16_1k(__builtin_bit_cast(v4h, a1), __builtin_bit_cast(v4h, pd), z4, 0, 0, 0);
      };
      diag();
      auto take_halo = [&]() INL {
        STAMP(6 + layer * 12);
        const unsigned nv = min(t, 2);
        auto stage = [&]() INL {
          v4u hs_ = zu;
          unsigned e0 = 0u - nv * key0, e1 = 0u - nv * key1;
#pragma unroll
          for (int i = 0; i < 2; ++i) {
            const unsigned none = t - 2 + i < 0 ? 0xfe000000u : 0u;
            hs_ = imma(hwt, hk[i], hs_); SB();
            hs_ = imma(hwt, hv[i], hs_); SB();
            e0 += hv[i].z - hk[i].z; e1 += hv[i].w - hk[i].w; PIN(e0); PIN(e1);
            lwr(kw + i * 16384, v4u{hk[i].x, hk[i].y, hk[i].z, hk[i].w | none});
            *(LAS v2u*)(vw + i * 16384 + 1024) = v2u{hv[i].x, hv[i].y};
            *(LAS v2u*)(vw + i * 16384 + 1536) = v2u{g3 ? 0x3f80u : hv[i].z, g3 ? 0u : hv[i].w};
            SB();
          }
          return row3_differs(hs_, e0, e1);
        };
        if (__builtin_expect(stage(), 0)) {
          do reload4(hk[0], hk[1], hv[0], hv[1], srdW, vl16, hs[0], hs[1], min(hs[0] + 1024, W_END), min(hs[1] + 1024, W_END));
          while (stage() && spend(budget));
        }
        wg_bar();
        STAMP(7 + layer * 12);
      };
      {
        const v4f cm = {-m, -m, -m, -m};
        v4f sa, sb; v4u a0, a1, b0, b1, pq;
#pragma unroll
        for (int j = 7; j >= 0; --j) {
          if (j >= 4 && (j < 6) == lead && (w >> 1) == 7 - j) take_halo();
          if (j == (lead ? 4 : 6)) VMKNOWN(4);
          if (!lead && j == 6 && w == 3) hint_put(2 * layer + 1);
          if (!lead && j <= 5 && j >= 4) load_wo(10 + 2 * (5 - j), 12 + 2 * (5 - j));
          if (!lead && j == POLL_OB) oh = hint_get(vrow, w == 3 ? soh : W_END);
          const v4u ka = lrd(kw + j * 4096), kb = lrd(kw + j * 4096 + 2048);
          const v4u n0 = cat(trd(vr + j * 4096 + 1024), trd(vr + j * 4096 + 3072)), n1 = cat(trd(vr + j * 4096 + 1536), trd(vr + j * 4096 + 3584));
          SB();
          if (j < 7) {
            const float e0 = ex2(sa.x), e1 = ex2(sa.y); SB();
            if (j < 6) { o0 = mfma(b0, pq, o0); SB(); }
            const float e2 = ex2(sa.z), e3 = ex2(sa.w); SB();
            if (j < 6) { o1 = mfma(b1, pq, o1); SB(); }
            const float e4 = ex2(sb.x), e5 = ex2(sb.y); SB();
            sa = mfma(ka, qf, cm); SB();
            const float e6 = ex2(sb.z), e7 = ex2(sb.w); SB();
            sb = mfma(kb, qf, cm); SB();
            pq = v4u{pk(e0, e1), pk(e2, e3), pk(e4, e5), pk(e6, e7)};
            b0 = a0; b1 = a1;
          } else {
            sa = mfma(ka, qf, cm); sb = mfma(kb, qf, cm);
          }
          a0 = n0; a1 = n1;
          SB();
        }
        o0 = mfma(b0, pq, o0); o1 = mfma(b1, pq, o1); SB();
#pragma unroll
        for (int r = 0; r < 4; ++r) sa[r] = (4 * g + r > c) ? sa[r] : NEG;
        const v4u pf = pexp(sa, sb);
        o1 = mfma(a1, pf, o1); o0 = mfma(a0, pf, o0);
      }
      den = xrow(sel3, o1.x);
      if (__builtin_expect(__any(!(den < 1.0e18f)), 0)) {
        float mt = m;
#pragma unroll 1
        for (int k = 0; k < 16; ++k) {
          v4f s = mfma(lrd(kw + k * 2048), qf, z4);
#pragma unroll
          for (int r = 0; r < 4; ++r) s[r] = (k > 0 || 4 * g + r > c) ? s[r] : NEG;
          mt = mx3(mt, mx(s.x, s.y), mx(s.z, s.w));
        }
        m = xmax1(mt);
        diag();
        const v4f cm = {-m, -m, -m, -m};
#pragma unroll 1
        for (int j = 7; j >= 0; --j) {
          v4f sa = mfma(lrd(kw + j * 4096), qf, cm);
          const v4f sb = mfma(lrd(kw + j * 4096 + 2048), qf, cm);
#pragma unroll
          for (int r = 0; r < 4; ++r) sa[r] = (j > 0 || 4 * g + r > c) ? sa[r] : NEG;
          const v4u pf = pexp(sa, sb);
          o0 = mfma(cat(trd(vr + j * 4096 + 1024), trd(vr + j * 4096 + 3072)), pf, o0);
          o1 = mfma(cat(trd(vr + j * 4096 + 1536), trd(vr + j * 4096 + 3584)), pf, o1);
        }
        den = xrow(sel3, o1.x);
      }
    }
    const unsigned ob = W_O + layer * 2097152 + t * 131072;
    {
      const float rd = __builtin_amdgcn_rcpf(den);
      o0 *= rd; o1 *= rd;
      v4u v = {pk(o0.x, o0.y), pk(o0.z, o0.w), pk(o1.x, o1.y), pk(o1.z, o1.w)};
      const v4u hs = imma(hwt, v, kc);
      v.z = g3 ? (hs.y << 13) + hs.x : v.z; v.w = g3 ? (hs.w << 13) + hs.z : v.w;
      __builtin_amdgcn_raw_buffer_store_b128(v, rs_ws, vl16, ob + (w * 16 + h) * 1024, SC1);
      keep(v);
    }
    __builtin_amdgcn_s_setprio(0);
    STAMP(8 + layer * 12);
    auto swap_kv = [&](int j0, int j1) INL {
#pragma unroll
      for (int j = j0; j < j1 && j < (lead ? 3 : 4); ++j) {
        const lptr a = lbase(w * 2048 + j * 16384 + vl16);
        v4u t0, t1;
        if (layer < 2) { t0 = lrd(a); t1 = lrd(a + 1024); }
        if (!first) { lwr(a, park[2 * j]); lwr(a + 1024, park[2 * j + 1]); }
        if (layer < 2) { park[2 * j] = t0; park[2 * j + 1] = t1; }
      }
      if (first) {
        asm volatile("s_waitcnt lgkmcnt(0)" ::: "memory");
#pragma unroll
        for (int j = j0; j < j1 && j < (lead ? 3 : 4); ++j) dma2s(w * 2048 + j * 16384, vl16, srdW, ptA + 16 * 86016 + j * 16384);
      }
    };
    if (first) {
      const v4u xr = swap16(park[0]);
      res0 = v4f{asf(xr.x << 16), asf(xr.x & 0xffff0000u), asf(xr.y << 16), asf(xr.y & 0xffff0000u)};
      res1 = v4f{asf(xr.z << 16), asf(xr.z & 0xffff0000u), asf(xr.w << 16), asf(xr.w & 0xffff0000u)};
    }
    unsigned op = ob + (2 * (h >> 2) + (lead ? 0 : 1)) * 16384 + (w & 3) * 1024;
    asm volatile("" : "+s"(op));
    v4u of[4];
    if (isA) swap_kv(0, 2);
    if (lead == isA && w == (isA ? 7 : 3)) *(LAS int*)(csg + 768) = lateness(oh, h, isA ? 165 : 125, key0, srdW, vrow, soh, budget);
    wg_bar();
    const int olate = __builtin_amdgcn_readfirstlane(*(LAS int*)(csg + 768));
    if (!isA && more) swap_kv(0, 2);
    pause(olate + (isA ? T_OA : T_OB) + (more ? 0 : 6));
#pragma unroll
    for (int i = 0; i < 4; ++i) of[i] = ld16(rs_ws, vl16, op + i * 4096);
    if (more && !first) swap_kv(2, 4);
    const unsigned qd = 7 * (w & 3) + (vl16 >> 8), qa = L_SCR + (lead ? 0 : 14336) + (qd >> 1) * 256 + (qd & 1) * 8 + (vl16 & 255);
    const lptr lo = lbase(qa), hi = lbase(g3 ? L_SCR + 32768 + vl16 : qa + 512);
    STAMP(9 + layer * 12);
    auto stage_o = [&]() INL {
      auto put = [&]() INL {
        v4u hs = zu;
        unsigned e0 = 0u - 4u * key0, e1 = 0u - 4u * key1;
#pragma unroll
        for (int i = 0; i < 4; ++i) {
          hs = imma(hwt, of[i], hs); SB();
          *(LAS v2u*)(lo + i * 3584) = v2u{of[i].x, of[i].y};
          *(LAS v2u*)(hi + i * 3584) = v2u{of[i].z, of[i].w};
          e0 += of[i].z; e1 += of[i].w; PIN(e0); PIN(e1);
          SB();
        }
        return row3_differs(hs, e0, e1);
      };
      if (__builtin_expect(put(), 0)) {
        do reload4(of[0], of[1], of[2], of[3], srdW, vl16, op, op + 4096, op + 8192, op + 12288);
        while (put() && spend(budget));
      }
    };
    stage_o();
    wg_bar();
    if (first) swap_kv(2, 4);
    const unsigned sx = W_HINT + (8 + layer) * 32768 + t * 2048;
    if (lead && w == 7) {
      if (more) {
        hint_put(8 + layer);
        __builtin_amdgcn_s_sleep(P_X);
        *(LAS int*)(csg + 832) = lateness(hint_get(vrow, sx), h, 70, key0, srdW, vrow, sx, budget);
      }
    }
    int w2 = w;
    asm volatile("" : "+s"(w2));
    if (!lead || w2 != 7) {
      const unsigned won = wos + (isA ? 28 * 14336 : 0);
      v4u bq[8];
#pragma unroll
      for (int i = 0; i < 4; ++i) { bq[2 * i] = lrd(sl16 + i * 1024); SB(); bq[2 * i + 1] = lrd(sl16 + 14336 + i * 1024); SB(); }
#pragma unroll
      for (int s = 0; s < 14; ++s) {
        const int i = (2 * s) & 7;
        if (!(s & 1)) { if (s < 12) LGKNOWN(4); else LGKNOWN(0); SB(); }
        res0 = mfma(wof[s], bq[i], res0); SB(); if (s + 4 < 14) { bq[i] = lrd(sl16 + (s + 4) * 1024); SB(); }
        res1 = mfma(wof[s], bq[i + 1], res1); SB(); if (s + 4 < 14) { bq[i + 1] = lrd(sl16 + 14336 + (s + 4) * 1024); SB(); }
        if (more && s >= 1 && (s - 1) % WOS == 0 && (s - 1) / WOS < 6) { const int n = (s - 1) / WOS; wof[n] = ld16(rs_ws, vl16 + (n & 3) * 1024, won + (n >> 2) * 4096); }
      }
      const v4u xo = swap16(v4u{pk(res0.x, res0.y), pk(res0.z, res0.w), pk(res1.x, res1.y), pk(res1.z, res1.w)});
      if (more) {
        __builtin_amdgcn_raw_buffer_store_b128(xo, rs_ws, pubx, W_X + (layer + 1) * TILEBUF + tp, SC1);
        keep(xo);
        auto r = __builtin_amdgcn_permlane32_swap(xo.x + xo.z, xo.y + xo.w, false, false);
        const unsigned r0 = r[0], r1 = r[1];
        *(LAS v2u*)(lbase(L_SCR + 28672 + w * 512 + (vl16 >> 1))) = v2u{r0 + r1, asu(sumsq8(xo))};
      } else {
        const unsigned lo = lane_id();
        __builtin_amdgcn_raw_buffer_store_b128(xo, __builtin_amdgcn_make_buffer_rsrc(p.out, 0, SEQ * DM * 2, 0x00020000),
                                               (lo & 31) * (DM * 2) + (2 * ft + (lo >> 5)) * 16, (t * 128 + (h >> 2) * 32) * (DM * 2), 0);
        keep(xo);
      }
    }
    STAMP(10 + layer * 12);
    if (more) {
      if (first) VMWAIT(7);
      wg_bar();
      const int late = __builtin_amdgcn_readfirstlane(*(LAS int*)(csg + 832));
      if (lead && w2 == 7) {
        float q = 0.f;
        unsigned a = 0u;
        const lptr sp = lbase(L_SCR + 28672 + (vl16 >> 1));
#pragma unroll
        for (int i = 0; i < 7; ++i) { const v2u v = *(LAS v2u*)(sp + i * 512); a += v.x; q += asf(v.y); }
        auto r = __builtin_amdgcn_permlane32_swap(asu(q), asu(q), false, false);
        auto ra = __builtin_amdgcn_permlane32_swap(a, a, false, false);
        const unsigned r0 = r[0], r1 = r[1], a0 = ra[0], a1 = ra[1], qs = asu(asf(r0) + asf(r1));
        const v4u rec = {qs, key0 - qs - a0, key1 - a1, 0u};
        __builtin_amdgcn_raw_buffer_store_b128(rec, rs_ws, vl16 < 512 ? (vl16 >> 8) * 1024 + (vl16 & 255) : OOB,
                                               W_ST + (layer + 1) * 131072 + (t * 8 + 2 * (h >> 2)) * 1024 + (h & 3) * 256, SC1);
        keep(rec);
        if (isA) hint_put(2 * layer + 2, max(late, 0));
        xwait = late + T_X - 12;
      } else {
        xwait = late + T_X;
      }
    }
  };
  auto finish = [&]() INL {
  };
  if (w_ >= 4) {
    run_layer(Layer<0>{}, Layer<1>{}); run_layer(Layer<1>{}, Layer<1>{}); run_layer(Layer<2>{}, Layer<1>{}); run_layer(Layer<3>{}, Layer<1>{});
    if (w_ == 7 && b == 0) __builtin_amdgcn_raw_buffer_store_b32(epoch, rs_ws, 0, W_KEY, 0);
#pragma unroll
    for (int s = 0; s < 14; ++s) asm volatile("" : "=v"(xf[s]));
    asm volatile("" : "=v"(k0), "=v"(k1), "=v"(v0), "=v"(v1), "=v"(ck0), "=v"(ck1), "=v"(cv0), "=v"(cv1), "=v"(var), "=v"(wphi[0]), "=v"(wphi[1]), "=v"(vres0));
  }
  int wz = w_;
  asm volatile("" : "+s"(wz));
  if (wz < 4) {
    run_layer(Layer<0>{}, Layer<0>{}); run_layer(Layer<1>{}, Layer<0>{}); run_layer(Layer<2>{}, Layer<0>{}); run_layer(Layer<3>{}, Layer<0>{});
  }
  finish();
}
}

extern "C" void kernel_launch(void* const* d_in, const int* in_sizes, int n_in, void* d_out, int out_size, void* d_ws, size_t ws_size,
                              hipStream_t stream) {
  (void)in_sizes; (void)n_in; (void)out_size; (void)ws_size;
  (void)hipFuncSetAttribute((const void*)fused_layers, hipFuncAttributeMaxDynamicSharedMemorySize, (int)L_TOTAL);
  Params p;
  p.x = d_in[0]; p.wA = d_in[1]; p.wphi = d_in[2]; p.woA = d_in[3]; p.wB = d_in[5]; p.woB = d_in[6]; p.out = d_out; p.ws = d_ws;
  hipLaunchKernelGGL(fused_layers, dim3(256), dim3(512), L_TOTAL, stream, p);
}
```

```cpp
#include <hip/hip_runtime.h>
#include <stdint.h>

#define LAS __attribute__((address_space(3)))
#define DEV __device__ __forceinline__
typedef unsigned v4u __attribute__((ext_vector_type(4)));
typedef unsigned v2u __attribute__((ext_vector_type(2)));
typedef float v4f __attribute__((ext_vector_type(4)));
typedef float v2f __attribute__((ext_vector_type(2)));
typedef int v4i __attribute__((ext_vector_type(4)));
typedef short v4s __attribute__((ext_vector_type(4)));
typedef __bf16 v8bf __attribute__((ext_vector_type(8)));
typedef __bf16 v2bf __attribute__((ext_vector_type(2)));
typedef __amdgpu_buffer_rsrc_t rsrc_t;

#define TPX 4
#define DLY_T 3
#define WO1 6
#define POLL_OB 2
#define T_KV 4
#define T_H 20
#define T_OA 0
#define T_OB 12
#define P_X 8
#define T_X 8
#define T_XL 0
#define T_OT 20
#define PRI0 1
#define RAMP 0
#define WOS 1
#define T_HL -8
#define ILV 1
#define WIN 4

namespace {
constexpr int SEQ = 2048, DM = 448;
constexpr unsigned L_WK = 0, L_WV = 28672, L_WQ = 57344, L_WQB = 86016, L_SCR = 114688, L_TOTAL = 163840;
constexpr unsigned L_CS = L_SCR + 23 * 2048;
constexpr unsigned L_KVF = L_SCR + 40960;
constexpr unsigned TILEBUF = 16 * 8 * 14 * 1024;
constexpr unsigned W_KEY = 0, W_HINT = 4096, W_FLAG = W_HINT + 11 * 32768, W_ST = W_HINT + 12 * 32768, W_KVP = W_ST + 4 * 131072, W_O = W_KVP + 2 * 1048576,
                   W_HALO = W_O + 4 * 2097152, W_X = W_HALO + 2 * 4194304, W_PT = W_X + 4 * TILEBUF, W_WO = W_PT + 2 * 16 * 86016,
                   W_CS = W_WO + 2 * 28 * 14336, W_END = W_CS + 16 * 56 * 128;
constexpr unsigned W_DBG = 64u << 20;
constexpr int SC1 = 16;
constexpr unsigned OOB = 0x40000000u;

DEV v4f mfma(v4u a, v4u b, v4f c) {
  return __builtin_amdgcn_mfma_f32_16x16x32_bf16(__builtin_bit_cast(v8bf, a), __builtin_bit_cast(v8bf, b), c, 0, 0, 0);
}
DEV unsigned pk(float a, float b) { v2f f = {a, b}; v2bf r = __builtin_convertvector(f, v2bf); return __builtin_bit_cast(unsigned, r); }
typedef LAS char* lptr;
DEV lptr lbase(unsigned v) { asm volatile("" : "+v"(v)); return (lptr)(size_t)v; }
DEV v2u trd(lptr a) { v4s r = __builtin_amdgcn_ds_read_tr16_b64_v4i16((LAS v4s*)a); return __builtin_bit_cast(v2u, r); }
DEV v4u cat(v2u a, v2u b) { return v4u{a.x, a.y, b.x, b.y}; }
DEV v4u lrd(lptr a) { return *(LAS v4u*)a; }
DEV void lwr(lptr a, v4u v) { *(LAS v4u*)a = v; }
#define VMWAIT(n) asm volatile("s_waitcnt vmcnt(" #n ")" ::: "memory")
DEV float asf(unsigned u) { return __builtin_bit_cast(float, u); }
DEV unsigned asu(float f) { return __builtin_bit_cast(unsigned, f); }
DEV void wg_bar() { asm volatile("s_waitcnt lgkmcnt(0)\n\ts_barrier" ::: "memory"); }
DEV void drain() { asm volatile("s_waitcnt vmcnt(0)" ::: "memory"); }
DEV void fence() { asm volatile("" ::: "memory"); }
DEV float ex2(float x) { return __builtin_amdgcn_exp2f(x); }

DEV unsigned lane_id() { unsigned r; asm volatile("v_mbcnt_lo_u32_b32 %0, -1, 0\n\tv_mbcnt_hi_u32_b32 %0, -1, %0" : "=v"(r)); __builtin_assume(r < 64); return r; }
DEV float mx(float a, float b) { return __builtin_elementwise_maximum(a, b); }
DEV float mn(float a, float b) { return __builtin_elementwise_minimum(a, b); }
DEV float mx3(float a, float b, float c) { return mx(mx(a, b), c); }
DEV float mn3(float a, float b, float c) { return mn(mn(a, b), c); }
DEV float xrow(float sel, float v) { const v4f z = {0.f, 0.f, 0.f, 0.f}; const v4f r = __builtin_amdgcn_mfma_f32_16x16x4f32(sel, v, z, 0, 0, 0); return r.x; }
DEV v4f xrow4(float sel, float v) { const v4f z = {0.f, 0.f, 0.f, 0.f}; return __builtin_amdgcn_mfma_f32_16x16x4f32(sel, v, z, 0, 0, 0); }
DEV v4u swap16(v4u v) {
  auto r = __builtin_amdgcn_permlane16_swap(v.x, v.z, false, false);
  auto q = __builtin_amdgcn_permlane16_swap(v.y, v.w, false, false);
  const unsigned r0 = r[0], r1 = r[1], q0 = q[0], q1 = q[1];
  return v4u{r0, q0, r1, q1};
}
DEV float sumsq8(v4u v) {
  const v4f lo = __builtin_bit_cast(v4f, v << 16), hi = __builtin_bit_cast(v4f, v & 0xffff0000u), q = lo * lo + hi * hi;
  return (q.x + q.y) + (q.z + q.w);
}
DEV void xmax2(float& a, float& b) {
  auto r = __builtin_amdgcn_permlane32_swap(asu(a), asu(b), false, false);
  unsigned r0 = r[0], r1 = r[1];
  float z = mx(asf(r0), asf(r1));
  auto q = __builtin_amdgcn_permlane16_swap(asu(z), asu(z), false, false);
  unsigned q0 = q[0], q1 = q[1];
  z = mx(asf(q0), asf(q1));
  auto p = __builtin_amdgcn_permlane32_swap(asu(z), asu(z), false, false);
  unsigned p0 = p[0], p1 = p[1];
  a = asf(p0); b = asf(p1);
}
DEV float xmax1(float a) {
  auto r = __builtin_amdgcn_permlane32_swap(asu(a), asu(a), false, false);
  unsigned r0 = r[0], r1 = r[1];
  float z = mx(asf(r0), asf(r1));
  auto q = __builtin_amdgcn_permlane16_swap(asu(z), asu(z), false, false);
  unsigned q0 = q[0], q1 = q[1];
  return mx(asf(q0), asf(q1));
}
DEV void dma2(unsigned ldsdst, unsigned va, unsigned vb, v4u srd, unsigned sa, unsigned sb) {
  asm volatile("s_mov_b32 m0, %0\n\ts_nop 3\n\tbuffer_load_dwordx4 %1, %3, %4 offen lds\n\tbuffer_load_dwordx4 %2, %3, %5 offen offset:1024 lds"
               :: "s"(ldsdst), "v"(va), "v"(vb), "s"(srd), "s"(sa), "s"(sb) : "memory");
}
DEV void keep(v4u v) { asm volatile("s_nop 1" :: "v"(v) : "memory"); }
DEV void keep(v4u v, v4u u) { asm volatile("s_nop 1" :: "v"(v), "v"(u) : "memory"); }
DEV void dma2s(unsigned ldsdst, unsigned v, v4u srd, unsigned so) {
  asm volatile("s_mov_b32 m0, %0\n\ts_nop 3\n\tbuffer_load_dwordx4 %1, %2, %3 offen sc1 lds\n\tbuffer_load_dwordx4 %1, %2, %3 offen offset:1024 sc1 lds"
               :: "s"(ldsdst), "v"(v), "s"(srd), "s"(so) : "memory");
}
DEV v4u ld16(rsrc_t rs, unsigned v, unsigned so) {
  const v4u r = __builtin_amdgcn_raw_buffer_load_b128(rs, v, so, SC1);
  __builtin_amdgcn_sched_barrier(0);
  return r;
}
DEV v4u mk_srd(const void* p, unsigned bytes) {
  unsigned long long a = (unsigned long long)p;
  return v4u{(unsigned)a, (unsigned)(a >> 32) & 0xffffu, bytes, 0x00020000u};
}
DEV v4u imma(v4u a, v4u b, v4u c) {
  const v4i r = __builtin_amdgcn_mfma_i32_16x16x64_i8(__builtin_bit_cast(v4i, a), __builtin_bit_cast(v4i, b), __builtin_bit_cast(v4i, c), 0, 0, 0);
  return __builtin_bit_cast(v4u, r);
}
DEV v4u weights(unsigned lo) {
  const unsigned a = (lo + 1) * 0x9e3779b1u, b = (a ^ (a >> 15)) * 0x85ebca77u, c = (b ^ (b >> 13)) * 0xc2b2ae3du, d = (c ^ (c >> 16)) * 0x27d4eb2fu;
  return v4u{(a ^ (a >> 16)) | 0x01010101u, (b ^ (b >> 16)) | 0x01010101u, (c ^ (c >> 16)) | 0x01010101u, (d ^ (d >> 16)) | 0x01010101u};
}
DEV void keys(unsigned e, unsigned& k0, unsigned& k1) { k0 = e * 0x9e3779b1u + 0x7f4a7c15u; k1 = e * 0x85ebca77u + 0x165667b1u; }
DEV unsigned xsum2u(unsigned a, unsigned b) {
  auto r = __builtin_amdgcn_permlane32_swap(a, b, false, false);
  const unsigned r0 = r[0], r1 = r[1], z = r0 + r1;
  auto q = __builtin_amdgcn_permlane16_swap(z, z, false, false);
  const unsigned q0 = q[0], q1 = q[1];
  return q0 + q1;
}
DEV void reload4(v4u& a, v4u& b, v4u& c, v4u& d, v4u srd, unsigned voff, unsigned sa, unsigned sb, unsigned sc, unsigned sd) {
  asm volatile("s_sleep 2\n\tbuffer_load_dwordx4 %0, %4, %5, %6 offen sc1\n\tbuffer_load_dwordx4 %1, %4, %5, %7 offen sc1\n\t"
               "buffer_load_dwordx4 %2, %4, %5, %8 offen sc1\n\tbuffer_load_dwordx4 %3, %4, %5, %9 offen sc1\n\ts_waitcnt vmcnt(0)"
               : "+v"(a), "+v"(b), "+v"(c), "+v"(d) : "v"(voff), "s"(srd), "s"(sa), "s"(sb), "s"(sc), "s"(sd) : "memory");
}
DEV bool spend(int& budget) { budget = max(budget - 1, 0); return budget != 0; }
DEV bool nonzero(unsigned v, unsigned long long lanes = ~0ull) { return (__builtin_amdgcn_uicmp(v, 0u, 33) & lanes) != 0ull; }
DEV bool row3_differs(v4u hs, unsigned e0, unsigned e1) {
  const unsigned long long m = __builtin_amdgcn_uicmp((hs.y << 13) + hs.x, e0, 33) | __builtin_amdgcn_uicmp((hs.w << 13) + hs.z, e1, 33);
  unsigned hi = (unsigned)(m >> 32);
  asm volatile("" : "+s"(hi));
  return (hi >> 16) != 0u;
}
DEV bool retry(bool bad, int& budget) {
  if (__builtin_expect(!__any(bad), 1)) return false;
  budget = max(budget - 1, 0);
  return __any(budget != 0);
}

DEV unsigned clock32() { unsigned long long t; asm volatile("s_memrealtime %0\n\ts_waitcnt lgkmcnt(0)" : "=s"(t) :: "memory"); return (unsigned)t; }
DEV int lateness(v2u v, int own, int seg, unsigned key, v4u srd, unsigned voff, unsigned soff, int& budget) {
  asm volatile("" : "+v"(v) :: "memory");
  int over = 0;
  if (__builtin_expect(nonzero(v.y - key, 0xffffull), 0)) {
    do {
      asm volatile("s_sleep 1\n\tbuffer_load_dwordx2 %0, %1, %2, %3 offen sc1\n\ts_waitcnt vmcnt(0)" : "+v"(v) : "v"(voff), "s"(srd), "s"(soff) : "memory");
    } while (nonzero(v.y - key, 0xffffull) && spend(budget));
    const unsigned tv = v.x;
    over = max((int)(clock32() - __builtin_amdgcn_readlane(tv, own)) - seg, 0);
  }
  const unsigned tv = v.x;
  int d = (int)(tv - __builtin_amdgcn_readlane(tv, own));
  d = max(d, __builtin_amdgcn_update_dpp(0, d, 0x128, 0xf, 0xf, false));
  d = max(d, __builtin_amdgcn_update_dpp(0, d, 0x124, 0xf, 0xf, false));
  d = max(d, __builtin_amdgcn_update_dpp(0, d, 0x122, 0xf, 0xf, false));
  d = max(d, __builtin_amdgcn_update_dpp(0, d, 0x121, 0xf, 0xf, false));
  return min(__builtin_amdgcn_readfirstlane(d), 1000) - over;
}
DEV void pause(int ticks) {
  asm volatile("s_cmp_lt_i32 %0, 1\n\ts_cbranch_scc1 2f\n1:\n\ts_sleep 1\n\ts_sub_i32 %0, %0, 4\n\ts_cmp_gt_i32 %0, 0\n\ts_cbranch_scc1 1b\n2:" : "+s"(ticks) :: "memory", "scc");
}

template <class F>
DEV void feature_map(v4u vf, const v4u (&wphi)[2], v4u& plus, v4u& minus, F mid) {
  const v4f z4 = {0.f, 0.f, 0.f, 0.f};
  const v4f p0 = mfma(wphi[0], vf, z4), p1 = mfma(wphi[1], vf, z4);
  float hi = mx3(mx3(mx3(p0.x, p0.y, p0.z), p0.w, p1.x), p1.y, p1.z);
  float lo = -mn3(mn3(mn3(p0.x, p0.y, p0.z), p0.w, p1.x), p1.y, p1.z);
  xmax2(hi, lo);
  mid();
  const v2f p01 = {p0.x, p0.y}, p23 = {p0.z, p0.w}, p45 = {p1.x, p1.y}, h2 = {hi, hi}, l2 = {lo, lo};
  v2f a01 = p01 - h2, a23 = p23 - h2, a45 = p45 - h2, b01 = p01 + l2, b23 = p23 + l2, b45 = p45 + l2;
  asm volatile("" : "+v"(a01), "+v"(a23), "+v"(a45), "+v"(b01), "+v"(b23), "+v"(b45));
  const v2f e01 = {ex2(a01.x), ex2(a01.y)}, e23 = {ex2(a23.x), ex2(a23.y)}, e45 = {ex2(a45.x), ex2(a45.y)};
  const float e6 = ex2(p1.z - hi);
  const v2f se = (e01 + e23) + e45;
  v4f te = xrow4(1.f, (se.x + e6) + se.y);
  __builtin_amdgcn_sched_barrier(0);
  const v2f f01 = {ex2(-b01.x), ex2(-b01.y)}, f23 = {ex2(-b23.x), ex2(-b23.y)}, f45 = {ex2(-b45.x), ex2(-b45.y)};
  const float f6 = ex2(-(p1.z + lo));
  const v2f sf = (f01 + f23) + f45;
  v4f tf = xrow4(1.f, (sf.x + f6) + sf.y);
  __builtin_amdgcn_sched_barrier(0);
  const float re = __builtin_amdgcn_rcpf(te.x);
  asm volatile("" :: "v"(te));
  const v2f x01 = e01 * re, x23 = e23 * re, x45 = e45 * re;
  plus = v4u{pk(x01.x, x01.y), pk(x23.x, x23.y), pk(x45.x, x45.y), pk(e6 * re, 0.f)};
  __builtin_amdgcn_sched_barrier(0);
  const float rf = __builtin_amdgcn_rcpf(tf.x);
  asm volatile("" :: "v"(tf));
  const v2f y01 = f01 * rf, y23 = f23 * rf, y45 = f45 * rf;
  minus = v4u{pk(y01.x, y01.y), pk(y23.x, y23.y), pk(y45.x, y45.y), pk(f6 * rf, 0.f)};
}

DEV v4u pexp(v4f a, v4f b) {
  return v4u{pk(ex2(a.x), ex2(a.y)), pk(ex2(a.z), ex2(a.w)), pk(ex2(b.x), ex2(b.y)), pk(ex2(b.z), ex2(b.w))};
}
#define VMKNOWN(n) __builtin_amdgcn_s_waitcnt(((n) & 15) | (((n) >> 4) << 14) | 0xf70)
#define LGKNOWN(n) __builtin_amdgcn_s_waitcnt(0xc07f | ((n) << 8))
#define PIN(x) asm volatile("" : "+v"(x))
#define SB() __builtin_amdgcn_sched_barrier(0)
#define INL __attribute__((always_inline))

#define STAMP(i) do {} while (0)

template <int N> struct Layer { static constexpr int value = N; };
struct Params {
  const void* x; const void* wA; const void* wphi; const void* woA; const void* wB; const void* woB; void* out; void* ws;
};

__global__ void __launch_bounds__(512, 2) fused_layers(Params) {
  typedef unsigned long long v8p __attribute__((ext_vector_type(8)));
  v8p args;
  asm volatile("s_load_dwordx16 %0, %1, 0x0" : "=s"(args) : "s"(__builtin_amdgcn_kernarg_segment_ptr()));
  const int b = blockIdx.x, xg = b & 7, xi = b >> 3;
  constexpr int HPX = 32 / TPX, NGT = 16 / TPX;
  const int t = (xg % NGT) * TPX + xi / HPX;
  const int h = (xg / NGT) * HPX + xi % HPX;
  const int wid = __builtin_amdgcn_readfirstlane(threadIdx.x >> 6) & 7;
  const int w_ = wid < 4 ? 7 - wid : wid - 4;
  const int l = threadIdx.x & 63, c = l & 15, g = l >> 4;
  const bool g3 = g == 3;
  STAMP(0);
  constexpr unsigned L_XS = 73728, L_JOB = L_WQ, L_CSP = L_XS + 81920;
  const unsigned chunk16 = ((l & 3) ^ ((l >> 5) << 1)) * 16, vqkv = (l >> 2) * (3 * DM * 2) + chunk16;
  const int kvp_ = (w_ >> 1) & 1, kvs = w_ & 1;
  const unsigned kvat = kvp_ * L_WV + kvs * 2048, kvso = kvs * (32 * 3 * DM * 2) + (kvp_ + 1) * DM * 2 + 56 * h;
  const unsigned xsb = L_XS + w_ * 10240, xso = (t * 128 + w_ * 16) * (DM * 2);
  const unsigned vx0 = (l >> 3) * (DM * 2) + ((l & 7) ^ (l >> 4)) * 16, vx1 = ((l >> 3) + 8) * (DM * 2) + ((l & 7) ^ (4 + (l >> 4))) * 16 - 1024;
  const bool job = t < 14 && (w_ < 4 || (w_ < 6 && h < 14));
  const unsigned jat = L_JOB + w_ * 2048;
  asm volatile("" :: "v"(vqkv), "v"(vx0), "v"(vx1), "s"(kvat), "s"(kvso), "s"(xsb), "s"(xso), "s"(jat));
  asm volatile("s_waitcnt lgkmcnt(0)" : "+s"(args));
  const Params p = {(const void*)args[0], (const void*)args[1], (const void*)args[2], (const void*)args[3], (const void*)args[4], (const void*)args[5], (void*)args[6], (void*)args[7]};
  const v4u srdA = mk_srd(p.wA, DM * 3 * DM * 2), srdX = mk_srd(p.x, SEQ * DM * 2);
  auto kvblock = [&](int j) INL { const unsigned so = kvso + j * (64 * 3 * DM * 2); dma2(kvat + j * 4096, vqkv, vqkv, srdA, so, so + 16 * 3 * DM * 2 - 1024); };
  auto xline = [&](int j, int up) INL {
    const unsigned so = xso + up * (64 * DM * 2) + j * 128;
    dma2(xsb + up * 40960 + (j % 5) * 2048, vx0, vx1, srdX, so, so);
  };
  if (w_ < 4) {
    __builtin_amdgcn_s_setprio(PRI0);
    const unsigned so = t * (32 * 3 * DM * 2) + (w_ == 3 ? 0 : w_ * DM * 2) + 56 * h;
    if (job) dma2(jat, vqkv, vqkv, mk_srd(w_ ? p.wB : p.wA, DM * 3 * DM * 2), so, so + 16 * 3 * DM * 2 - 1024);
    xline(0, 0); xline(0, 1);
  } else {
    if (job) {
      const unsigned v = (l >> 2) * (DM * 2) + chunk16, so = t * (32 * DM * 2) + 64 * h;
      dma2(jat, v, v, mk_srd(w_ == 5 ? p.woB : p.woA, DM * DM * 2), so, so + 16 * DM * 2 - 1024);
    } else if (w_ == 7) {
      const unsigned e = (l >> 2) & 7, g0 = l >> 5, d0 = e < 4 ? 4 * g0 + e : 12 + 3 * g0 + e, d1 = d0 + (e < 4 ? 8 : 6);
      dma2(jat, e < 7 ? d0 * 56 + chunk16 + 1024 : OOB, e < 7 ? d1 * 56 + chunk16 : OOB,
           mk_srd((const char*)p.wphi - 1024, 16 * 28 * 28 * 2 + 1024), h * 28 * 28 * 2, h * 28 * 28 * 2);
    }
    kvblock(0); kvblock(1);
  }
  unsigned epoch, key0, key1, keyk;
  asm volatile("s_load_dword %0, %1, 0x0" : "=s"(epoch) : "s"(p.ws));
  const rsrc_t rs_ws = __builtin_amdgcn_make_buffer_rsrc(p.ws, 0, W_END, 0x00020000);
  const v4u srdW = mk_srd(p.ws, W_END);
  unsigned vl16 = l * 16;
  asm volatile("" : "+v"(vl16));
  const int ft = 7 * (h & 3) + min(w_, 6), nx = 2 * ft + (g >> 1);
  const unsigned pubx = w_ == 7 ? OOB : (g & 1) * 14336 + (nx >> 2) * 1024 + ((nx & 3) * 16 + c) * 16;
  const unsigned tw = (t * 8 + w_) * 14336, tp = (t * 8 + 2 * (h >> 2)) * 14336;
  const unsigned ptA = W_PT + h * 86016 + w_ * 2048;
  int budget = 1 << 16;
  v4u wphi[2];
  v4u park[8];
  v4u xf[14];
  v4f k0, k1, v0, v1, ck0, ck1, cv0, cv1;
  float var;
  v4f res0, res1;
  auto hint_put = [&](int slot, int ahead = 0) INL {
    __builtin_amdgcn_raw_buffer_store_b64(v2u{clock32() + ahead, key0}, rs_ws, vl16 < 256 ? vl16 >> 1 : OOB, W_HINT + slot * 32768 + (t * 16 + h) * 128, SC1);
  };
  auto hint_get = [&](unsigned v, unsigned so) INL { SB(); const v2u r = __builtin_amdgcn_raw_buffer_load_b64(rs_ws, v, so, SC1); SB(); return r; };
  const unsigned q4 = (l & 15) >> 2, p4 = l & 3;
  const lptr l16 = (lptr)(size_t)vl16, lq16 = lbase(L_WQ + l * 16);
  const lptr stw = lbase(L_SCR + c * 32 + ((g ^ (c >> 2)) * 8));
  const lptr str = lbase(L_SCR + (4 * g + q4) * 32 + ((p4 ^ g) * 8));
  const lptr sl16 = lbase(L_SCR + l * 16), csg = lbase(L_CS + g * 16);
  const unsigned vcol = l < 16 ? l * 2048 : OOB, vrow = l < 16 ? l * 128 : OOB;
  const float sel3 = g3 ? 1.f : 0.f;
  v4u hwt = weights(l);
  if (g3) { hwt.z = 0u; hwt.w = 0u; }
  const v4u zu = {0u, 0u, 0u, 0u};

  v4u wof[14];
  const unsigned wos = W_WO + ft * 14336;
  auto kv_gemm = [&](const int s0, const int s1, auto step) INL {
    const v4f z4 = {0.f, 0.f, 0.f, 0.f};
    if (s0 == 0) { k0 = z4; k1 = z4; v0 = z4; v1 = z4; }
    v4u a[8];
#pragma unroll
    for (int i = 0; i < 2; ++i) { const int o = (s0 + i) * 2048; a[4 * i] = lrd(l16 + o); a[4 * i + 1] = lrd(l16 + o + 1024); a[4 * i + 2] = lrd(l16 + L_WV + o); a[4 * i + 3] = lrd(l16 + L_WV + o + 1024); SB(); }
#pragma unroll
    for (int s = s0; s < s1; ++s) {
      const int i = 4 * (s & 1), o = (s + 2) * 2048;
      if (s < s1 - 1) LGKNOWN(4); else LGKNOWN(0);
      SB();
      k1 = mfma(a[i + 1], xf[s], k1); SB();
      step(s);
      if (s + 2 < s1) { a[i + 1] = lrd(l16 + o + 1024); SB(); }
      k0 = mfma(a[i], xf[s], k0); SB(); if (s + 2 < s1) { a[i] = lrd(l16 + o); SB(); }
      v0 = mfma(a[i + 2], xf[s], v0); SB(); if (s + 2 < s1) { a[i + 2] = lrd(l16 + L_WV + o); SB(); }
      v1 = mfma(a[i + 3], xf[s], v1); SB(); if (s + 2 < s1) { a[i + 3] = lrd(l16 + L_WV + o + 1024); SB(); }
    }
  };
  v4u hwk = weights(l);
  hwk.w &= 0xffffu;
  asm volatile("" : "+v"(hwk.z), "+v"(hwk.w), "+v"(hwt));
  v4u fl0, fl1;
  auto flags_wait = [&](v2u f, unsigned v, unsigned so, unsigned long long lanes) INL {
    while (nonzero((f.x ^ key0) | (f.y ^ key1), lanes) && spend(budget))
      asm volatile("s_sleep 3\n\tbuffer_load_dwordx2 %0, %1, %2, %3 offen sc1\n\ts_waitcnt vmcnt(0)" : "+v"(f) : "v"(v), "s"(srdW), "s"(so) : "memory");
  };
  unsigned vres0;
  {
    unsigned a0 = (8 * g + (c >> 2)) * 64 + ((((l & 3) >> 1) ^ ((g & 1) << 1)) * 16) + (l & 1) * 8;
    unsigned sl3 = c >= 12 ? 4 * c - 45 : c + ((c * 11) >> 5);
    const unsigned one = 0x3f803f80u;
    v4u one8 = {one, one, one, one};
    const v4f z4 = {0.f, 0.f, 0.f, 0.f};
    auto turn = [&](lptr r0, lptr r1, bool pad, v4u& f0, v4u& f1) INL {
      f0 = cat(trd(r0), trd(r0 + 256)); f1 = cat(trd(r1), trd(r1 + 256));
      f1 = v4u{pad ? one : f1.x, pad ? one : f1.y, pad ? one : f1.z, pad ? one : f1.w};
    };
    const unsigned xa = xsb + c * 128 + (g ^ (c >> 1)) * 16;
    const lptr xa0 = lbase(xa), xa1 = lbase(xa ^ 64), cp = lbase(L_CSP + g * 16);
    unsigned vres = (t * 128 + (2 * (h >> 2) + (g & 1)) * 16 + c) * (DM * 2) + nx * 16;
    unsigned pubx_ = pubx, vcol_ = vcol, vrow_ = vrow;
    float dg0 = c == 4 * g ? 1.f : 0.f, dg1 = c == 4 * g + 1 ? 1.f : 0.f, dg2 = c == 4 * g + 2 ? 1.f : 0.f, dg3 = c == 4 * g + 3 ? 1.f : 0.f;
    asm volatile("" : "+v"(vres), "+v"(a0), "+v"(sl3), "+v"(one8), "+v"(dg0), "+v"(dg1), "+v"(dg2), "+v"(dg3) : "v"(pubx_), "v"(vcol_), "v"(vrow_), "v"(sel3));
    {
      const bool spread = w_ == 7 || (w_ < 4 && w_ != 2);
      const unsigned slot = spread ? sl3 : c;
      const lptr jr0 = lbase(jat + a0), jr1 = lbase(jat + (a0 ^ 32));
      unsigned vj1 = ((w_ & 6) == 4 ? 14336 : 1024) + (vl16 & 768) + slot * 16, vcs = g == 0 ? c * 4 : g == 1 ? 64 + slot * 4 : OOB;
      unsigned dst = !job ? W_END : w_ < 4 ? W_PT + ((w_ ? 16 : 0) + h) * 86016 + ((w_ ? w_ - 1 : 2) * 14 + t) * 2048 : W_WO + ((w_ - 4) * 28 + 2 * h) * 14336 + t * 1024;
      unsigned dcs = (job && w_ < 4) ? W_CS + (h * 56 + w_ * 14 + t) * 128 : W_END;
      asm volatile("" : "+v"(vj1), "+v"(vcs));
      asm volatile("" : "+s"(dst), "+s"(dcs));
      STAMP(51);
      if (w_ < 4) { xline(1, 0); xline(1, 1); } else { kvblock(2); kvblock(3); }
      asm volatile("s_waitcnt lgkmcnt(0)" : "+s"(epoch) :: "memory");
      epoch += 1u;
      keys(epoch, key0, key1);
      keyk = ((vl16 & 512) ? key1 : key0) >> ((vl16 & 256) ? 14 : 0);
      asm volatile("" : "+v"(keyk));
      VMWAIT(8);
      STAMP(47);
      v4u f0, f1;
      turn(jr0, jr1, spread && c >= 12, f0, f1);
      if (w_ == 7) { lwr(lq16 + 7 * 2048, f0); lwr(lbase(jat + vj1), f1); }
      __builtin_amdgcn_raw_buffer_store_b128(f0, rs_ws, vl16, dst, SC1);
      __builtin_amdgcn_raw_buffer_store_b128(f1, rs_ws, vj1, dst, SC1);
      keep(f0, f1);
      const v4f c0 = mfma(one8, f0, z4), c1 = mfma(one8, f1, z4);
      __builtin_amdgcn_raw_buffer_store_b32(asu(g ? c1.x : c0.x), rs_ws, vcs, dcs, SC1);
      fence();
      STAMP(52);
    }
    const unsigned kslot = kvp_ ? c : sl3;
    const lptr kr0 = lbase(kvat + a0), kr1 = lbase(kvat + (a0 ^ 32)), kw0 = lbase(kvat + vl16), kw1 = lbase(kvat + 1024 + (vl16 & 768) + kslot * 16);
    const lptr cw0 = lbase(L_CSP + (w_ & 3) * 128 + c * 4), cw1 = lbase(L_CSP + (w_ & 3) * 128 + 64 + kslot * 4);
    const bool kpad = !kvp_ && c >= 12;
    v4f cs0 = z4, cs1 = z4;
    auto kvturn = [&](int j) INL {
      v4u f0, f1;
      turn(kr0 + j * 4096, kr1 + j * 4096, kpad, f0, f1);
      lwr(kw0 + j * 4096, f0); lwr(kw1 + j * 4096, f1);
      cs0 = mfma(one8, f0, cs0); cs1 = mfma(one8, f1, cs1);
    };
    if (w_ >= 4) {
      VMWAIT(7); kvturn(0); kvturn(1);
      VMWAIT(3); kvturn(2); kvturn(3);
      kvblock(4); kvblock(5); kvblock(6);
      VMWAIT(6);
    } else {
      xline(2, 0); xline(2, 1); xline(3, 0); xline(3, 1);
      __builtin_amdgcn_s_setprio(0);
      xline(4, 0); xline(4, 1);
      VMWAIT(4);
    }
    STAMP(48);
    wg_bar();
    STAMP(49);
#pragma unroll
    for (int j = 0; j < 2; ++j) { xf[2 * j] = lrd(xa0 + j * 2048); xf[2 * j + 1] = lrd(xa1 + j * 2048); }
    if (w_ == 6) __builtin_amdgcn_raw_buffer_store_b64(v2u{key0, key1}, rs_ws, vl16 < 256 ? vl16 >> 1 : OOB, W_FLAG + (t * 16 + h) * 128, SC1);
    v4f gram = z4, pa, pb;
    kv_gemm(0, 8, [&](int s) INL {
      gram = mfma(xf[s], xf[s], gram);
      if (s < 2) { xf[4 + 2 * s] = lrd(xa0 + (2 + s) * 2048); xf[5 + 2 * s] = lrd(xa1 + (2 + s) * 2048); }
      if (s == 1) xline(5, 0);
      if (s == 3) xline(6, 0);
      if (s == 5) { wphi[0] = lrd(lq16 + 7 * 2048); wphi[1] = lrd(lq16 + 7 * 2048 + 1024); }
      SB();
    });
    STAMP(50);
    v2u fc;
    if (w_ == 7) asm volatile("buffer_load_dwordx2 %0, %1, %2, %3 offen sc1\n\ts_waitcnt vmcnt(1)" : "=&v"(fc) : "v"(vcol), "s"(srdW), "s"(W_FLAG + h * 128) : "memory");
    else drain();
    if (w_ >= 4) { kvturn(4); kvturn(5); kvturn(6); *(LAS float*)(cw0) = cs0.x; *(LAS float*)(cw1) = cs1.x; }
#pragma unroll
    for (int j = 5; j < 7; ++j) { xf[2 * j] = lrd(xa0 + (j - 5) * 2048); xf[2 * j + 1] = lrd(xa1 + (j - 5) * 2048); }
    if (w_ == 7) {
      asm volatile("s_waitcnt vmcnt(0)" : "+v"(fc) :: "memory");
      flags_wait(fc, vcol, W_FLAG + h * 128, 0xffffull);
    }
    wg_bar();
    STAMP(54);
    xf[8] = lrd(xa0 + 4 * 2048); xf[9] = lrd(xa1 + 4 * 2048);
    wg_bar();
    kv_gemm(8, 14, [&](int s) INL {
      if (s < 11) { gram = mfma(xf[2 * s - 8], xf[2 * s - 8], gram); gram = mfma(xf[2 * s - 7], xf[2 * s - 7], gram); }
      if (s == 8) dma2s(L_WQ + w_ * 2048, vl16, srdW, ptA + L_WQ);
      if (s == 11 && w_ < 6) dma2s(L_WQ + 16384 + w_ * 2048, vl16, srdW, ptA + L_WQ + 16384);
      if (s >= 9 && s < 13) { const v4f u = pa + pb; if (s == 9) ck0 = u; else if (s == 10) ck1 = u; else if (s == 11) cv0 = u; else cv1 = u; }
      if (s < 12) { const int o = ((s - 8) >> 1) * 256 + ((s - 8) & 1) * 64; pa = *(LAS v4f*)(cp + o); pb = *(LAS v4f*)(cp + o + 128); }
      if (s == 12) var = xrow(1.f / DM, (gram.x * dg0 + gram.y * dg1) + (gram.z * dg2 + gram.w * dg3));
      SB();
    });
    if (w_ == 6) { *(LAS v4f*)(csg) = ck0; *(LAS v4f*)(csg + 64) = ck1; *(LAS v4f*)(csg + 128) = cv0; *(LAS v4f*)(csg + 192) = cv1; }
    vres0 = vres;
  }
  STAMP(56);
  int xwait;
  v4u kc = {key0, 0u, key1, 0u};
  PIN(kc);
  auto run_layer = [&](auto kind, auto role) INL {
    constexpr int layer = decltype(kind)::value, rep = layer >> 1;
    constexpr bool isA = !(layer & 1), first = layer == 0, more = layer < 3, lead = decltype(role)::value;
    int w = w_;
    asm volatile("" : "+s"(w));
    __builtin_assume(lead ? (w >= 4 && w < 8) : (w >= 0 && w < 4));
    const v4f z4 = {0.f, 0.f, 0.f, 0.f};
    const unsigned wob = (lead && w == 7) ? W_END - 12288 : wos + (isA ? 0 : 28 * 14336), wov = (lead && w == 7) ? OOB : vl16;
    auto load_wo = [&](int s0, int s1) INL {
#pragma unroll
      for (int s = s0; s < s1; ++s) wof[s] = ld16(rs_ws, wov + (s & 3) * 1024, wob + (s >> 2) * 4096);
    };
    v2u hh;
    unsigned vh;
    const unsigned shh = W_HINT + 2 * layer * 32768 + (t - 2) * 2048 + h * 128;
    if (!isA) { const unsigned lo = lane_id(); vh = lo >= 16 ? OOB : (lo < 2 && (int)lo < t) ? (1 - lo) * 2048 : 4096; }
    const lptr cs = csg + (isA ? 0 : 384);
    float rstd, nmr;
    auto stats = [&]() INL {
      const float mean = k1.w * (1.f / DM);
      rstd = __builtin_amdgcn_rsqf(mx(fmaf(-mean, mean, var), 0.f) + 1e-5f); nmr = -mean * rstd;
      PIN(rstd); PIN(nmr);
    };
    unsigned hp = t < 15 ? W_HALO + rep * 4194304 + h * 262144 + (t * 8 + w) * 2048 : W_END;
    if (!isA) asm volatile("" : "+s"(hp));
    if (!first) {
      unsigned xs[4], sts = W_ST + layer * 131072 + (t * 8 + w) * 1024;
#pragma unroll
      for (int i = 0; i < 4; ++i) { xs[i] = W_X + layer * TILEBUF + tw + i * 4096; asm volatile("" : "+s"(xs[i])); }
      asm volatile("" : "+s"(sts));
      if (!isA) PIN(vh);
      pause(xwait);
      auto ldx = [&](int s) INL { xf[s] = ld16(rs_ws, vl16 + (s & 3) * 1024, xs[s >> 2]); };
      for (;;) {
#pragma unroll
        for (int s = 0; s < WIN; ++s) ldx(s);
        STAMP(2 + layer * 12);
        v4u st;
        unsigned c0 = 0u - key0, c1 = 0u - key1;
        kv_gemm(0, 14, [&](int s) INL {
          const int n0 = WIN + s + (s < RAMP ? s : RAMP), n1 = WIN + s + 1 + (s + 1 < RAMP ? s + 1 : RAMP);
#pragma unroll
          for (int n = n0; n < n1; ++n) {
            if (n < 14) ldx(n);
            if (n == 14) st = ld16(rs_ws, vl16, sts);
            if (n == 15 && !isA) hh = hint_get(vh, shh);
          }
          c0 += xf[s].x + xf[s].z; c1 += xf[s].y + xf[s].w; PIN(c0); PIN(c1);
          if (s == 13) { var = xrow(1.f / DM, asf(st.x)); c0 += st.x + st.y; c1 += st.z; PIN(c0); PIN(c1); }
          if (s == 12) { ck0 = *(LAS v4f*)(cs); ck1 = *(LAS v4f*)(cs + 64); cv0 = *(LAS v4f*)(cs + 128); cv1 = *(LAS v4f*)(cs + 192); }
          SB();
        });
        c0 = xsum2u(c0, c1);
        stats();
        if (__builtin_expect(!nonzero(c0), 1) || !spend(budget)) break;
        __builtin_amdgcn_s_sleep(4);
      }
    }
    STAMP(3 + layer * 12);
    if (first) stats();
    auto ln7 = [&](v4f a0, v4f a1, v4f c0, v4f c1, float sc, float sh, float pad) INL {
      a0 = a0 * sc + c0 * sh;
      const v2f u = v2f{a1.x, a1.y} * sc + v2f{c1.x, c1.y} * sh;
      return v4u{pk(a0.x, a0.y), pk(a0.z, a0.w), pk(u.x, u.y), pk(fmaf(a1.z, sc, c1.z * sh), pad)};
    };
    auto q_gemm = [&](float sc, float pad) INL {
      v4f q0 = z4, q1 = z4, c0, c1;
      const lptr lq = lq16 + (isA ? 0 : L_WQB - L_WQ);
      v4u a[8];
#pragma unroll
      for (int i = 0; i < 8; ++i) { a[i] = lrd(lq + i * 1024); SB(); }
#pragma unroll
      for (int s = 0; s < 14; ++s) {
        const int i = (2 * s) & 7;
        if (!(s & 1)) { if (s < 12) LGKNOWN(4); else LGKNOWN(0); SB(); }
        q0 = mfma(a[i], xf[s], q0); SB(); if (s + 4 < 14) { a[i] = lrd(lq + (s + 4) * 2048); SB(); }
        q1 = mfma(a[i + 1], xf[s], q1); SB(); if (s + 4 < 14) { a[i + 1] = lrd(lq + (s + 4) * 2048 + 1024); SB(); }
        if (s == 12) { c0 = *(LAS v4f*)(cs + 256); c1 = *(LAS v4f*)(cs + 320); SB(); }
      }
      v4u r = ln7(q0, q1, c0, c1, rstd * sc, nmr * sc, pad);
      asm volatile("" : "+v"(r) :: "memory");
      return r;
    };
    constexpr float L2E = 1.4426950408889634f;
    const v4u kf = isA ? ln7(k0, k1, ck0, ck1, rstd * L2E, nmr * L2E, 0.f) : ln7(k0, k1, ck0, ck1, rstd, nmr, 0.f);
    auto v_pack = [&]() INL {
      const v4f a0 = v0 * rstd + cv0 * nmr, a1 = v1 * rstd + cv1 * nmr;
      return v4u{pk(a0.x, a0.y), pk(a0.z, a0.w), g3 ? 0x3f80u : pk(a1.x, a1.y), g3 ? 0u : pk(a1.z, a1.w)};
    };
    v4f o0, o1;
    float den;
    const unsigned soh = W_HINT + (2 * layer + 1) * 32768 + t * 2048;
    v2u oh;

    if constexpr (isA) {
      if (lead && w == 7) hint_put(2 * layer);
      if (!first) { if (!lead) load_wo(6, 10); }
      else if (lead) { fl0 = ld16(rs_ws, vl16, W_CS + (h * 56 + 14 * (w & 3)) * 128); fl1 = ld16(rs_ws, vl16 < 768 ? vl16 + 1024 : OOB, W_CS + (h * 56 + 14 * (w & 3)) * 128); }
      v2u fw;
      const unsigned vfw = ((vl16 >> 4) & 15) * 2048 + (vl16 >> 8) * 128, sfw = W_FLAG + ((7 * (h & 3)) >> 1) * 128;
      if (first && !lead) fw = hint_get(vfw, w == 0 ? sfw : W_END);
      const v4u vf = v_pack();
      v4u pp, pm, qf;
      feature_map(kf, wphi, pp, pm, [&]() INL { SB(); if (!first && !lead) load_wo(10, 14); });
      {
        const lptr tb = stw + w * 3072;
        *(LAS v2u*)(tb) = v2u{pp.x, pp.y}; *(LAS v2u*)(tb + 512) = v2u{pp.z, pp.w};
        *(LAS v2u*)(tb + 1024) = v2u{pm.x, pm.y}; *(LAS v2u*)(tb + 1536) = v2u{pm.z, pm.w};
        *(LAS v2u*)(tb + 2048) = v2u{vf.x, vf.y}; *(LAS v2u*)(tb + 2560) = v2u{vf.z, vf.w};
      }
      if (first) {
        VMKNOWN(0);
        if (!lead && w == 0) flags_wait(fw, vfw, sfw, ~0ull);
        if (lead) {
          v4f cq = __builtin_bit_cast(v4f, fl0) + __builtin_bit_cast(v4f, fl1);
#pragma unroll
          for (int i = 0; i < 4; ++i) {
            float u = cq[i];
            u += asf(__builtin_amdgcn_update_dpp(0, asu(u), 0x128, 0xf, 0xf, false));
            cq[i] = xrow(1.f, u);
          }
          *(LAS v4f*)(lbase(L_CS + 256 + (w & 3) * 128 + (vl16 & 127))) = cq;
        }
      }
      v4u hwk_ = {hwt.x, hwt.y, hwk.z, hwk.w};
      unsigned kvp = W_KVP + rep * 1048576 + h * 65536 + (w & 3) * 1024 + (lead ? t * 4096 : 0);
      const lptr pa = lbase((size_t)str + (w & 2) * 512), pb = lbase((size_t)str + 2048 + (w & 1) * 512);
      asm volatile("" : "+v"(hwk_));
      asm volatile("" : "+s"(kvp));
      wg_bar();
      STAMP(4 + layer * 12);
      auto residual = [&]() INL {
        park[0] = __builtin_amdgcn_raw_buffer_load_b128(__builtin_amdgcn_make_buffer_rsrc((void*)p.x, 0, SEQ * DM * 2, 0x00020000), vres0, 0, 0); SB();
      };
      if (lead) {
        __builtin_amdgcn_s_setprio(2);
        v4u bv[4], x0[4], x1[4];
#pragma unroll
        for (int kk = 0; kk < 4; ++kk) {
          bv[kk] = cat(trd(pb + kk * 6144), trd(pb + kk * 6144 + 3072));
          x0[kk] = cat(trd(pa + kk * 6144), trd(pa + kk * 6144 + 3072));
          x1[kk] = cat(trd(pa + kk * 6144 + 512), trd(pa + kk * 6144 + 3072 + 512));
          SB();
        }
        v4f d0 = z4, d1 = z4;
#pragma unroll
        for (int kk = 0; kk < 4; ++kk) { d0 = mfma(x0[kk], bv[kk], d0); d1 = mfma(x1[kk], bv[kk], d1); }
        v4u dv = {pk(d0.x, d0.y), pk(d0.z, d0.w), pk(d1.x, d1.y), pk(d1.z, 0.f)};
        const v4u hs = imma(hwk_, dv, zu);
        dv.w |= ((hs.x + keyk) & 0x3fffu) << 16;
        __builtin_amdgcn_raw_buffer_store_b128(dv, rs_ws, vl16, kvp, SC1);
        keep(dv);
        __builtin_amdgcn_s_setprio(0);
        STAMP(5 + layer * 12);
        if (first) { load_wo(0, 7); residual(); } else load_wo(6, 14);
        qf = q_gemm(L2E, 0.f);
        feature_map(qf, wphi, pp, pm, []() {});
        PIN(pp); PIN(pm);
        if (first) {
          asm volatile("" : "+v"(pp), "+v"(pm) :: "memory");
          load_wo(7, 14);
#pragma unroll
          for (int j = 0; j < 4; ++j) if (j < 3 || w < 6) dma2s(L_WQB + (w & 3) * 2048 + j * 8192, vl16, srdW, ptA - 8192 + 16 * 86016 + L_WQ + j * 8192);
        }
      } else {
        const unsigned sh = W_HINT + 2 * layer * 32768 + h * 128;
        __builtin_amdgcn_s_setprio(1);
        __builtin_amdgcn_s_sleep(DLY_T);
        const v2u hv = hint_get(vcol, sh);
        if (first) { load_wo(0, 4); residual(); }
        qf = q_gemm(L2E, 0.f);
        pause(lateness(hv, t, 150, key0, srdW, vcol, sh, budget) + T_KV);
        if (w == 0) hint_put(2 * layer + 1);
        fence();
        STAMP(6 + layer * 12);
        v4u kp[16];
#pragma unroll
        for (int i = 0; i < 8; ++i) kp[i] = ld16(rs_ws, vl16, kvp + i * 4096);
        feature_map(qf, wphi, pp, pm, [&]() INL {
          SB();
#pragma unroll
          for (int i = 8; i < 16; ++i) kp[i] = ld16(rs_ws, vl16, kvp + i * 4096);
        });
        PIN(pp); PIN(pm);
        const unsigned lo = lane_id();
        const unsigned bit = ((lo >> 4) == ((lo >> 2) & 3)) ? ((lo & 1) ? 0x3f800000u : 0x3f80u) : 0u;
        const unsigned u0 = (lo & 2) ? 0u : bit, u1 = (lo & 2) ? bit : 0u;
        const v4u sa = {u0, u1, 0u, 0u}, sb = {0u, 0u, u0, u1};
        const lptr kd = lbase((size_t)sl16 + (L_KVF - L_SCR) + w * 1024);
        auto add_up = [&](bool once) INL {
          v4f lo4 = z4, hi4 = z4;
          v4u hs = zu;
          unsigned ck = 0u - 16u * keyk;
          SB();
#pragma unroll
          for (int i = 0; i < 16; ++i) {
            hs = imma(hwk_, kp[i], hs); lo4 = mfma(sa, kp[i], lo4); hi4 = mfma(sb, kp[i], hi4);
            ck += kp[i].w >> 16; PIN(ck);
            SB();
            if (first && once && i < 10) load_wo(4 + i, 5 + i);
          }
          const bool bad = nonzero((hs.x - ck) & 0x3fffu);
          SB();
          lwr(kd, v4u{pk(lo4.x, lo4.y), pk(lo4.z, lo4.w), pk(hi4.x, hi4.y), pk(hi4.z, 0.f)});
          return bad;
        };
        if (__builtin_expect(add_up(true), 0)) {
          do {
#pragma unroll
            for (int i = 0; i < 16; i += 4) reload4(kp[i], kp[i + 1], kp[i + 2], kp[i + 3], srdW, vl16, kvp + i * 4096, kvp + (i + 1) * 4096, kvp + (i + 2) * 4096, kvp + (i + 3) * 4096);
          } while (add_up(false) && spend(budget));
        }
      }
      __builtin_amdgcn_s_setprio(0);
      wg_bar();
      STAMP(7 + layer * 12);
      v4u kv[4];
      kv[0] = lrd(sl16 + (L_KVF - L_SCR) + 1024); SB(); kv[1] = lrd(sl16 + (L_KVF - L_SCR) + 3072); SB();
      kv[2] = lrd(sl16 + (L_KVF - L_SCR)); SB(); kv[3] = lrd(sl16 + (L_KVF - L_SCR) + 2048);
      SB();
      if (lead) oh = hint_get(vrow, w == 7 ? soh : W_END);
      o1 = mfma(kv[0], pp, z4); o1 = mfma(kv[1], pm, o1); SB();
      o0 = mfma(kv[2], pp, z4); o0 = mfma(kv[3], pm, o0); SB();
      { const v4f e6 = {1e-6f, 1e-6f, 1e-6f, 1e-6f}, r = __builtin_amdgcn_mfma_f32_16x16x4f32(sel3, o1.x, e6, 0, 0, 0); den = r.x; }
    } else {
      const unsigned hb = W_HALO + rep * 4194304 + h * 262144;
      __builtin_amdgcn_raw_buffer_store_b128(kf, rs_ws, vl16, hp, SC1); SB();
      const v4u hsk = imma(hwt, kf, v4u{kf.z + key0, 0u, kf.w + key1, 0u}); SB();
      v4u vf = v_pack();
      const v4u hs_v = imma(hwt, vf, hsk); SB();
      const lptr kw = sl16 + w * 2048, vw = stw + w * 2048, vr = str + w * 2048;
      *(LAS v2u*)(vw + 32768 + 1024) = v2u{vf.x, vf.y}; *(LAS v2u*)(vw + 32768 + 1536) = v2u{vf.z, vf.w};
      if (!lead || w < 7) lwr(kw + 32768, kf);
      SB();
      vf.z = g3 ? (hs_v.y << 13) + hs_v.x : vf.z; vf.w = g3 ? (hs_v.w << 13) + hs_v.z : vf.w;
      __builtin_amdgcn_raw_buffer_store_b128(vf, rs_ws, vl16 + 1024, hp, SC1); keep(vf);
      STAMP(4 + layer * 12);
      v4u hk[2], hv[2];
      unsigned hs[2];
#pragma unroll
      for (int i = 0; i < 2; ++i) hs[i] = t - 2 + i >= 0 ? hb + ((t - 2 + i) * 8 + w) * 2048 : W_END;
      auto fetch = [&](int margin) INL {
        pause(lateness(hh, 2, 200, key0, srdW, vh, shh, budget) + margin);
#pragma unroll
        for (int i = 0; i < 2; ++i) { hk[i] = ld16(rs_ws, vl16, hs[i]); hv[i] = ld16(rs_ws, vl16 + 1024, hs[i]); }
      };
      if (lead) load_wo(6, 10);
      wg_bar();
      if (!lead) { fetch(T_H); __builtin_amdgcn_s_setprio(1); }
      const v4u qf = q_gemm(0.18898223650461357f * L2E, 1.f);
      if (lead) { fetch(T_HL); load_wo(10, 14); } else load_wo(6, 10);
      const float NEG = -3.0e38f;
      v4f sd = mfma(kf, qf, z4);
#pragma unroll
      for (int r = 0; r < 4; ++r) sd[r] = (4 * g + r <= c) ? sd[r] : NEG;
      float m = xmax1(mx(mx3(sd.x, sd.y, sd.z), sd.w));
      auto diag = [&]() INL {
        const v2u a0 = trd(vr + 32768 + 1024), a1 = trd(vr + 32768 + 1536);
        const v4f e = sd - m;
        const v2u pd = {pk(ex2(e.x), ex2(e.y)), pk(ex2(e.z), ex2(e.w))};
        typedef short v4h __attribute__((ext_vector_type(4)));
        o0 = __builtin_amdgcn_mfma_f32_16x16x16bf16_1k(__builtin_bit_cast(v4h, a0), __builtin_bit_cast(v4h, pd), z4, 0, 0, 0);
        o1 = __builtin_amdgcn_mfma_f32_16x16x16bf16_1k(__builtin_bit_cast(v4h, a1), __builtin_bit_cast(v4h, pd), z4, 0, 0, 0);
      };
      diag();
      auto take_halo = [&]() INL {
        STAMP(6 + layer * 12);
        const unsigned nv = min(t, 2);
        auto stage = [&]() INL {
          v4u hs_ = zu;
          unsigned e0 = 0u - nv * key0, e1 = 0u - nv * key1;
#pragma unroll
          for (int i = 0; i < 2; ++i) {
            const unsigned none = t - 2 + i < 0 ? 0xfe000000u : 0u;
            hs_ = imma(hwt, hk[i], hs_); SB();
            hs_ = imma(hwt, hv[i], hs_); SB();
            e0 += hv[i].z - hk[i].z; e1 += hv[i].w - hk[i].w; PIN(e0); PIN(e1);
            lwr(kw + i * 16384, v4u{hk[i].x, hk[i].y, hk[i].z, hk[i].w | none});
            *(LAS v2u*)(vw + i * 16384 + 1024) = v2u{hv[i].x, hv[i].y};
            *(LAS v2u*)(vw + i * 16384 + 1536) = v2u{g3 ? 0x3f80u : hv[i].z, g3 ? 0u : hv[i].w};
            SB();
          }
          return row3_differs(hs_, e0, e1);
        };
        if (__builtin_expect(stage(), 0)) {
          do reload4(hk[0], hk[1], hv[0], hv[1], srdW, vl16, hs[0], hs[1], min(hs[0] + 1024, W_END), min(hs[1] + 1024, W_END));
          while (stage() && spend(budget));
        }
        wg_bar();
        STAMP(7 + layer * 12);
      };
      {
        const v4f cm = {-m, -m, -m, -m};
        v4f sa, sb; v4u a0, a1, b0, b1, pq;
#pragma unroll
        for (int j = 7; j >= 0; --j) {
          if (j >= 4 && (j < 6) == lead && (w >> 1) == 7 - j) take_halo();
          if (j == (lead ? 4 : 6)) VMKNOWN(4);
          if (!lead && j == 6 && w == 3) hint_put(2 * layer + 1);
          if (!lead && j <= 5 && j >= 4) load_wo(10 + 2 * (5 - j), 12 + 2 * (5 - j));
          if (!lead && j == POLL_OB) oh = hint_get(vrow, w == 3 ? soh : W_END);
          const v4u ka = lrd(kw + j * 4096), kb = lrd(kw + j * 4096 + 2048);
          const v4u n0 = cat(trd(vr + j * 4096 + 1024), trd(vr + j * 4096 + 3072)), n1 = cat(trd(vr + j * 4096 + 1536), trd(vr + j * 4096 + 3584));
          SB();
          if (j < 7) {
            const float e0 = ex2(sa.x), e1 = ex2(sa.y); SB();
            if (j < 6) { o0 = mfma(b0, pq, o0); SB(); }
            const float e2 = ex2(sa.z), e3 = ex2(sa.w); SB();
            if (j < 6) { o1 = mfma(b1, pq, o1); SB(); }
            const float e4 = ex2(sb.x), e5 = ex2(sb.y); SB();
            sa = mfma(ka, qf, cm); SB();
            const float e6 = ex2(sb.z), e7 = ex2(sb.w); SB();
            sb = mfma(kb, qf, cm); SB();
            pq = v4u{pk(e0, e1), pk(e2, e3), pk(e4, e5), pk(e6, e7)};
            b0 = a0; b1 = a1;
          } else {
            sa = mfma(ka, qf, cm); sb = mfma(kb, qf, cm);
          }
          a0 = n0; a1 = n1;
          SB();
        }
        o0 = mfma(b0, pq, o0); o1 = mfma(b1, pq, o1); SB();
#pragma unroll
        for (int r = 0; r < 4; ++r) sa[r] = (4 * g + r > c) ? sa[r] : NEG;
        const v4u pf = pexp(sa, sb);
        o1 = mfma(a1, pf, o1); o0 = mfma(a0, pf, o0);
      }
      den = xrow(sel3, o1.x);
      if (__builtin_expect(__any(!(den < 1.0e18f)), 0)) {
        float mt = m;
#pragma unroll 1
        for (int k = 0; k < 16; ++k) {
          v4f s = mfma(lrd(kw + k * 2048), qf, z4);
#pragma unroll
          for (int r = 0; r < 4; ++r) s[r] = (k > 0 || 4 * g + r > c) ? s[r] : NEG;
          mt = mx3(mt, mx(s.x, s.y), mx(s.z, s.w));
        }
        m = xmax1(mt);
        diag();
        const v4f cm = {-m, -m, -m, -m};
#pragma unroll 1
        for (int j = 7; j >= 0; --j) {
          v4f sa = mfma(lrd(kw + j * 4096), qf, cm);
          const v4f sb = mfma(lrd(kw + j * 4096 + 2048), qf, cm);
#pragma unroll
          for (int r = 0; r < 4; ++r) sa[r] = (j > 0 || 4 * g + r > c) ? sa[r] : NEG;
          const v4u pf = pexp(sa, sb);
          o0 = mfma(cat(trd(vr + j * 4096 + 1024), trd(vr + j * 4096 + 3072)), pf, o0);
          o1 = mfma(cat(trd(vr + j * 4096 + 1536), trd(vr + j * 4096 + 3584)), pf, o1);
        }
        den = xrow(sel3, o1.x);
      }
    }
    const unsigned ob = W_O + layer * 2097152 + t * 131072;
    {
      const float rd = __builtin_amdgcn_rcpf(den);
      o0 *= rd; o1 *= rd;
      v4u v = {pk(o0.x, o0.y), pk(o0.z, o0.w), pk(o1.x, o1.y), pk(o1.z, o1.w)};
      const v4u hs = imma(hwt, v, kc);
      v.z = g3 ? (hs.y << 13) + hs.x : v.z; v.w = g3 ? (hs.w << 13) + hs.z : v.w;
      __builtin_amdgcn_raw_buffer_store_b128(v, rs_ws, vl16, ob + (w * 16 + h) * 1024, SC1);
      keep(v);
    }
    __builtin_amdgcn_s_setprio(0);
    STAMP(8 + layer * 12);
    auto swap_kv = [&](int j0, int j1) INL {
#pragma unroll
      for (int j = j0; j < j1 && j < (lead ? 3 : 4); ++j) {
        const lptr a = lbase(w * 2048 + j * 16384 + vl16);
        v4u t0, t1;
        if (layer < 2) { t0 = lrd(a); t1 = lrd(a + 1024); }
        if (!first) { lwr(a, park[2 * j]); lwr(a + 1024, park[2 * j + 1]); }
        if (layer < 2) { park[2 * j] = t0; park[2 * j + 1] = t1; }
      }
      if (first) {
        asm volatile("s_waitcnt lgkmcnt(0)" ::: "memory");
#pragma unroll
        for (int j = j0; j < j1 && j < (lead ? 3 : 4); ++j) dma2s(w * 2048 + j * 16384, vl16, srdW, ptA + 16 * 86016 + j * 16384);
      }
    };
    if (first) {
      const v4u xr = swap16(park[0]);
      res0 = v4f{asf(xr.x << 16), asf(xr.x & 0xffff0000u), asf(xr.y << 16), asf(xr.y & 0xffff0000u)};
      res1 = v4f{asf(xr.z << 16), asf(xr.z & 0xffff0000u), asf(xr.w << 16), asf(xr.w & 0xffff0000u)};
    }
    unsigned op = ob + (2 * (h >> 2) + (lead ? 0 : 1)) * 16384 + (w & 3) * 1024;
    asm volatile("" : "+s"(op));
    v4u of[4];
    if (isA) swap_kv(0, 2);
    if (lead == isA && w == (isA ? 7 : 3)) *(LAS int*)(csg + 768) = lateness(oh, h, isA ? 165 : 125, key0, srdW, vrow, soh, budget);
    wg_bar();
    const int olate = __builtin_amdgcn_readfirstlane(*(LAS int*)(csg + 768));
    if (!isA && more) swap_kv(0, 2);
    pause(olate + (isA ? T_OA : T_OB) + (more ? 0 : 6));
#pragma unroll
    for (int i = 0; i < 4; ++i) of[i] = ld16(rs_ws, vl16, op + i * 4096);
    if (more && !first) swap_kv(2, 4);
    const unsigned qd = 7 * (w & 3) + (vl16 >> 8), qa = L_SCR + (lead ? 0 : 14336) + (qd >> 1) * 256 + (qd & 1) * 8 + (vl16 & 255);
    const lptr lo = lbase(qa), hi = lbase(g3 ? L_SCR + 32768 + vl16 : qa + 512);
    STAMP(9 + layer * 12);
    auto stage_o = [&]() INL {
      auto put = [&]() INL {
        v4u hs = zu;
        unsigned e0 = 0u - 4u * key0, e1 = 0u - 4u * key1;
#pragma unroll
        for (int i = 0; i < 4; ++i) {
          hs = imma(hwt, of[i], hs); SB();
          *(LAS v2u*)(lo + i * 3584) = v2u{of[i].x, of[i].y};
          *(LAS v2u*)(hi + i * 3584) = v2u{of[i].z, of[i].w};
          e0 += of[i].z; e1 += of[i].w; PIN(e0); PIN(e1);
          SB();
        }
        return row3_differs(hs, e0, e1);
      };
      if (__builtin_expect(put(), 0)) {
        do reload4(of[0], of[1], of[2], of[3], srdW, vl16, op, op + 4096, op + 8192, op + 12288);
        while (put() && spend(budget));
      }
    };
    stage_o();
    wg_bar();
    if (first) swap_kv(2, 4);
    const unsigned sx = W_HINT + (8 + layer) * 32768 + t * 2048;
    if (lead && w == 7) {
      if (more) {
        hint_put(8 + layer);
        __builtin_amdgcn_s_sleep(P_X);
        *(LAS int*)(csg + 832) = lateness(hint_get(vrow, sx), h, 70, key0, srdW, vrow, sx, budget);
      }
    }
    int w2 = w;
    asm volatile("" : "+s"(w2));
    if (!lead || w2 != 7) {
      const unsigned won = wos + (isA ? 28 * 14336 : 0);
      v4u bq[8];
#pragma unroll
      for (int i = 0; i < 4; ++i) { bq[2 * i] = lrd(sl16 + i * 1024); SB(); bq[2 * i + 1] = lrd(sl16 + 14336 + i * 1024); SB(); }
#pragma unroll
      for (int s = 0; s < 14; ++s) {
        const int i = (2 * s) & 7;
        if (!(s & 1)) { if (s < 12) LGKNOWN(4); else LGKNOWN(0); SB(); }
        res0 = mfma(wof[s], bq[i], res0); SB(); if (s + 4 < 14) { bq[i] = lrd(sl16 + (s + 4) * 1024); SB(); }
        res1 = mfma(wof[s], bq[i + 1], res1); SB(); if (s + 4 < 14) { bq[i + 1] = lrd(sl16 + 14336 + (s + 4) * 1024); SB(); }
        if (more && s >= 1 && (s - 1) % WOS == 0 && (s - 1) / WOS < 6) { const int n = (s - 1) / WOS; wof[n] = ld16(rs_ws, vl16 + (n & 3) * 1024, won + (n >> 2) * 4096); }
      }
      const v4u xo = swap16(v4u{pk(res0.x, res0.y), pk(res0.z, res0.w), pk(res1.x, res1.y), pk(res1.z, res1.w)});
      if (more) {
        __builtin_amdgcn_raw_buffer_store_b128(xo, rs_ws, pubx, W_X + (layer + 1) * TILEBUF + tp, SC1);
        keep(xo);
        auto r = __builtin_amdgcn_permlane32_swap(xo.x + xo.z, xo.y + xo.w, false, false);
        const unsigned r0 = r[0], r1 = r[1];
        *(LAS v2u*)(lbase(L_SCR + 28672 + w * 512 + (vl16 >> 1))) = v2u{r0 + r1, asu(sumsq8(xo))};
      } else {
        const unsigned lo = lane_id();
        __builtin_amdgcn_raw_buffer_store_b128(xo, __builtin_amdgcn_make_buffer_rsrc(p.out, 0, SEQ * DM * 2, 0x00020000),
                                               (lo & 31) * (DM * 2) + (2 * ft + (lo >> 5)) * 16, (t * 128 + (h >> 2) * 32) * (DM * 2), 0);
        keep(xo);
      }
    }
    STAMP(10 + layer * 12);
    if (more) {
      if (first) VMWAIT(7);
      wg_bar();
      const int late = __builtin_amdgcn_readfirstlane(*(LAS int*)(csg + 832));
      if (lead && w2 == 7) {
        float q = 0.f;
        unsigned a = 0u;
        const lptr sp = lbase(L_SCR + 28672 + (vl16 >> 1));
#pragma unroll
        for (int i = 0; i < 7; ++i) { const v2u v = *(LAS v2u*)(sp + i * 512); a += v.x; q += asf(v.y); }
        auto r = __builtin_amdgcn_permlane32_swap(asu(q), asu(q), false, false);
        auto ra = __builtin_amdgcn_permlane32_swap(a, a, false, false);
        const unsigned r0 = r[0], r1 = r[1], a0 = ra[0], a1 = ra[1], qs = asu(asf(r0) + asf(r1));
        const v4u rec = {qs, key0 - qs - a0, key1 - a1, 0u};
        __builtin_amdgcn_raw_buffer_store_b128(rec, rs_ws, vl16 < 512 ? (vl16 >> 8) * 1024 + (vl16 & 255) : OOB,
                                               W_ST + (layer + 1) * 131072 + (t * 8 + 2 * (h >> 2)) * 1024 + (h & 3) * 256, SC1);
        keep(rec);
        if (isA) hint_put(2 * layer + 2, max(late, 0));
        xwait = late + T_X - 12;
      } else {
        xwait = late + T_X;
      }
    }
  };
  auto finish = [&]() INL {
  };
  if (w_ >= 4) {
    run_layer(Layer<0>{}, Layer<1>{}); run_layer(Layer<1>{}, Layer<1>{}); run_layer(Layer<2>{}, Layer<1>{}); run_layer(Layer<3>{}, Layer<1>{});
    if (w_ == 7 && b == 0) __builtin_amdgcn_raw_buffer_store_b32(epoch, rs_ws, 0, W_KEY, 0);
#pragma unroll
    for (int s = 0; s < 14; ++s) asm volatile("" : "=v"(xf[s]));
    asm volatile("" : "=v"(k0), "=v"(k1), "=v"(v0), "=v"(v1), "=v"(ck0), "=v"(ck1), "=v"(cv0), "=v"(cv1), "=v"(var), "=v"(wphi[0]), "=v"(wphi[1]), "=v"(vres0));
  }
  int wz = w_;
  asm volatile("" : "+s"(wz));
  if (wz < 4) {
    run_layer(Layer<0>{}, Layer<0>{}); run_layer(Layer<1>{}, Layer<0>{}); run_layer(Layer<2>{}, Layer<0>{}); run_layer(Layer<3>{}, Layer<0>{});
  }
  finish();
}
}

extern "C" void kernel_launch(void* const* d_in, const int* in_sizes, int n_in, void* d_out, int out_size, void* d_ws, size_t ws_size,
                              hipStream_t stream) {
  (void)in_sizes; (void)n_in; (void)out_size; (void)ws_size;
  (void)hipFuncSetAttribute((const void*)fused_layers, hipFuncAttributeMaxDynamicSharedMemorySize, (int)L_TOTAL);
  Params p;
  p.x = d_in[0]; p.wA = d_in[1]; p.wphi = d_in[2]; p.woA = d_in[3]; p.wB = d_in[5]; p.woB = d_in[6]; p.out = d_out; p.ws = d_ws;
  hipLaunchKernelGGL(fused_layers, dim3(256), dim3(512), L_TOTAL, stream, p);
}
```

```cpp
#include <hip/hip_runtime.h>
#include <stdint.h>

#define LAS __attribute__((address_space(3)))
#define DEV __device__ __forceinline__
typedef unsigned v4u __attribute__((ext_vector_type(4)));
typedef unsigned v2u __attribute__((ext_vector_type(2)));
typedef float v4f __attribute__((ext_vector_type(4)));
typedef float v2f __attribute__((ext_vector_type(2)));
typedef int v4i __attribute__((ext_vector_type(4)));
typedef short v4s __attribute__((ext_vector_type(4)));
typedef __bf16 v8bf __attribute__((ext_vector_type(8)));
typedef __bf16 v2bf __attribute__((ext_vector_type(2)));
typedef __amdgpu_buffer_rsrc_t rsrc_t;

#define TPX 4
#define DLY_T 3
#define WO1 6
#define POLL_OB 1
#define T_KV 4
#define T_H 24
#define T_OA 0
#define T_OB 12
#define P_X 11
#define T_X 8
#define T_XL 0
#define T_OT 20
#define PRI0 1
#define RAMP 0
#define WOS 1
#define T_HL -4
#define P_F 440
#define BOFF 4
#define NW0 2
#define KVE 1
#define PBACK 99
#define ILV 1
#define WIN 5

namespace {
constexpr int SEQ = 2048, DM = 448;
constexpr unsigned L_WK = 0, L_WV = 28672, L_WQ = 57344, L_WQB = 86016, L_SCR = 114688, L_TOTAL = 163840;
constexpr unsigned L_CS = L_SCR + 23 * 2048;
constexpr unsigned L_KVF = L_SCR + 40960;
constexpr unsigned TILEBUF = 16 * 8 * 14 * 1024;
constexpr unsigned W_KEY = 0, W_HINT = 4096, W_FLAG = W_HINT + 11 * 32768, W_ST = W_HINT + 12 * 32768, W_KVP = W_ST + 4 * 131072, W_O = W_KVP + 2 * 1048576,
                   W_HALO = W_O + 4 * 2097152, W_X = W_HALO + 2 * 4194304, W_PT = W_X + 4 * TILEBUF, W_WO = W_PT + 2 * 16 * 86016,
                   W_CS = W_WO + 2 * 28 * 14336, W_END = W_CS + 16 * 56 * 128;
constexpr unsigned W_DBG = 64u << 20;
constexpr int SC1 = 16;
constexpr unsigned OOB = 0x40000000u;

DEV v4f mfma(v4u a, v4u b, v4f c) {
  return __builtin_amdgcn_mfma_f32_16x16x32_bf16(__builtin_bit_cast(v8bf, a), __builtin_bit_cast(v8bf, b), c, 0, 0, 0);
}
DEV unsigned pk(float a, float b) { v2f f = {a, b}; v2bf r = __builtin_convertvector(f, v2bf); return __builtin_bit_cast(unsigned, r); }
typedef LAS char* lptr;
DEV lptr lbase(unsigned v) { asm volatile("" : "+v"(v)); return (lptr)(size_t)v; }
DEV v2u trd(lptr a) { v4s r = __builtin_amdgcn_ds_read_tr16_b64_v4i16((LAS v4s*)a); return __builtin_bit_cast(v2u, r); }
DEV v4u cat(v2u a, v2u b) { return v4u{a.x, a.y, b.x, b.y}; }
DEV v4u lrd(lptr a) { return *(LAS v4u*)a; }
DEV void lwr(lptr a, v4u v) { *(LAS v4u*)a = v; }
#define VMWAIT(n) asm volatile("s_waitcnt vmcnt(" #n ")" ::: "memory")
DEV float asf(unsigned u) { return __builtin_bit_cast(float, u); }
DEV unsigned asu(float f) { return __builtin_bit_cast(unsigned, f); }
DEV void wg_bar() { asm volatile("s_waitcnt lgkmcnt(0)\n\ts_barrier" ::: "memory"); }
DEV void drain() { asm volatile("s_waitcnt vmcnt(0)" ::: "memory"); }
DEV void fence() { asm volatile("" ::: "memory"); }
DEV float ex2(float x) { return __builtin_amdgcn_exp2f(x); }

DEV unsigned lane_id() { unsigned r; asm volatile("v_mbcnt_lo_u32_b32 %0, -1, 0\n\tv_mbcnt_hi_u32_b32 %0, -1, %0" : "=v"(r)); __builtin_assume(r < 64); return r; }
DEV float mx(float a, float b) { return __builtin_elementwise_maximum(a, b); }
DEV float mn(float a, float b) { return __builtin_elementwise_minimum(a, b); }
DEV float mx3(float a, float b, float c) { return mx(mx(a, b), c); }
DEV float mn3(float a, float b, float c) { return mn(mn(a, b), c); }
DEV float xrow(float sel, float v) { const v4f z = {0.f, 0.f, 0.f, 0.f}; const v4f r = __builtin_amdgcn_mfma_f32_16x16x4f32(sel, v, z, 0, 0, 0); return r.x; }
DEV v4f xrow4(float sel, float v) { const v4f z = {0.f, 0.f, 0.f, 0.f}; return __builtin_amdgcn_mfma_f32_16x16x4f32(sel, v, z, 0, 0, 0); }
DEV v4u swap16(v4u v) {
  auto r = __builtin_amdgcn_permlane16_swap(v.x, v.z, false, false);
  auto q = __builtin_amdgcn_permlane16_swap(v.y, v.w, false, false);
  const unsigned r0 = r[0], r1 = r[1], q0 = q[0], q1 = q[1];
  return v4u{r0, q0, r1, q1};
}
DEV float sumsq8(v4u v) {
  const v4f lo = __builtin_bit_cast(v4f, v << 16), hi = __builtin_bit_cast(v4f, v & 0xffff0000u), q = lo * lo + hi * hi;
  return (q.x + q.y) + (q.z + q.w);
}
DEV void xmax2(float& a, float& b) {
  auto r = __builtin_amdgcn_permlane32_swap(asu(a), asu(b), false, false);
  unsigned r0 = r[0], r1 = r[1];
  float z = mx(asf(r0), asf(r1));
  auto q = __builtin_amdgcn_permlane16_swap(asu(z), asu(z), false, false);
  unsigned q0 = q[0], q1 = q[1];
  z = mx(asf(q0), asf(q1));
  auto p = __builtin_amdgcn_permlane32_swap(asu(z), asu(z), false, false);
  unsigned p0 = p[0], p1 = p[1];
  a = asf(p0); b = asf(p1);
}
DEV float xmax1(float a) {
  auto r = __builtin_amdgcn_permlane32_swap(asu(a), asu(a), false, false);
  unsigned r0 = r[0], r1 = r[1];
  float z = mx(asf(r0), asf(r1));
  auto q = __builtin_amdgcn_permlane16_swap(asu(z), asu(z), false, false);
  unsigned q0 = q[0], q1 = q[1];
  return mx(asf(q0), asf(q1));
}
DEV void dma2(unsigned ldsdst, unsigned va, unsigned vb, v4u srd, unsigned sa, unsigned sb) {
  asm volatile("s_mov_b32 m0, %0\n\ts_nop 3\n\tbuffer_load_dwordx4 %1, %3, %4 offen lds\n\tbuffer_load_dwordx4 %2, %3, %5 offen offset:1024 lds"
               :: "s"(ldsdst), "v"(va), "v"(vb), "s"(srd), "s"(sa), "s"(sb) : "memory");
}
DEV void keep(v4u v) { asm volatile("s_nop 1" :: "v"(v) : "memory"); }
DEV void keep(v4u v, v4u u) { asm volatile("s_nop 1" :: "v"(v), "v"(u) : "memory"); }
DEV void dma2s(unsigned ldsdst, unsigned v, v4u srd, unsigned so) {
  asm volatile("s_mov_b32 m0, %0\n\ts_nop 3\n\tbuffer_load_dwordx4 %1, %2, %3 offen sc1 lds\n\tbuffer_load_dwordx4 %1, %2, %3 offen offset:1024 sc1 lds"
               :: "s"(ldsdst), "v"(v), "s"(srd), "s"(so) : "memory");
}
DEV v4u ld16(rsrc_t rs, unsigned v, unsigned so) {
  const v4u r = __builtin_amdgcn_raw_buffer_load_b128(rs, v, so, SC1);
  __builtin_amdgcn_sched_barrier(0);
  return r;
}
DEV v4u mk_srd(const void* p, unsigned bytes) {
  unsigned long long a = (unsigned long long)p;
  return v4u{(unsigned)a, (unsigned)(a >> 32) & 0xffffu, bytes, 0x00020000u};
}
DEV v4u imma(v4u a, v4u b, v4u c) {
  const v4i r = __builtin_amdgcn_mfma_i32_16x16x64_i8(__builtin_bit_cast(v4i, a), __builtin_bit_cast(v4i, b), __builtin_bit_cast(v4i, c), 0, 0, 0);
  return __builtin_bit_cast(v4u, r);
}
DEV v4u weights(unsigned lo) {
  const unsigned a = (lo + 1) * 0x9e3779b1u, b = (a ^ (a >> 15)) * 0x85ebca77u, c = (b ^ (b >> 13)) * 0xc2b2ae3du, d = (c ^ (c >> 16)) * 0x27d4eb2fu;
  return v4u{(a ^ (a >> 16)) | 0x01010101u, (b ^ (b >> 16)) | 0x01010101u, (c ^ (c >> 16)) | 0x01010101u, (d ^ (d >> 16)) | 0x01010101u};
}
DEV void keys(unsigned e, unsigned& k0, unsigned& k1) { k0 = e * 0x9e3779b1u + 0x7f4a7c15u; k1 = e * 0x85ebca77u + 0x165667b1u; }
DEV unsigned xsum2u(unsigned a, unsigned b) {
  auto r = __builtin_amdgcn_permlane32_swap(a, b, false, false);
  const unsigned r0 = r[0], r1 = r[1], z = r0 + r1;
  auto q = __builtin_amdgcn_permlane16_swap(z, z, false, false);
  const unsigned q0 = q[0], q1 = q[1];
  return q0 + q1;
}
DEV void reload4(v4u& a, v4u& b, v4u& c, v4u& d, v4u srd, unsigned voff, unsigned sa, unsigned sb, unsigned sc, unsigned sd) {
  asm volatile("s_sleep 2\n\tbuffer_load_dwordx4 %0, %4, %5, %6 offen sc1\n\tbuffer_load_dwordx4 %1, %4, %5, %7 offen sc1\n\t"
               "buffer_load_dwordx4 %2, %4, %5, %8 offen sc1\n\tbuffer_load_dwordx4 %3, %4, %5, %9 offen sc1\n\ts_waitcnt vmcnt(0)"
               : "+v"(a), "+v"(b), "+v"(c), "+v"(d) : "v"(voff), "s"(srd), "s"(sa), "s"(sb), "s"(sc), "s"(sd) : "memory");
}
DEV bool spend(int& budget) { budget = max(budget - 1, 0); return budget != 0; }
DEV bool nonzero(unsigned v, unsigned long long lanes = ~0ull) { return (__builtin_amdgcn_uicmp(v, 0u, 33) & lanes) != 0ull; }
DEV bool row3_differs(v4u hs, unsigned e0, unsigned e1) {
  const unsigned long long m = __builtin_amdgcn_uicmp((hs.y << 13) + hs.x, e0, 33) | __builtin_amdgcn_uicmp((hs.w << 13) + hs.z, e1, 33);
  unsigned hi = (unsigned)(m >> 32);
  asm volatile("" : "+s"(hi));
  return (hi >> 16) != 0u;
}
DEV bool retry(bool bad, int& budget) {
  if (__builtin_expect(!__any(bad), 1)) return false;
  budget = max(budget - 1, 0);
  return __any(budget != 0);
}

DEV unsigned clock32() { unsigned long long t; asm volatile("s_memrealtime %0\n\ts_waitcnt lgkmcnt(0)" : "=s"(t) :: "memory"); return (unsigned)t; }
DEV int lateness(v2u v, int own, int seg, unsigned key, v4u srd, unsigned voff, unsigned soff, int& budget) {
  asm volatile("" : "+v"(v) :: "memory");
  int over = 0;
  if (__builtin_expect(nonzero(v.y - key, 0xffffull), 0)) {
    do {
      asm volatile("s_sleep %4\n\tbuffer_load_dwordx2 %0, %1, %2, %3 offen sc1\n\ts_waitcnt vmcnt(0)" : "+v"(v) : "v"(voff), "s"(srd), "s"(soff), "n"(BOFF) : "memory");
    } while (nonzero(v.y - key, 0xffffull) && spend(budget));
    const unsigned tv = v.x;
    over = max((int)(clock32() - __builtin_amdgcn_readlane(tv, own)) - seg, 0);
  }
  const unsigned tv = v.x;
  int d = (int)(tv - __builtin_amdgcn_readlane(tv, own));
  d = max(d, __builtin_amdgcn_update_dpp(0, d, 0x128, 0xf, 0xf, false));
  d = max(d, __builtin_amdgcn_update_dpp(0, d, 0x124, 0xf, 0xf, false));
  d = max(d, __builtin_amdgcn_update_dpp(0, d, 0x122, 0xf, 0xf, false));
  d = max(d, __builtin_amdgcn_update_dpp(0, d, 0x121, 0xf, 0xf, false));
  return min(__builtin_amdgcn_readfirstlane(d), 1000) - over;
}
DEV void pause(int ticks) {
  asm volatile("s_cmp_lt_i32 %0, 1\n\ts_cbranch_scc1 2f\n1:\n\ts_sleep 1\n\ts_sub_i32 %0, %0, 4\n\ts_cmp_gt_i32 %0, 0\n\ts_cbranch_scc1 1b\n2:" : "+s"(ticks) :: "memory", "scc");
}

template <class F>
DEV void feature_map(v4u vf, const v4u (&wphi)[2], v4u& plus, v4u& minus, F mid) {
  const v4f z4 = {0.f, 0.f, 0.f, 0.f};
  const v4f p0 = mfma(wphi[0], vf, z4), p1 = mfma(wphi[1], vf, z4);
  float hi = mx3(mx3(mx3(p0.x, p0.y, p0.z), p0.w, p1.x), p1.y, p1.z);
  float lo = -mn3(mn3(mn3(p0.x, p0.y, p0.z), p0.w, p1.x), p1.y, p1.z);
  xmax2(hi, lo);
  mid();
  const v2f p01 = {p0.x, p0.y}, p23 = {p0.z, p0.w}, p45 = {p1.x, p1.y}, h2 = {hi, hi}, l2 = {lo, lo};
  v2f a01 = p01 - h2, a23 = p23 - h2, a45 = p45 - h2, b01 = p01 + l2, b23 = p23 + l2, b45 = p45 + l2;
  asm volatile("" : "+v"(a01), "+v"(a23), "+v"(a45), "+v"(b01), "+v"(b23), "+v"(b45));
  const v2f e01 = {ex2(a01.x), ex2(a01.y)}, e23 = {ex2(a23.x), ex2(a23.y)}, e45 = {ex2(a45.x), ex2(a45.y)};
  const float e6 = ex2(p1.z - hi);
  const v2f se = (e01 + e23) + e45;
  v4f te = xrow4(1.f, (se.x + e6) + se.y);
  __builtin_amdgcn_sched_barrier(0);
  const v2f f01 = {ex2(-b01.x), ex2(-b01.y)}, f23 = {ex2(-b23.x), ex2(-b23.y)}, f45 = {ex2(-b45.x), ex2(-b45.y)};
  const float f6 = ex2(-(p1.z + lo));
  const v2f sf = (f01 + f23) + f45;
  v4f tf = xrow4(1.f, (sf.x + f6) + sf.y);
  __builtin_amdgcn_sched_barrier(0);
  const float re = __builtin_amdgcn_rcpf(te.x);
  asm volatile("" :: "v"(te));
  const v2f x01 = e01 * re, x23 = e23 * re, x45 = e45 * re;
  plus = v4u{pk(x01.x, x01.y), pk(x23.x, x23.y), pk(x45.x, x45.y), pk(e6 * re, 0.f)};
  __builtin_amdgcn_sched_barrier(0);
  const float rf = __builtin_amdgcn_rcpf(tf.x);
  asm volatile("" :: "v"(tf));
  const v2f y01 = f01 * rf, y23 = f23 * rf, y45 = f45 * rf;
  minus = v4u{pk(y01.x, y01.y), pk(y23.x, y23.y), pk(y45.x, y45.y), pk(f6 * rf, 0.f)};
}

DEV v4u pexp(v4f a, v4f b) {
  return v4u{pk(ex2(a.x), ex2(a.y)), pk(ex2(a.z), ex2(a.w)), pk(ex2(b.x), ex2(b.y)), pk(ex2(b.z), ex2(b.w))};
}
#define VMKNOWN(n) __builtin_amdgcn_s_waitcnt(((n) & 15) | (((n) >> 4) << 14) | 0xf70)
#define LGKNOWN(n) __builtin_amdgcn_s_waitcnt(0xc07f | ((n) << 8))
#define PIN(x) asm volatile("" : "+v"(x))
#define SB() __builtin_amdgcn_sched_barrier(0)
#define INL __attribute__((always_inline))

#define STAMP(i) do {} while (0)

template <int N> struct Layer { static constexpr int value = N; };
struct Params {
  const void* x; const void* wA; const void* wphi; const void* woA; const void* wB; const void* woB; void* out; void* ws;
};

__global__ void __launch_bounds__(512, 2) fused_layers(Params) {
  typedef unsigned long long v8p __attribute__((ext_vector_type(8)));
  v8p args;
  asm volatile("s_load_dwordx16 %0, %1, 0x0" : "=s"(args) : "s"(__builtin_amdgcn_kernarg_segment_ptr()));
  unsigned long long born;
  asm volatile("s_memrealtime %0" : "=s"(born));
  const int b = blockIdx.x, xg = b & 7, xi = b >> 3;
  constexpr int HPX = 32 / TPX, NGT = 16 / TPX;
  const int t = (xg % NGT) * TPX + xi / HPX;
  const int h = (xg / NGT) * HPX + xi % HPX;
  const int wid = __builtin_amdgcn_readfirstlane(threadIdx.x >> 6) & 7;
  const int w_ = wid < 4 ? 7 - wid : wid - 4;
  const int l = threadIdx.x & 63, c = l & 15, g = l >> 4;
  const bool g3 = g == 3;
  STAMP(0);
  constexpr unsigned L_XS = 73728, L_JOB = L_WQ, L_CSP = L_XS + 81920;
  const unsigned chunk16 = ((l & 3) ^ ((l >> 5) << 1)) * 16, chunk56 = chunk16 == 48 ? 40 : chunk16, vqkv = (l >> 2) * (3 * DM * 2) + chunk56;
  const int kvp_ = (w_ >> 1) & 1, kvs = w_ & 1;
  const unsigned kvat = kvp_ * L_WV + kvs * 2048, kvso = kvs * (32 * 3 * DM * 2) + (kvp_ + 1) * DM * 2 + 56 * h;
  const unsigned xsb = L_XS + w_ * 10240, xso = (t * 128 + w_ * 16) * (DM * 2);
  const unsigned vx0 = (l >> 3) * (DM * 2) + ((l & 7) ^ (l >> 4)) * 16, vx1 = ((l >> 3) + 8) * (DM * 2) + ((l & 7) ^ (4 + (l >> 4))) * 16 - 1024;
  const bool job = t < 14 && (w_ < 4 || (w_ < 6 && h < 14));
  const unsigned jat = L_JOB + w_ * 2048;
  asm volatile("" :: "v"(vqkv), "v"(vx0), "v"(vx1), "s"(kvat), "s"(kvso), "s"(xsb), "s"(xso), "s"(jat));
  asm volatile("s_waitcnt lgkmcnt(0)" : "+s"(args), "+s"(born));
  const Params p = {(const void*)args[0], (const void*)args[1], (const void*)args[2], (const void*)args[3], (const void*)args[4], (const void*)args[5], (void*)args[6], (void*)args[7]};
  const v4u srdA = mk_srd(p.wA, DM * 3 * DM * 2), srdX = mk_srd(p.x, SEQ * DM * 2);
  auto kvblock = [&](int j) INL { const unsigned so = kvso + j * (64 * 3 * DM * 2); dma2(kvat + j * 4096, vqkv, vqkv, srdA, so, so + 16 * 3 * DM * 2 - 1024); };
  auto xline = [&](int j, int up) INL {
    const unsigned so = xso + up * (64 * DM * 2) + j * 128;
    dma2(xsb + up * 40960 + (j % 5) * 2048, vx0, vx1, srdX, so, so);
  };
  if (w_ < 4) {
    __builtin_amdgcn_s_setprio(PRI0);
    const unsigned so = t * (32 * 3 * DM * 2) + (w_ == 3 ? 0 : w_ * DM * 2) + 56 * (h ^ 1);
    if (job) dma2(jat, vqkv, vqkv, mk_srd(w_ ? p.wB : p.wA, DM * 3 * DM * 2), so, so + 16 * 3 * DM * 2 - 1024);
    xline(0, 0); xline(0, 1);
  } else {
    if (job) {
      const unsigned v = (l >> 2) * (DM * 2) + chunk16, so = t * (32 * DM * 2) + 64 * h;
      dma2(jat, v, v, mk_srd(w_ == 5 ? p.woB : p.woA, DM * DM * 2), so, so + 16 * DM * 2 - 1024);
    } else if (w_ == 7) {
      const unsigned e = (l >> 2) & 7, g0 = l >> 5, d0 = e < 4 ? 4 * g0 + e : 12 + 3 * g0 + e, d1 = d0 + (e < 4 ? 8 : 6);
      dma2(jat, e < 7 ? d0 * 56 + chunk56 + 1024 : OOB, e < 7 ? d1 * 56 + chunk56 : OOB,
           mk_srd((const char*)p.wphi - 1024, 16 * 28 * 28 * 2 + 1024), h * 28 * 28 * 2, h * 28 * 28 * 2);
    }
    kvblock(0); kvblock(1);
  }
  unsigned epoch, key0, key1, keyk;
  asm volatile("s_load_dword %0, %1, 0x0" : "=s"(epoch) : "s"(p.ws));
  const rsrc_t rs_ws = __builtin_amdgcn_make_buffer_rsrc(p.ws, 0, W_END, 0x00020000);
  const v4u srdW = mk_srd(p.ws, W_END);
  unsigned vl16 = l * 16;
  asm volatile("" : "+v"(vl16));
  const int ft = 7 * (h & 3) + min(w_, 6), nx = 2 * ft + (g >> 1);
  const unsigned pubx = w_ == 7 ? OOB : (g & 1) * 14336 + (nx >> 2) * 1024 + ((nx & 3) * 16 + c) * 16;
  const unsigned tw = (t * 8 + w_) * 14336, tp = (t * 8 + 2 * (h >> 2)) * 14336;
  const unsigned ptA = W_PT + h * 86016 + w_ * 2048;
  int budget = 1 << 16;
  v4u wphi[2];
  v4u park[8];
  v4u xf[14];
  v4f k0, k1, v0, v1, ck0, ck1, cv0, cv1;
  float var;
  v4f res0, res1;
  auto hint_put = [&](int slot, int ahead = 0) INL {
    __builtin_amdgcn_raw_buffer_store_b64(v2u{clock32() + ahead, key0}, rs_ws, vl16 < 256 ? vl16 >> 1 : OOB, W_HINT + slot * 32768 + (t * 16 + h) * 128, SC1);
  };
  auto hint_get = [&](unsigned v, unsigned so) INL { SB(); const v2u r = __builtin_amdgcn_raw_buffer_load_b64(rs_ws, v, so, SC1); SB(); return r; };
  const unsigned q4 = (l & 15) >> 2, p4 = l & 3;
  const lptr l16 = (lptr)(size_t)vl16, lq16 = lbase(L_WQ + l * 16);
  const lptr stw = lbase(L_SCR + c * 32 + ((g ^ (c >> 2)) * 8));
  const lptr str = lbase(L_SCR + (4 * g + q4) * 32 + ((p4 ^ g) * 8));
  const lptr sl16 = lbase(L_SCR + l * 16), csg = lbase(L_CS + g * 16);
  const unsigned vcol = l < 16 ? l * 2048 : OOB, vrow = l < 16 ? l * 128 : OOB;
  const float sel3 = g3 ? 1.f : 0.f;
  v4u hwt = weights(l);
  if (g3) { hwt.z = 0u; hwt.w = 0u; }
  const v4u zu = {0u, 0u, 0u, 0u};

  v4u wof[14];
  const unsigned wos = W_WO + ft * 14336;
  auto kv_gemm = [&](const int s0, const int s1, auto step) INL {
    const v4f z4 = {0.f, 0.f, 0.f, 0.f};
    if (s0 == 0) { k0 = z4; k1 = z4; v0 = z4; v1 = z4; }
    v4u a[8];
#pragma unroll
    for (int i = 0; i < 2; ++i) { const int o = (s0 + i) * 2048; a[4 * i] = lrd(l16 + o); a[4 * i + 1] = lrd(l16 + o + 1024); a[4 * i + 2] = lrd(l16 + L_WV + o); a[4 * i + 3] = lrd(l16 + L_WV + o + 1024); SB(); }
#pragma unroll
    for (int s = s0; s < s1; ++s) {
      const int i = 4 * (s & 1), o = (s + 2) * 2048;
      if (s < s1 - 1) LGKNOWN(4); else LGKNOWN(0);
      SB();
      k1 = mfma(a[i + 1], xf[s], k1); SB();
      step(s);
      if (s + 2 < s1) { a[i + 1] = lrd(l16 + o + 1024); SB(); }
      k0 = mfma(a[i], xf[s], k0); SB(); if (s + 2 < s1) { a[i] = lrd(l16 + o); SB(); }
      v0 = mfma(a[i + 2], xf[s], v0); SB(); if (s + 2 < s1) { a[i + 2] = lrd(l16 + L_WV + o); SB(); }
      v1 = mfma(a[i + 3], xf[s], v1); SB(); if (s + 2 < s1) { a[i + 3] = lrd(l16 + L_WV + o + 1024); SB(); }
    }
  };
  v4u hwk = weights(l);
  hwk.w &= 0xffffu;
  asm volatile("" : "+v"(hwk.z), "+v"(hwk.w), "+v"(hwt));
  v4u fl0, fl1;
  auto flags_wait = [&](v2u f, unsigned v, unsigned so, unsigned long long lanes) INL {
    while (nonzero((f.x ^ key0) | (f.y ^ key1), lanes) && spend(budget))
      asm volatile("s_sleep 3\n\tbuffer_load_dwordx2 %0, %1, %2, %3 offen sc1\n\ts_waitcnt vmcnt(0)" : "+v"(f) : "v"(v), "s"(srdW), "s"(so) : "memory");
  };
  unsigned vres0;
  {
    unsigned a0 = (8 * g + (c >> 2)) * 64 + ((((l & 3) >> 1) ^ ((g & 1) << 1)) * 16) + (l & 1) * 8;
    unsigned a1 = (a0 ^ 32) ^ ((l & 2) << 2);
    unsigned sl3 = c >= 12 ? 4 * c - 45 : c + ((c * 11) >> 5);
    const unsigned one = 0x3f803f80u;
    v4u one8 = {one, one, one, one};
    const v4f z4 = {0.f, 0.f, 0.f, 0.f};
    auto turn = [&](lptr r0, lptr r1, bool pad, v4u& f0, v4u& f1) INL {
      f0 = cat(trd(r0), trd(r0 + 256)); f1 = cat(trd(r1), trd(r1 + 256));
      f1 = v4u{pad ? one : f1.x, pad ? one : f1.y, pad ? one : f1.z, pad ? one : f1.w};
    };
    const unsigned xa = xsb + c * 128 + (g ^ (c >> 1)) * 16;
    const lptr xa0 = lbase(xa), xa1 = lbase(xa ^ 64), cp = lbase(L_CSP + g * 16);
    unsigned vres = (t * 128 + (2 * (h >> 2) + (g & 1)) * 16 + c) * (DM * 2) + nx * 16;
    unsigned pubx_ = pubx, vcol_ = vcol, vrow_ = vrow;
    float dg0 = c == 4 * g ? 1.f : 0.f, dg1 = c == 4 * g + 1 ? 1.f : 0.f, dg2 = c == 4 * g + 2 ? 1.f : 0.f, dg3 = c == 4 * g + 3 ? 1.f : 0.f;
    asm volatile("" : "+v"(vres), "+v"(a0), "+v"(a1), "+v"(sl3), "+v"(one8), "+v"(dg0), "+v"(dg1), "+v"(dg2), "+v"(dg3) : "v"(pubx_), "v"(vcol_), "v"(vrow_), "v"(sel3));
    {
      const bool spread = w_ == 7 || (w_ < 4 && w_ != 2);
      const unsigned slot = spread ? sl3 : c;
      const lptr jr0 = lbase(jat + a0), jr1 = lbase(jat + ((w_ & 6) == 4 ? a0 ^ 32 : a1));
      unsigned vj1 = ((w_ & 6) == 4 ? 14336 : 1024) + (vl16 & 768) + slot * 16, vcs = g == 0 ? c * 4 : g == 1 ? 64 + slot * 4 : OOB;
      unsigned dst = !job ? W_END : w_ < 4 ? W_PT + ((w_ ? 16 : 0) + (h ^ 1)) * 86016 + ((w_ ? w_ - 1 : 2) * 14 + t) * 2048 : W_WO + ((w_ - 4) * 28 + 2 * h) * 14336 + t * 1024;
      unsigned dcs = (job && w_ < 4) ? W_CS + ((h ^ 1) * 56 + w_ * 14 + t) * 128 : W_END;
      asm volatile("" : "+v"(vj1), "+v"(vcs));
      asm volatile("" : "+s"(dst), "+s"(dcs));
      STAMP(51);
      if (w_ < 4) { xline(1, 0); xline(1, 1); } else { kvblock(2); kvblock(3); }
      asm volatile("s_waitcnt lgkmcnt(0)" : "+s"(epoch) :: "memory");
      epoch += 1u;
      keys(epoch, key0, key1);
      keyk = ((vl16 & 512) ? key1 : key0) >> ((vl16 & 256) ? 14 : 0);
      asm volatile("" : "+v"(keyk));
      VMWAIT(8);
      STAMP(47);
      v4u f0, f1;
      turn(jr0, jr1, spread && c >= 12, f0, f1);
      if (w_ == 7) { lwr(lq16 + 7 * 2048, f0); lwr(lbase(jat + vj1), f1); }
      __builtin_amdgcn_raw_buffer_store_b128(f0, rs_ws, vl16, dst, SC1);
      __builtin_amdgcn_raw_buffer_store_b128(f1, rs_ws, vj1, dst, SC1);
      keep(f0, f1);
      const v4f c0 = mfma(one8, f0, z4), c1 = mfma(one8, f1, z4);
      __builtin_amdgcn_raw_buffer_store_b32(asu(g ? c1.x : c0.x), rs_ws, vcs, dcs, SC1);
      fence();
      STAMP(52);
    }
    const unsigned kslot = kvp_ ? c : sl3;
    const lptr kr0 = lbase(kvat + a0), kr1 = lbase(kvat + a1), kw0 = lbase(kvat + vl16), kw1 = lbase(kvat + 1024 + (vl16 & 768) + kslot * 16);
    const lptr cw0 = lbase(L_CSP + (w_ & 3) * 128 + c * 4), cw1 = lbase(L_CSP + (w_ & 3) * 128 + 64 + kslot * 4);
    const bool kpad = !kvp_ && c >= 12;
    v4f cs0 = z4, cs1 = z4;
    auto kvturn = [&](int j) INL {
      v4u f0, f1;
      turn(kr0 + j * 4096, kr1 + j * 4096, kpad, f0, f1);
      lwr(kw0 + j * 4096, f0); lwr(kw1 + j * 4096, f1);
      cs0 = mfma(one8, f0, cs0); cs1 = mfma(one8, f1, cs1);
    };
    if (w_ >= 4) {
      VMWAIT(7); kvturn(0); kvturn(1);
      VMWAIT(3); kvturn(2); kvturn(3);
      kvblock(4); xline(4, 0); kvblock(5); kvblock(6);
      VMWAIT(8);
    } else {
      xline(2, 0); xline(2, 1); xline(3, 0); xline(3, 1);
      __builtin_amdgcn_s_setprio(0);
      xline(4, 0);
      VMWAIT(2);
    }
    STAMP(48);
    wg_bar();
    STAMP(49);
#pragma unroll
    for (int j = 0; j < 2; ++j) { xf[2 * j] = lrd(xa0 + j * 2048); xf[2 * j + 1] = lrd(xa1 + j * 2048); }
    if (w_ == 6) __builtin_amdgcn_raw_buffer_store_b64(v2u{key0, key1}, rs_ws, vl16 < 256 ? vl16 >> 1 : OOB, W_FLAG + (t * 16 + h) * 128, SC1);
    v4f gram = z4, pa, pb;
    kv_gemm(0, 8, [&](int s) INL {
      gram = mfma(xf[s], xf[s], gram);
      if (s < 2) { xf[4 + 2 * s] = lrd(xa0 + (2 + s) * 2048); xf[5 + 2 * s] = lrd(xa1 + (2 + s) * 2048); }
      if (s == 1) xline(5, 0);
      if (s == 3) xline(6, 0);
      if (s == 5) { wphi[0] = lrd(lq16 + 7 * 2048); wphi[1] = lrd(lq16 + 7 * 2048 + 1024); }
      SB();
    });
    STAMP(50);
    v2u fc;
    const unsigned vfl = w_ == 7 ? (vl16 & 240) * 128 : (vl16 & 240) * 128 + (vl16 >> 8) * 128, sfl = W_FLAG + (w_ == 7 ? h ^ 1 : (7 * (h & 3)) >> 1) * 128;
    const bool now = w_ >= 6 && (int)(clock32() - (unsigned)born) >= P_F;
    if (now) asm volatile("s_nop 4\n\tbuffer_load_dwordx2 %0, %1, %2, %3 offen sc1\n\ts_waitcnt vmcnt(1)" : "=&v"(fc) : "v"(vfl), "s"(srdW), "s"(sfl) : "memory");
    else drain();
    if (w_ >= 4) { kvturn(4); kvturn(5); kvturn(6); *(LAS float*)(cw0) = cs0.x; *(LAS float*)(cw1) = cs1.x; }
#pragma unroll
    for (int j = 4; j < 7; ++j) { xf[2 * j] = lrd(xa0 + (j % 5) * 2048); xf[2 * j + 1] = lrd(xa1 + (j % 5) * 2048); }
    if (w_ >= 6) {
      asm volatile("s_waitcnt vmcnt(0)" : "+v"(fc) :: "memory");
      if (!now) { pause(P_F - 8 - (int)(clock32() - (unsigned)born)); fc = v2u{~key0, 0u}; }
      flags_wait(fc, vfl, sfl, ~0ull);
    }
    wg_bar();
    STAMP(54);
    kv_gemm(8, 14, [&](int s) INL {
      if (s < 11) { gram = mfma(xf[2 * s - 8], xf[2 * s - 8], gram); gram = mfma(xf[2 * s - 7], xf[2 * s - 7], gram); }
      if (s == 8) {
        const unsigned so = w_ >= 4 ? W_CS + (h * 56 + 14 * (w_ & 3)) * 128 : W_END;
        fl0 = ld16(rs_ws, vl16, so); fl1 = ld16(rs_ws, vl16 < 768 ? vl16 + 1024 : OOB, so);
        dma2s(L_WQ + w_ * 2048, vl16, srdW, ptA + L_WQ);
      }
      if (s == 9 && w_ < 6) dma2s(L_WQ + 16384 + w_ * 2048, vl16, srdW, ptA + L_WQ + 16384);
      if (s >= 10) {
#pragma unroll
        for (int n = NW0 * (s - 10); n < NW0 * (s - 9); ++n) wof[n] = ld16(rs_ws, w_ == 7 ? OOB : vl16, wos + n * 1024);
      }
      if (s >= 9 && s < 13) { const v4f u = pa + pb; if (s == 9) ck0 = u; else if (s == 10) ck1 = u; else if (s == 11) cv0 = u; else cv1 = u; }
      if (s < 12) { const int o = ((s - 8) >> 1) * 256 + ((s - 8) & 1) * 64; pa = *(LAS v4f*)(cp + o); pb = *(LAS v4f*)(cp + o + 128); }
      if (s == 12) var = xrow(1.f / DM, (gram.x * dg0 + gram.y * dg1) + (gram.z * dg2 + gram.w * dg3));
      SB();
    });
    if (w_ == 6) { *(LAS v4f*)(csg) = ck0; *(LAS v4f*)(csg + 64) = ck1; *(LAS v4f*)(csg + 128) = cv0; *(LAS v4f*)(csg + 192) = cv1; }
    vres0 = vres;
  }
  STAMP(56);
  int xwait;
  v4u kc = {key0, 0u, key1, 0u};
  PIN(kc);
  auto run_layer = [&](auto kind, auto role) INL {
    constexpr int layer = decltype(kind)::value, rep = layer >> 1;
    constexpr bool isA = !(layer & 1), first = layer == 0, more = layer < 3, lead = decltype(role)::value;
    int w = w_;
    asm volatile("" : "+s"(w));
    __builtin_assume(lead ? (w >= 4 && w < 8) : (w >= 0 && w < 4));
    const v4f z4 = {0.f, 0.f, 0.f, 0.f};
    const unsigned wob = (lead && w == 7) ? W_END - 12288 : wos + (isA ? 0 : 28 * 14336), wov = (lead && w == 7) ? OOB : vl16;
    auto load_wo = [&](int s0, int s1) INL {
#pragma unroll
      for (int s = s0; s < s1; ++s) wof[s] = ld16(rs_ws, wov + (s & 3) * 1024, wob + (s >> 2) * 4096);
    };
    v2u hh;
    unsigned vh;
    const unsigned shh = W_HINT + 2 * layer * 32768 + (t - 2) * 2048 + h * 128;
    if (!isA && lead) { const unsigned lo = lane_id(); vh = lo >= 16 ? OOB : (lo < 2 && (int)lo < t) ? (1 - lo) * 2048 : 4096; }
    const lptr cs = csg + (isA ? 0 : 384);
    float rstd, nmr;
    auto stats = [&]() INL {
      const float mean = k1.w * (1.f / DM);
      rstd = __builtin_amdgcn_rsqf(mx(fmaf(-mean, mean, var), 0.f) + 1e-5f); nmr = -mean * rstd;
      PIN(rstd); PIN(nmr);
    };
    unsigned hp = t < 15 ? W_HALO + rep * 4194304 + h * 262144 + (t * 8 + w) * 2048 : W_END;
    if (!isA) asm volatile("" : "+s"(hp));
    if (!first) {
      unsigned xs[4], sts = W_ST + layer * 131072 + (t * 8 + w) * 1024;
#pragma unroll
      for (int i = 0; i < 4; ++i) { xs[i] = W_X + layer * TILEBUF + tw + i * 4096; asm volatile("" : "+s"(xs[i])); }
      asm volatile("" : "+s"(sts));
      if (!isA && lead) PIN(vh);
      pause(xwait);
      auto ldx = [&](int s) INL { xf[s] = ld16(rs_ws, vl16 + (s & 3) * 1024, xs[s >> 2]); };
      for (;;) {
#pragma unroll
        for (int s = 0; s < WIN; ++s) ldx(s);
        STAMP(2 + layer * 12);
        v4u st;
        unsigned c0 = 0u - key0, c1 = 0u - key1;
        kv_gemm(0, 14, [&](int s) INL {
          const int n0 = WIN + s + (s < RAMP ? s : RAMP), n1 = WIN + s + 1 + (s + 1 < RAMP ? s + 1 : RAMP);
#pragma unroll
          for (int n = n0; n < n1; ++n) {
            if (n < 14) ldx(n);
            if (n == 14) st = ld16(rs_ws, vl16, sts);
            if (n == 15 && !isA && lead) hh = hint_get(vh, w == 4 ? shh : W_END);
          }
          c0 += xf[s].x + xf[s].z; c1 += xf[s].y + xf[s].w; PIN(c0); PIN(c1);
          if (s == 13) { var = xrow(1.f / DM, asf(st.x)); c0 += st.x + st.y; c1 += st.z; PIN(c0); PIN(c1); }
          if (s == 12) { ck0 = *(LAS v4f*)(cs); ck1 = *(LAS v4f*)(cs + 64); cv0 = *(LAS v4f*)(cs + 128); cv1 = *(LAS v4f*)(cs + 192); }
          SB();
        });
        c0 = xsum2u(c0, c1);
        stats();
        if (__builtin_expect(!nonzero(c0), 1) || !spend(budget)) break;
        __builtin_amdgcn_s_sleep(4);
      }
    }
    STAMP(3 + layer * 12);
    if (first) stats();
    auto ln7 = [&](v4f a0, v4f a1, v4f c0, v4f c1, float sc, float sh, float pad) INL {
      a0 = a0 * sc + c0 * sh;
      const v2f u = v2f{a1.x, a1.y} * sc + v2f{c1.x, c1.y} * sh;
      return v4u{pk(a0.x, a0.y), pk(a0.z, a0.w), pk(u.x, u.y), pk(fmaf(a1.z, sc, c1.z * sh), pad)};
    };
    auto q_gemm = [&](float sc, float pad) INL {
      v4f q0 = z4, q1 = z4, c0, c1;
      const lptr lq = lq16 + (isA ? 0 : L_WQB - L_WQ);
      v4u a[8];
#pragma unroll
      for (int i = 0; i < 8; ++i) { a[i] = lrd(lq + i * 1024); SB(); }
#pragma unroll
      for (int s = 0; s < 14; ++s) {
        const int i = (2 * s) & 7;
        if (!(s & 1)) { if (s < 12) LGKNOWN(4); else LGKNOWN(0); SB(); }
        q0 = mfma(a[i], xf[s], q0); SB(); if (s + 4 < 14) { a[i] = lrd(lq + (s + 4) * 2048); SB(); }
        q1 = mfma(a[i + 1], xf[s], q1); SB(); if (s + 4 < 14) { a[i + 1] = lrd(lq + (s + 4) * 2048 + 1024); SB(); }
        if (s == 12) { c0 = *(LAS v4f*)(cs + 256); c1 = *(LAS v4f*)(cs + 320); SB(); }
      }
      v4u r = ln7(q0, q1, c0, c1, rstd * sc, nmr * sc, pad);
      asm volatile("" : "+v"(r) :: "memory");
      return r;
    };
    constexpr float L2E = 1.4426950408889634f;
    const v4u kf = isA ? ln7(k0, k1, ck0, ck1, rstd * L2E, nmr * L2E, 0.f) : ln7(k0, k1, ck0, ck1, rstd, nmr, 0.f);
    auto v_pack = [&]() INL {
      const v4f a0 = v0 * rstd + cv0 * nmr, a1 = v1 * rstd + cv1 * nmr;
      return v4u{pk(a0.x, a0.y), pk(a0.z, a0.w), g3 ? 0x3f80u : pk(a1.x, a1.y), g3 ? 0u : pk(a1.z, a1.w)};
    };
    auto swap_kv = [&](int j0, int j1) INL {
#pragma unroll
      for (int j = j0; j < j1 && j < (lead ? 3 : 4); ++j) {
        const lptr a = lbase(w * 2048 + j * 16384 + vl16);
        v4u t0, t1;
        if (layer < 2) { t0 = lrd(a); t1 = lrd(a + 1024); }
        if (!first) { lwr(a, park[2 * j]); lwr(a + 1024, park[2 * j + 1]); }
        if (layer < 2) { park[2 * j] = t0; park[2 * j + 1] = t1; }
      }
      if (first) {
        asm volatile("s_waitcnt lgkmcnt(0)" ::: "memory");
#pragma unroll
        for (int j = j0; j < j1 && j < (lead ? 3 : 4); ++j) dma2s(w * 2048 + j * 16384, vl16, srdW, ptA + 16 * 86016 + j * 16384);
      }
    };
    v4u xres;
    v4f o0, o1;
    float den;
    const unsigned soh = W_HINT + (2 * layer + 1) * 32768 + t * 2048;
    v2u oh;

    if constexpr (isA) {
      if (lead && w == 7) hint_put(2 * layer);
      if (!first) { if (!lead) load_wo(6, 10); }
      else if (lead) {
        v4f cq = __builtin_bit_cast(v4f, fl0) + __builtin_bit_cast(v4f, fl1);
#pragma unroll
        for (int i = 0; i < 4; ++i) {
          float u = cq[i];
          u += asf(__builtin_amdgcn_update_dpp(0, asu(u), 0x128, 0xf, 0xf, false));
          cq[i] = xrow(1.f, u);
        }
        *(LAS v4f*)(lbase(L_CS + 256 + (w & 3) * 128 + (vl16 & 127))) = cq;
      }
      if (first) load_wo(4 * NW0, 4 * NW0 + 3);
      const v4u vf = v_pack();
      v4u pp, pm, qf;
      feature_map(kf, wphi, pp, pm, [&]() INL { SB(); if (first) load_wo(4 * NW0 + 3, 4 * NW0 + 6); else if (!lead) load_wo(10, 14); });
      {
        const lptr tb = stw + w * 3072;
        *(LAS v2u*)(tb) = v2u{pp.x, pp.y}; *(LAS v2u*)(tb + 512) = v2u{pp.z, pp.w};
        *(LAS v2u*)(tb + 1024) = v2u{pm.x, pm.y}; *(LAS v2u*)(tb + 1536) = v2u{pm.z, pm.w};
        *(LAS v2u*)(tb + 2048) = v2u{vf.x, vf.y}; *(LAS v2u*)(tb + 2560) = v2u{vf.z, vf.w};
      }
      if (first) VMWAIT(6);
      v4u hwk_ = {hwt.x, hwt.y, hwk.z, hwk.w};
      unsigned kvp = W_KVP + rep * 1048576 + h * 65536 + (w & 3) * 1024 + (lead ? t * 4096 : 0);
      const lptr pa = lbase((size_t)str + (w & 2) * 512), pb = lbase((size_t)str + 2048 + (w & 1) * 512);
      asm volatile("" : "+v"(hwk_));
      asm volatile("" : "+s"(kvp));
      wg_bar();
      STAMP(4 + layer * 12);
      auto residual = [&]() INL {
        xres = __builtin_amdgcn_raw_buffer_load_b128(__builtin_amdgcn_make_buffer_rsrc((void*)p.x, 0, SEQ * DM * 2, 0x00020000), vres0, 0, 0); SB();
      };
      if (lead) {
        __builtin_amdgcn_s_setprio(2);
        v4u bv[4], x0[4], x1[4];
#pragma unroll
        for (int kk = 0; kk < 4; ++kk) {
          bv[kk] = cat(trd(pb + kk * 6144), trd(pb + kk * 6144 + 3072));
          x0[kk] = cat(trd(pa + kk * 6144), trd(pa + kk * 6144 + 3072));
          x1[kk] = cat(trd(pa + kk * 6144 + 512), trd(pa + kk * 6144 + 3072 + 512));
          SB();
        }
        v4f d0 = z4, d1 = z4;
#pragma unroll
        for (int kk = 0; kk < 4; ++kk) { d0 = mfma(x0[kk], bv[kk], d0); d1 = mfma(x1[kk], bv[kk], d1); }
        v4u dv = {pk(d0.x, d0.y), pk(d0.z, d0.w), pk(d1.x, d1.y), pk(d1.z, 0.f)};
        const v4u hs = imma(hwk_, dv, zu);
        dv.w |= ((hs.x + keyk) & 0x3fffu) << 16;
        __builtin_amdgcn_raw_buffer_store_b128(dv, rs_ws, vl16, kvp, SC1);
        keep(dv);
        __builtin_amdgcn_s_setprio(0);
        STAMP(5 + layer * 12);
        if (first) {
          load_wo(4 * NW0 + 6, 14); residual();
#pragma unroll
          for (int j = 0; j < 4; ++j) if (j < 3 || w < 6) dma2s(L_WQB + (w & 3) * 2048 + j * 8192, vl16, srdW, ptA - 8192 + 16 * 86016 + L_WQ + j * 8192);
        } else load_wo(6, 14);
        qf = q_gemm(L2E, 0.f);
        feature_map(qf, wphi, pp, pm, []() {});
        PIN(pp); PIN(pm);
        if (first && KVE) swap_kv(0, 4);
      } else {
        const unsigned sh = W_HINT + 2 * layer * 32768 + h * 128;
        __builtin_amdgcn_s_setprio(1);
        __builtin_amdgcn_s_sleep(DLY_T);
        const v2u hv = hint_get(vcol, sh);
        if (first) { load_wo(4 * NW0 + 6, 14); residual(); }
        qf = q_gemm(L2E, 0.f);
        pause(lateness(hv, t, 150, key0, srdW, vcol, sh, budget) + T_KV);
        if (w == 0) hint_put(2 * layer + 1);
        fence();
        STAMP(6 + layer * 12);
        v4u kp[16];
#pragma unroll
        for (int i = 0; i < 8; ++i) kp[i] = ld16(rs_ws, vl16, kvp + i * 4096);
        feature_map(qf, wphi, pp, pm, [&]() INL {
          SB();
#pragma unroll
          for (int i = 8; i < 16; ++i) kp[i] = ld16(rs_ws, vl16, kvp + i * 4096);
        });
        PIN(pp); PIN(pm);
        const unsigned lo = lane_id();
        const unsigned bit = ((lo >> 4) == ((lo >> 2) & 3)) ? ((lo & 1) ? 0x3f800000u : 0x3f80u) : 0u;
        const unsigned u0 = (lo & 2) ? 0u : bit, u1 = (lo & 2) ? bit : 0u;
        const v4u sa = {u0, u1, 0u, 0u}, sb = {0u, 0u, u0, u1};
        const lptr kd = lbase((size_t)sl16 + (L_KVF - L_SCR) + w * 1024);
        auto add_up = [&](bool once) INL {
          v4f lo4 = z4, hi4 = z4;
          v4u hs = zu;
          unsigned ck = 0u - 16u * keyk;
          SB();
#pragma unroll
          for (int i = 0; i < 16; ++i) {
            hs = imma(hwk_, kp[i], hs); lo4 = mfma(sa, kp[i], lo4); hi4 = mfma(sb, kp[i], hi4);
            ck += kp[i].w >> 16; PIN(ck);
            SB();
          }
          const bool bad = nonzero((hs.x - ck) & 0x3fffu);
          SB();
          lwr(kd, v4u{pk(lo4.x, lo4.y), pk(lo4.z, lo4.w), pk(hi4.x, hi4.y), pk(hi4.z, 0.f)});
          return bad;
        };
        if (__builtin_expect(add_up(true), 0)) {
          do {
#pragma unroll
            for (int i = 0; i < 16; i += 4) reload4(kp[i], kp[i + 1], kp[i + 2], kp[i + 3], srdW, vl16, kvp + i * 4096, kvp + (i + 1) * 4096, kvp + (i + 2) * 4096, kvp + (i + 3) * 4096);
          } while (add_up(false) && spend(budget));
        }
      }
      __builtin_amdgcn_s_setprio(0);
      wg_bar();
      STAMP(7 + layer * 12);
      v4u kv[4];
      kv[0] = lrd(sl16 + (L_KVF - L_SCR) + 1024); SB(); kv[1] = lrd(sl16 + (L_KVF - L_SCR) + 3072); SB();
      kv[2] = lrd(sl16 + (L_KVF - L_SCR)); SB(); kv[3] = lrd(sl16 + (L_KVF - L_SCR) + 2048);
      SB();
      if (lead) oh = hint_get(vrow, w == 7 ? soh : W_END);
      o1 = mfma(kv[0], pp, z4); o1 = mfma(kv[1], pm, o1); SB();
      o0 = mfma(kv[2], pp, z4); o0 = mfma(kv[3], pm, o0); SB();
      { const v4f e6 = {1e-6f, 1e-6f, 1e-6f, 1e-6f}, r = __builtin_amdgcn_mfma_f32_16x16x4f32(sel3, o1.x, e6, 0, 0, 0); den = r.x; }
    } else {
      const unsigned hb = W_HALO + rep * 4194304 + h * 262144;
      __builtin_amdgcn_raw_buffer_store_b128(kf, rs_ws, vl16, hp, SC1); SB();
      const v4u hsk = imma(hwt, kf, v4u{kf.z + key0, 0u, kf.w + key1, 0u}); SB();
      v4u vf = v_pack();
      const v4u hs_v = imma(hwt, vf, hsk); SB();
      const lptr kw = sl16 + w * 2048, vw = stw + w * 2048, vr = str + w * 2048;
      *(LAS v2u*)(vw + 32768 + 1024) = v2u{vf.x, vf.y}; *(LAS v2u*)(vw + 32768 + 1536) = v2u{vf.z, vf.w};
      if (!lead || w < 7) lwr(kw + 32768, kf);
      SB();
      vf.z = g3 ? (hs_v.y << 13) + hs_v.x : vf.z; vf.w = g3 ? (hs_v.w << 13) + hs_v.z : vf.w;
      __builtin_amdgcn_raw_buffer_store_b128(vf, rs_ws, vl16 + 1024, hp, SC1); keep(vf);
      STAMP(4 + layer * 12);
      v4u hk[2], hv[2];
      unsigned hs[2];
#pragma unroll
      for (int i = 0; i < 2; ++i) hs[i] = t - 2 + i >= 0 ? hb + ((t - 2 + i) * 8 + w) * 2048 : W_END;
      int hlate;
      auto fetch = [&](int margin) INL {
        pause(__builtin_amdgcn_readfirstlane(hlate) + margin);
#pragma unroll
        for (int i = 0; i < 2; ++i) { hk[i] = ld16(rs_ws, vl16, hs[i]); hv[i] = ld16(rs_ws, vl16 + 1024, hs[i]); }
      };
      if (lead && w == 4) *(LAS int*)(csg + 896) = lateness(hh, 2, 200, key0, srdW, vh, shh, budget);
      if (lead) load_wo(6, 10);
      wg_bar();
      hlate = *(LAS int*)(csg + 896);
      if (!lead) { fetch(T_H); __builtin_amdgcn_s_setprio(1); }
      const v4u qf = q_gemm(0.18898223650461357f * L2E, 1.f);
      if (lead) { fetch(T_HL); load_wo(10, 14); } else load_wo(6, 10);
      const float NEG = -3.0e38f;
      v4f sd = mfma(kf, qf, z4);
#pragma unroll
      for (int r = 0; r < 4; ++r) sd[r] = (4 * g + r <= c) ? sd[r] : NEG;
      float m = xmax1(mx(mx3(sd.x, sd.y, sd.z), sd.w));
      auto diag = [&]() INL {
        const v2u a0 = trd(vr + 32768 + 1024), a1 = trd(vr + 32768 + 1536);
        const v4f e = sd - m;
        const v2u pd = {pk(ex2(e.x), ex2(e.y)), pk(ex2(e.z), ex2(e.w))};
        typedef short v4h __attribute__((ext_vector_type(4)));
        o0 = __builtin_amdgcn_mfma_f32_16x16x16bf16_1k(__builtin_bit_cast(v4h, a0), __builtin_bit_cast(v4h, pd), z4, 0, 0, 0);
        o1 = __builtin_amdgcn_mfma_f32_16x16x16bf16_1k(__builtin_bit_cast(v4h, a1), __builtin_bit_cast(v4h, pd), z4, 0, 0, 0);
      };
      diag();
      auto take_halo = [&]() INL {
        STAMP(6 + layer * 12);
        const unsigned nv = min(t, 2);
        auto stage = [&]() INL {
          v4u hs_ = zu;
          unsigned e0 = 0u - nv * key0, e1 = 0u - nv * key1;
#pragma unroll
          for (int i = 0; i < 2; ++i) {
            const unsigned none = t - 2 + i < 0 ? 0xfe000000u : 0u;
            hs_ = imma(hwt, hk[i], hs_); SB();
            hs_ = imma(hwt, hv[i], hs_); SB();
            e0 += hv[i].z - hk[i].z; e1 += hv[i].w - hk[i].w; PIN(e0); PIN(e1);
            lwr(kw + i * 16384, v4u{hk[i].x, hk[i].y, hk[i].z, hk[i].w | none});
            *(LAS v2u*)(vw + i * 16384 + 1024) = v2u{hv[i].x, hv[i].y};
            *(LAS v2u*)(vw + i * 16384 + 1536) = v2u{g3 ? 0x3f80u : hv[i].z, g3 ? 0u : hv[i].w};
            SB();
          }
          return row3_differs(hs_, e0, e1);
        };
        if (__builtin_expect(stage(), 0)) {
          do reload4(hk[0], hk[1], hv[0], hv[1], srdW, vl16, hs[0], hs[1], min(hs[0] + 1024, W_END), min(hs[1] + 1024, W_END));
          while (stage() && spend(budget));
        }
        wg_bar();
        STAMP(7 + layer * 12);
      };
      {
        const v4f cm = {-m, -m, -m, -m};
        v4f sa, sb; v4u a0, a1, b0, b1, pq;
#pragma unroll
        for (int j = 7; j >= 0; --j) {
          if (j >= 4 && (j < 6) == lead && (w >> 1) == 7 - j) take_halo();
          if (j == (lead ? 4 : 6)) VMKNOWN(4);
          if (!lead && j == 6 && w == 3) hint_put(2 * layer + 1);
          if (!lead && j <= 5 && j >= 4) load_wo(10 + 2 * (5 - j), 12 + 2 * (5 - j));
          if (!lead && j == PBACK) __builtin_amdgcn_s_setprio(0);
          if (!lead && j == POLL_OB) oh = hint_get(vrow, w == 3 ? soh : W_END);
          const v4u ka = lrd(kw + j * 4096), kb = lrd(kw + j * 4096 + 2048);
          const v4u n0 = cat(trd(vr + j * 4096 + 1024), trd(vr + j * 4096 + 3072)), n1 = cat(trd(vr + j * 4096 + 1536), trd(vr + j * 4096 + 3584));
          SB();
          if (j < 7) {
            const float e0 = ex2(sa.x), e1 = ex2(sa.y); SB();
            if (j < 6) { o0 = mfma(b0, pq, o0); SB(); }
            const float e2 = ex2(sa.z), e3 = ex2(sa.w); SB();
            if (j < 6) { o1 = mfma(b1, pq, o1); SB(); }
            const float e4 = ex2(sb.x), e5 = ex2(sb.y); SB();
            sa = mfma(ka, qf, cm); SB();
            const float e6 = ex2(sb.z), e7 = ex2(sb.w); SB();
            sb = mfma(kb, qf, cm); SB();
            pq = v4u{pk(e0, e1), pk(e2, e3), pk(e4, e5), pk(e6, e7)};
            b0 = a0; b1 = a1;
          } else {
            sa = mfma(ka, qf, cm); sb = mfma(kb, qf, cm);
          }
          a0 = n0; a1 = n1;
          SB();
        }
        o0 = mfma(b0, pq, o0); o1 = mfma(b1, pq, o1); SB();
#pragma unroll
        for (int r = 0; r < 4; ++r) sa[r] = (4 * g + r > c) ? sa[r] : NEG;
        const v4u pf = pexp(sa, sb);
        o1 = mfma(a1, pf, o1); o0 = mfma(a0, pf, o0);
      }
      den = xrow(sel3, o1.x);
      if (__builtin_expect(__any(!(den < 1.0e18f)), 0)) {
        float mt = m;
#pragma unroll 1
        for (int k = 0; k < 16; ++k) {
          v4f s = mfma(lrd(kw + k * 2048), qf, z4);
#pragma unroll
          for (int r = 0; r < 4; ++r) s[r] = (k > 0 || 4 * g + r > c) ? s[r] : NEG;
          mt = mx3(mt, mx(s.x, s.y), mx(s.z, s.w));
        }
        m = xmax1(mt);
        diag();
        const v4f cm = {-m, -m, -m, -m};
#pragma unroll 1
        for (int j = 7; j >= 0; --j) {
          v4f sa = mfma(lrd(kw + j * 4096), qf, cm);
          const v4f sb = mfma(lrd(kw + j * 4096 + 2048), qf, cm);
#pragma unroll
          for (int r = 0; r < 4; ++r) sa[r] = (j > 0 || 4 * g + r > c) ? sa[r] : NEG;
          const v4u pf = pexp(sa, sb);
          o0 = mfma(cat(trd(vr + j * 4096 + 1024), trd(vr + j * 4096 + 3072)), pf, o0);
          o1 = mfma(cat(trd(vr + j * 4096 + 1536), trd(vr + j * 4096 + 3584)), pf, o1);
        }
        den = xrow(sel3, o1.x);
      }
    }
    const unsigned ob = W_O + layer * 2097152 + t * 131072;
    {
      const float rd = __builtin_amdgcn_rcpf(den);
      o0 *= rd; o1 *= rd;
      v4u v = {pk(o0.x, o0.y), pk(o0.z, o0.w), pk(o1.x, o1.y), pk(o1.z, o1.w)};
      const v4u hs = imma(hwt, v, kc);
      v.z = g3 ? (hs.y << 13) + hs.x : v.z; v.w = g3 ? (hs.w << 13) + hs.z : v.w;
      __builtin_amdgcn_raw_buffer_store_b128(v, rs_ws, vl16, ob + (w * 16 + h) * 1024, SC1);
      keep(v);
    }
    __builtin_amdgcn_s_setprio(0);
    STAMP(8 + layer * 12);
    if (first) {
      const v4u xr = swap16(xres);
      res0 = v4f{asf(xr.x << 16), asf(xr.x & 0xffff0000u), asf(xr.y << 16), asf(xr.y & 0xffff0000u)};
      res1 = v4f{asf(xr.z << 16), asf(xr.z & 0xffff0000u), asf(xr.w << 16), asf(xr.w & 0xffff0000u)};
    }
    unsigned op = ob + (2 * (h >> 2) + (lead ? 0 : 1)) * 16384 + (w & 3) * 1024;
    asm volatile("" : "+s"(op));
    v4u of[4];
    if (isA && !(first && KVE && lead)) swap_kv(0, first && KVE ? 4 : 2);
    if (lead == isA && w == (isA ? 7 : 3)) *(LAS int*)(csg + 768) = lateness(oh, h, isA ? 165 : 125, key0, srdW, vrow, soh, budget);
    wg_bar();
    const int olate = __builtin_amdgcn_readfirstlane(*(LAS int*)(csg + 768));
    if (!isA && more) swap_kv(0, 2);
    pause(olate + (isA ? T_OA : T_OB) + (more ? 0 : 6));
#pragma unroll
    for (int i = 0; i < 4; ++i) of[i] = ld16(rs_ws, vl16, op + i * 4096);
    if (more && !first) swap_kv(2, 4);
    const unsigned qd = 7 * (w & 3) + (vl16 >> 8), qa = L_SCR + (lead ? 0 : 14336) + (qd >> 1) * 256 + (qd & 1) * 8 + (vl16 & 255);
    const lptr lo = lbase(qa), hi = lbase(g3 ? L_SCR + 32768 + vl16 : qa + 512);
    STAMP(9 + layer * 12);
    auto stage_o = [&]() INL {
      auto put = [&]() INL {
        v4u hs = zu;
        unsigned e0 = 0u - 4u * key0, e1 = 0u - 4u * key1;
#pragma unroll
        for (int i = 0; i < 4; ++i) {
          hs = imma(hwt, of[i], hs); SB();
          *(LAS v2u*)(lo + i * 3584) = v2u{of[i].x, of[i].y};
          *(LAS v2u*)(hi + i * 3584) = v2u{of[i].z, of[i].w};
          e0 += of[i].z; e1 += of[i].w; PIN(e0); PIN(e1);
          SB();
        }
        return row3_differs(hs, e0, e1);
      };
      if (__builtin_expect(put(), 0)) {
        do reload4(of[0], of[1], of[2], of[3], srdW, vl16, op, op + 4096, op + 8192, op + 12288);
        while (put() && spend(budget));
      }
    };
    stage_o();
    wg_bar();
    if (first && !KVE) swap_kv(2, 4);
    const unsigned sx = W_HINT + (8 + layer) * 32768 + t * 2048;
    if (lead && w == 7) {
      if (more) {
        hint_put(8 + layer);
        __builtin_amdgcn_s_sleep(P_X);
        *(LAS int*)(csg + 832) = lateness(hint_get(vrow, sx), h, 70, key0, srdW, vrow, sx, budget);
      }
    }
    int w2 = w;
    asm volatile("" : "+s"(w2));
    if (!lead || w2 != 7) {
      const unsigned won = wos + (isA ? 28 * 14336 : 0);
      v4u bq[8];
#pragma unroll
      for (int i = 0; i < 4; ++i) { bq[2 * i] = lrd(sl16 + i * 1024); SB(); bq[2 * i + 1] = lrd(sl16 + 14336 + i * 1024); SB(); }
#pragma unroll
      for (int s = 0; s < 14; ++s) {
        const int i = (2 * s) & 7;
        if (!(s & 1)) { if (s < 12) LGKNOWN(4); else LGKNOWN(0); SB(); }
        res0 = mfma(wof[s], bq[i], res0); SB(); if (s + 4 < 14) { bq[i] = lrd(sl16 + (s + 4) * 1024); SB(); }
        res1 = mfma(wof[s], bq[i + 1], res1); SB(); if (s + 4 < 14) { bq[i + 1] = lrd(sl16 + 14336 + (s + 4) * 1024); SB(); }
        if (more && s >= 1 && (s - 1) % WOS == 0 && (s - 1) / WOS < 6) { const int n = (s - 1) / WOS; wof[n] = ld16(rs_ws, vl16 + (n & 3) * 1024, won + (n >> 2) * 4096); }
      }
      const v4u xo = swap16(v4u{pk(res0.x, res0.y), pk(res0.z, res0.w), pk(res1.x, res1.y), pk(res1.z, res1.w)});
      if (more) {
        __builtin_amdgcn_raw_buffer_store_b128(xo, rs_ws, pubx, W_X + (layer + 1) * TILEBUF + tp, SC1);
        keep(xo);
        auto r = __builtin_amdgcn_permlane32_swap(xo.x + xo.z, xo.y + xo.w, false, false);
        const unsigned r0 = r[0], r1 = r[1];
        *(LAS v2u*)(lbase(L_SCR + 28672 + w * 512 + (vl16 >> 1))) = v2u{r0 + r1, asu(sumsq8(xo))};
      } else {
        const unsigned lo = lane_id();
        __builtin_amdgcn_raw_buffer_store_b128(xo, __builtin_amdgcn_make_buffer_rsrc(p.out, 0, SEQ * DM * 2, 0x00020000),
                                               (lo & 31) * (DM * 2) + (2 * ft + (lo >> 5)) * 16, (t * 128 + (h >> 2) * 32) * (DM * 2), 0);
        keep(xo);
      }
    }
    STAMP(10 + layer * 12);
    if (more) {
      if (first) VMWAIT(7);
      wg_bar();
      const int late = __builtin_amdgcn_readfirstlane(*(LAS int*)(csg + 832));
      if (lead && w2 == 7) {
        float q = 0.f;
        unsigned a = 0u;
        const lptr sp = lbase(L_SCR + 28672 + (vl16 >> 1));
#pragma unroll
        for (int i = 0; i < 7; ++i) { const v2u v = *(LAS v2u*)(sp + i * 512); a += v.x; q += asf(v.y); }
        auto r = __builtin_amdgcn_permlane32_swap(asu(q), asu(q), false, false);
        auto ra = __builtin_amdgcn_permlane32_swap(a, a, false, false);
        const unsigned r0 = r[0], r1 = r[1], a0 = ra[0], a1 = ra[1], qs = asu(asf(r0) + asf(r1));
        const v4u rec = {qs, key0 - qs - a0, key1 - a1, 0u};
        __builtin_amdgcn_raw_buffer_store_b128(rec, rs_ws, vl16 < 512 ? (vl16 >> 8) * 1024 + (vl16 & 255) : OOB,
                                               W_ST + (layer + 1) * 131072 + (t * 8 + 2 * (h >> 2)) * 1024 + (h & 3) * 256, SC1);
        keep(rec);
        if (isA) hint_put(2 * layer + 2, max(late, 0));
        xwait = late + T_X - 12;
      } else {
        xwait = late + T_X;
      }
    }
  };
  auto finish = [&]() INL {
  };
  if (w_ >= 4) {
    run_layer(Layer<0>{}, Layer<1>{}); run_layer(Layer<1>{}, Layer<1>{}); run_layer(Layer<2>{}, Layer<1>{}); run_layer(Layer<3>{}, Layer<1>{});
    if (w_ == 7 && b == 0) __builtin_amdgcn_raw_buffer_store_b32(epoch, rs_ws, 0, W_KEY, 0);
#pragma unroll
    for (int s = 0; s < 14; ++s) asm volatile("" : "=v"(xf[s]));
    asm volatile("" : "=v"(k0), "=v"(k1), "=v"(v0), "=v"(v1), "=v"(ck0), "=v"(ck1), "=v"(cv0), "=v"(cv1), "=v"(var), "=v"(wphi[0]), "=v"(wphi[1]), "=v"(vres0));
  }
  int wz = w_;
  asm volatile("" : "+s"(wz));
  if (wz < 4) {
    run_layer(Layer<0>{}, Layer<0>{}); run_layer(Layer<1>{}, Layer<0>{}); run_layer(Layer<2>{}, Layer<0>{}); run_layer(Layer<3>{}, Layer<0>{});
  }
  finish();
}
}

extern "C" void kernel_launch(void* const* d_in, const int* in_sizes, int n_in, void* d_out, int out_size, void* d_ws, size_t ws_size,
                              hipStream_t stream) {
  (void)in_sizes; (void)n_in; (void)out_size; (void)ws_size;
  (void)hipFuncSetAttribute((const void*)fused_layers, hipFuncAttributeMaxDynamicSharedMemorySize, (int)L_TOTAL);
  Params p;
  p.x = d_in[0]; p.wA = d_in[1]; p.wphi = d_in[2]; p.woA = d_in[3]; p.wB = d_in[5]; p.woB = d_in[6]; p.out = d_out; p.ws = d_ws;
  hipLaunchKernelGGL(fused_layers, dim3(256), dim3(512), L_TOTAL, stream, p);
}
```

```cpp
#include <hip/hip_runtime.h>
#include <stdint.h>

#define LAS __attribute__((address_space(3)))
#define DEV __device__ __forceinline__
typedef unsigned v4u __attribute__((ext_vector_type(4)));
typedef unsigned v2u __attribute__((ext_vector_type(2)));
typedef float v4f __attribute__((ext_vector_type(4)));
typedef float v2f __attribute__((ext_vector_type(2)));
typedef int v4i __attribute__((ext_vector_type(4)));
typedef short v4s __attribute__((ext_vector_type(4)));
typedef __bf16 v8bf __attribute__((ext_vector_type(8)));
typedef __bf16 v2bf __attribute__((ext_vector_type(2)));
typedef __amdgpu_buffer_rsrc_t rsrc_t;

#define TPX 4
#define DLY_T 3
#define WO1 6
#define POLL_OB 1
#define T_KV 4
#define T_H 24
#define T_OA 0
#define T_OB 12
#define P_X 11
#define T_X 8
#define T_XL 0
#define T_OT 20
#define PRI0 1
#define RAMP 0
#define WOS 1
#define T_HL -4
#define P_F 440
#define BOFF 4
#define CODEB 60000
#define NW0 2
#define KVE 1
#define PBACK 99
#define ILV 1
#define WIN 5

namespace {
constexpr int SEQ = 2048, DM = 448;
constexpr unsigned L_WK = 0, L_WV = 28672, L_WQ = 57344, L_WQB = 86016, L_SCR = 114688, L_TOTAL = 163840;
constexpr unsigned L_CS = L_SCR + 23 * 2048;
constexpr unsigned L_KVF = L_SCR + 40960;
constexpr unsigned TILEBUF = 16 * 8 * 14 * 1024;
constexpr unsigned W_KEY = 0, W_HINT = 4096, W_FLAG = W_HINT + 11 * 32768, W_ST = W_HINT + 12 * 32768, W_KVP = W_ST + 4 * 131072, W_O = W_KVP + 2 * 1048576,
                   W_HALO = W_O + 4 * 2097152, W_X = W_HALO + 2 * 4194304, W_PT = W_X + 4 * TILEBUF, W_WO = W_PT + 2 * 16 * 86016,
                   W_CS = W_WO + 2 * 28 * 14336, W_END = W_CS + 16 * 56 * 128;
constexpr unsigned W_DBG = 64u << 20;
constexpr int SC1 = 16;
constexpr unsigned OOB = 0x40000000u;

DEV v4f mfma(v4u a, v4u b, v4f c) {
  return __builtin_amdgcn_mfma_f32_16x16x32_bf16(__builtin_bit_cast(v8bf, a), __builtin_bit_cast(v8bf, b), c, 0, 0, 0);
}
DEV unsigned pk(float a, float b) { v2f f = {a, b}; v2bf r = __builtin_convertvector(f, v2bf); return __builtin_bit_cast(unsigned, r); }
typedef LAS char* lptr;
DEV lptr lbase(unsigned v) { asm volatile("" : "+v"(v)); return (lptr)(size_t)v; }
DEV v2u trd(lptr a) { v4s r = __builtin_amdgcn_ds_read_tr16_b64_v4i16((LAS v4s*)a); return __builtin_bit_cast(v2u, r); }
DEV v4u cat(v2u a, v2u b) { return v4u{a.x, a.y, b.x, b.y}; }
DEV v4u lrd(lptr a) { return *(LAS v4u*)a; }
DEV void lwr(lptr a, v4u v) { *(LAS v4u*)a = v; }
#define VMWAIT(n) asm volatile("s_waitcnt vmcnt(" #n ")" ::: "memory")
DEV float asf(unsigned u) { return __builtin_bit_cast(float, u); }
DEV unsigned asu(float f) { return __builtin_bit_cast(unsigned, f); }
DEV void wg_bar() { asm volatile("s_waitcnt lgkmcnt(0)\n\ts_barrier" ::: "memory"); }
DEV void drain() { asm volatile("s_waitcnt vmcnt(0)" ::: "memory"); }
DEV void fence() { asm volatile("" ::: "memory"); }
DEV float ex2(float x) { return __builtin_amdgcn_exp2f(x); }

DEV unsigned lane_id() { unsigned r; asm volatile("v_mbcnt_lo_u32_b32 %0, -1, 0\n\tv_mbcnt_hi_u32_b32 %0, -1, %0" : "=v"(r)); __builtin_assume(r < 64); return r; }
DEV float mx(float a, float b) { return __builtin_elementwise_maximum(a, b); }
DEV float mn(float a, float b) { return __builtin_elementwise_minimum(a, b); }
DEV float mx3(float a, float b, float c) { return mx(mx(a, b), c); }
DEV float mn3(float a, float b, float c) { return mn(mn(a, b), c); }
DEV float xrow(float sel, float v) { const v4f z = {0.f, 0.f, 0.f, 0.f}; const v4f r = __builtin_amdgcn_mfma_f32_16x16x4f32(sel, v, z, 0, 0, 0); return r.x; }
DEV v4f xrow4(float sel, float v) { const v4f z = {0.f, 0.f, 0.f, 0.f}; return __builtin_amdgcn_mfma_f32_16x16x4f32(sel, v, z, 0, 0, 0); }
DEV v4u swap16(v4u v) {
  auto r = __builtin_amdgcn_permlane16_swap(v.x, v.z, false, false);
  auto q = __builtin_amdgcn_permlane16_swap(v.y, v.w, false, false);
  const unsigned r0 = r[0], r1 = r[1], q0 = q[0], q1 = q[1];
  return v4u{r0, q0, r1, q1};
}
DEV float sumsq8(v4u v) {
  const v4f lo = __builtin_bit_cast(v4f, v << 16), hi = __builtin_bit_cast(v4f, v & 0xffff0000u), q = lo * lo + hi * hi;
  return (q.x + q.y) + (q.z + q.w);
}
DEV void xmax2(float& a, float& b) {
  auto r = __builtin_amdgcn_permlane32_swap(asu(a), asu(b), false, false);
  unsigned r0 = r[0], r1 = r[1];
  float z = mx(asf(r0), asf(r1));
  auto q = __builtin_amdgcn_permlane16_swap(asu(z), asu(z), false, false);
  unsigned q0 = q[0], q1 = q[1];
  z = mx(asf(q0), asf(q1));
  auto p = __builtin_amdgcn_permlane32_swap(asu(z), asu(z), false, false);
  unsigned p0 = p[0], p1 = p[1];
  a = asf(p0); b = asf(p1);
}
DEV float xmax1(float a) {
  auto r = __builtin_amdgcn_permlane32_swap(asu(a), asu(a), false, false);
  unsigned r0 = r[0], r1 = r[1];
  float z = mx(asf(r0), asf(r1));
  auto q = __builtin_amdgcn_permlane16_swap(asu(z), asu(z), false, false);
  unsigned q0 = q[0], q1 = q[1];
  return mx(asf(q0), asf(q1));
}
DEV void dma2(unsigned ldsdst, unsigned va, unsigned vb, v4u srd, unsigned sa, unsigned sb) {
  asm volatile("s_mov_b32 m0, %0\n\ts_nop 3\n\tbuffer_load_dwordx4 %1, %3, %4 offen lds\n\tbuffer_load_dwordx4 %2, %3, %5 offen offset:1024 lds"
               :: "s"(ldsdst), "v"(va), "v"(vb), "s"(srd), "s"(sa), "s"(sb) : "memory");
}
DEV void keep(v4u v) { asm volatile("s_nop 1" :: "v"(v) : "memory"); }
DEV void keep(v4u v, v4u u) { asm volatile("s_nop 1" :: "v"(v), "v"(u) : "memory"); }
DEV void dma2s(unsigned ldsdst, unsigned v, v4u srd, unsigned so) {
  asm volatile("s_mov_b32 m0, %0\n\ts_nop 3\n\tbuffer_load_dwordx4 %1, %2, %3 offen sc1 lds\n\tbuffer_load_dwordx4 %1, %2, %3 offen offset:1024 sc1 lds"
               :: "s"(ldsdst), "v"(v), "s"(srd), "s"(so) : "memory");
}
DEV v4u ld16(rsrc_t rs, unsigned v, unsigned so) {
  const v4u r = __builtin_amdgcn_raw_buffer_load_b128(rs, v, so, SC1);
  __builtin_amdgcn_sched_barrier(0);
  return r;
}
DEV v4u mk_srd(const void* p, unsigned bytes) {
  unsigned long long a = (unsigned long long)p;
  return v4u{(unsigned)a, (unsigned)(a >> 32) & 0xffffu, bytes, 0x00020000u};
}
DEV v4u imma(v4u a, v4u b, v4u c) {
  const v4i r = __builtin_amdgcn_mfma_i32_16x16x64_i8(__builtin_bit_cast(v4i, a), __builtin_bit_cast(v4i, b), __builtin_bit_cast(v4i, c), 0, 0, 0);
  return __builtin_bit_cast(v4u, r);
}
DEV v4u weights(unsigned lo) {
  const unsigned a = (lo + 1) * 0x9e3779b1u, b = (a ^ (a >> 15)) * 0x85ebca77u, c = (b ^ (b >> 13)) * 0xc2b2ae3du, d = (c ^ (c >> 16)) * 0x27d4eb2fu;
  return v4u{(a ^ (a >> 16)) | 0x01010101u, (b ^ (b >> 16)) | 0x01010101u, (c ^ (c >> 16)) | 0x01010101u, (d ^ (d >> 16)) | 0x01010101u};
}
DEV void keys(unsigned e, unsigned& k0, unsigned& k1) { k0 = e * 0x9e3779b1u + 0x7f4a7c15u; k1 = e * 0x85ebca77u + 0x165667b1u; }
DEV unsigned xsum2u(unsigned a, unsigned b) {
  auto r = __builtin_amdgcn_permlane32_swap(a, b, false, false);
  const unsigned r0 = r[0], r1 = r[1], z = r0 + r1;
  auto q = __builtin_amdgcn_permlane16_swap(z, z, false, false);
  const unsigned q0 = q[0], q1 = q[1];
  return q0 + q1;
}
DEV void reload4(v4u& a, v4u& b, v4u& c, v4u& d, v4u srd, unsigned voff, unsigned sa, unsigned sb, unsigned sc, unsigned sd) {
  asm volatile("s_sleep 2\n\tbuffer_load_dwordx4 %0, %4, %5, %6 offen sc1\n\tbuffer_load_dwordx4 %1, %4, %5, %7 offen sc1\n\t"
               "buffer_load_dwordx4 %2, %4, %5, %8 offen sc1\n\tbuffer_load_dwordx4 %3, %4, %5, %9 offen sc1\n\ts_waitcnt vmcnt(0)"
               : "+v"(a), "+v"(b), "+v"(c), "+v"(d) : "v"(voff), "s"(srd), "s"(sa), "s"(sb), "s"(sc), "s"(sd) : "memory");
}
DEV bool spend(int& budget) { budget = max(budget - 1, 0); return budget != 0; }
DEV bool nonzero(unsigned v, unsigned long long lanes = ~0ull) { return (__builtin_amdgcn_uicmp(v, 0u, 33) & lanes) != 0ull; }
DEV bool row3_differs(v4u hs, unsigned e0, unsigned e1) {
  const unsigned long long m = __builtin_amdgcn_uicmp((hs.y << 13) + hs.x, e0, 33) | __builtin_amdgcn_uicmp((hs.w << 13) + hs.z, e1, 33);
  unsigned hi = (unsigned)(m >> 32);
  asm volatile("" : "+s"(hi));
  return (hi >> 16) != 0u;
}
DEV bool retry(bool bad, int& budget) {
  if (__builtin_expect(!__any(bad), 1)) return false;
  budget = max(budget - 1, 0);
  return __any(budget != 0);
}

DEV unsigned clock32() { unsigned long long t; asm volatile("s_memrealtime %0\n\ts_waitcnt lgkmcnt(0)" : "=s"(t) :: "memory"); return (unsigned)t; }
DEV int lateness(v2u v, int own, int seg, unsigned key, v4u srd, unsigned voff, unsigned soff, int& budget) {
  asm volatile("" : "+v"(v) :: "memory");
  int over = 0;
  if (__builtin_expect(nonzero(v.y - key, 0xffffull), 0)) {
    do {
      asm volatile("s_sleep %4\n\tbuffer_load_dwordx2 %0, %1, %2, %3 offen sc1\n\ts_waitcnt vmcnt(0)" : "+v"(v) : "v"(voff), "s"(srd), "s"(soff), "n"(BOFF) : "memory");
    } while (nonzero(v.y - key, 0xffffull) && spend(budget));
    const unsigned tv = v.x;
    over = max((int)(clock32() - __builtin_amdgcn_readlane(tv, own)) - seg, 0);
  }
  const unsigned tv = v.x;
  int d = (int)(tv - __builtin_amdgcn_readlane(tv, own));
  d = max(d, __builtin_amdgcn_update_dpp(0, d, 0x128, 0xf, 0xf, false));
  d = max(d, __builtin_amdgcn_update_dpp(0, d, 0x124, 0xf, 0xf, false));
  d = max(d, __builtin_amdgcn_update_dpp(0, d, 0x122, 0xf, 0xf, false));
  d = max(d, __builtin_amdgcn_update_dpp(0, d, 0x121, 0xf, 0xf, false));
  return min(__builtin_amdgcn_readfirstlane(d), 1000) - over;
}
DEV void pause(int ticks) {
  asm volatile("s_cmp_lt_i32 %0, 1\n\ts_cbranch_scc1 2f\n1:\n\ts_sleep 1\n\ts_sub_i32 %0, %0, 4\n\ts_cmp_gt_i32 %0, 0\n\ts_cbranch_scc1 1b\n2:" : "+s"(ticks) :: "memory", "scc");
}

template <class F>
DEV void feature_map(v4u vf, const v4u (&wphi)[2], v4u& plus, v4u& minus, F mid) {
  const v4f z4 = {0.f, 0.f, 0.f, 0.f};
  const v4f p0 = mfma(wphi[0], vf, z4), p1 = mfma(wphi[1], vf, z4);
  float hi = mx3(mx3(mx3(p0.x, p0.y, p0.z), p0.w, p1.x), p1.y, p1.z);
  float lo = -mn3(mn3(mn3(p0.x, p0.y, p0.z), p0.w, p1.x), p1.y, p1.z);
  xmax2(hi, lo);
  mid();
  const v2f p01 = {p0.x, p0.y}, p23 = {p0.z, p0.w}, p45 = {p1.x, p1.y}, h2 = {hi, hi}, l2 = {lo, lo};
  v2f a01 = p01 - h2, a23 = p23 - h2, a45 = p45 - h2, b01 = p01 + l2, b23 = p23 + l2, b45 = p45 + l2;
  asm volatile("" : "+v"(a01), "+v"(a23), "+v"(a45), "+v"(b01), "+v"(b23), "+v"(b45));
  const v2f e01 = {ex2(a01.x), ex2(a01.y)}, e23 = {ex2(a23.x), ex2(a23.y)}, e45 = {ex2(a45.x), ex2(a45.y)};
  const float e6 = ex2(p1.z - hi);
  const v2f se = (e01 + e23) + e45;
  v4f te = xrow4(1.f, (se.x + e6) + se.y);
  __builtin_amdgcn_sched_barrier(0);
  const v2f f01 = {ex2(-b01.x), ex2(-b01.y)}, f23 = {ex2(-b23.x), ex2(-b23.y)}, f45 = {ex2(-b45.x), ex2(-b45.y)};
  const float f6 = ex2(-(p1.z + lo));
  const v2f sf = (f01 + f23) + f45;
  v4f tf = xrow4(1.f, (sf.x + f6) + sf.y);
  __builtin_amdgcn_sched_barrier(0);
  const float re = __builtin_amdgcn_rcpf(te.x);
  asm volatile("" :: "v"(te));
  const v2f x01 = e01 * re, x23 = e23 * re, x45 = e45 * re;
  plus = v4u{pk(x01.x, x01.y), pk(x23.x, x23.y), pk(x45.x, x45.y), pk(e6 * re, 0.f)};
  __builtin_amdgcn_sched_barrier(0);
  const float rf = __builtin_amdgcn_rcpf(tf.x);
  asm volatile("" :: "v"(tf));
  const v2f y01 = f01 * rf, y23 = f23 * rf, y45 = f45 * rf;
  minus = v4u{pk(y01.x, y01.y), pk(y23.x, y23.y), pk(y45.x, y45.y), pk(f6 * rf, 0.f)};
}

DEV v4u pexp(v4f a, v4f b) {
  return v4u{pk(ex2(a.x), ex2(a.y)), pk(ex2(a.z), ex2(a.w)), pk(ex2(b.x), ex2(b.y)), pk(ex2(b.z), ex2(b.w))};
}
#define VMKNOWN(n) __builtin_amdgcn_s_waitcnt(((n) & 15) | (((n) >> 4) << 14) | 0xf70)
#define LGKNOWN(n) __builtin_amdgcn_s_waitcnt(0xc07f | ((n) << 8))
#define PIN(x) asm volatile("" : "+v"(x))
#define SB() __builtin_amdgcn_sched_barrier(0)
#define INL __attribute__((always_inline))

#define STAMP(i) do {} while (0)

template <int N> struct Layer { static constexpr int value = N; };
struct Params {
  const void* x; const void* wA; const void* wphi; const void* woA; const void* wB; const void* woB; void* out; void* ws;
};

__global__ void __launch_bounds__(512, 2) fused_layers(Params) {
  typedef unsigned long long v8p __attribute__((ext_vector_type(8)));
  v8p args;
  asm volatile("s_load_dwordx16 %0, %1, 0x0" : "=s"(args) : "s"(__builtin_amdgcn_kernarg_segment_ptr()));
  unsigned long long born;
  asm volatile("s_memrealtime %0" : "=s"(born));
  const int b = blockIdx.x, xg = b & 7, xi = b >> 3;
  constexpr int HPX = 32 / TPX, NGT = 16 / TPX;
  const int t = (xg % NGT) * TPX + xi / HPX;
  const int h = (xg / NGT) * HPX + xi % HPX;
  const int wid = __builtin_amdgcn_readfirstlane(threadIdx.x >> 6) & 7;
  const int w_ = wid < 4 ? 7 - wid : wid - 4;
  const int l = threadIdx.x & 63, c = l & 15, g = l >> 4;
  const bool g3 = g == 3;
  STAMP(0);
  constexpr unsigned L_XS = 73728, L_JOB = L_WQ, L_CSP = L_XS + 81920;
  const unsigned chunk16 = ((l & 3) ^ ((l >> 5) << 1)) * 16, chunk56 = chunk16 == 48 ? 40 : chunk16, vqkv = (l >> 2) * (3 * DM * 2) + chunk56;
  const int kvp_ = (w_ >> 1) & 1, kvs = w_ & 1;
  const unsigned kvat = kvp_ * L_WV + kvs * 2048, kvso = kvs * (32 * 3 * DM * 2) + (kvp_ + 1) * DM * 2 + 56 * h;
  const unsigned xsb = L_XS + w_ * 10240, xso = (t * 128 + w_ * 16) * (DM * 2);
  const unsigned vx0 = (l >> 3) * (DM * 2) + ((l & 7) ^ (l >> 4)) * 16, vx1 = ((l >> 3) + 8) * (DM * 2) + ((l & 7) ^ (4 + (l >> 4))) * 16 - 1024;
  const bool job = t < 14 && (w_ < 4 || (w_ < 6 && h < 14));
  const unsigned jat = L_JOB + w_ * 2048;
  asm volatile("" :: "v"(vqkv), "v"(vx0), "v"(vx1), "s"(kvat), "s"(kvso), "s"(xsb), "s"(xso), "s"(jat));
  asm volatile("s_waitcnt lgkmcnt(0)" : "+s"(args), "+s"(born));
  const Params p = {(const void*)args[0], (const void*)args[1], (const void*)args[2], (const void*)args[3], (const void*)args[4], (const void*)args[5], (void*)args[6], (void*)args[7]};
  const v4u srdA = mk_srd(p.wA, DM * 3 * DM * 2), srdX = mk_srd(p.x, SEQ * DM * 2);
  auto kvblock = [&](int j) INL { const unsigned so = kvso + j * (64 * 3 * DM * 2); dma2(kvat + j * 4096, vqkv, vqkv, srdA, so, so + 16 * 3 * DM * 2 - 1024); };
  auto xline = [&](int j, int up) INL {
    const unsigned so = xso + up * (64 * DM * 2) + j * 128;
    dma2(xsb + up * 40960 + (j % 5) * 2048, vx0, vx1, srdX, so, so);
  };
  if (w_ < 4) {
    __builtin_amdgcn_s_setprio(PRI0);
    const unsigned so = t * (32 * 3 * DM * 2) + (w_ == 3 ? 0 : w_ * DM * 2) + 56 * (h ^ 1);
    if (job) dma2(jat, vqkv, vqkv, mk_srd(w_ ? p.wB : p.wA, DM * 3 * DM * 2), so, so + 16 * 3 * DM * 2 - 1024);
    xline(0, 0); xline(0, 1);
  } else {
    if (job) {
      const unsigned v = (l >> 2) * (DM * 2) + chunk16, so = t * (32 * DM * 2) + 64 * h;
      dma2(jat, v, v, mk_srd(w_ == 5 ? p.woB : p.woA, DM * DM * 2), so, so + 16 * DM * 2 - 1024);
    } else if (w_ == 7) {
      const unsigned e = (l >> 2) & 7, g0 = l >> 5, d0 = e < 4 ? 4 * g0 + e : 12 + 3 * g0 + e, d1 = d0 + (e < 4 ? 8 : 6);
      dma2(jat, e < 7 ? d0 * 56 + chunk56 + 1024 : OOB, e < 7 ? d1 * 56 + chunk56 : OOB,
           mk_srd((const char*)p.wphi - 1024, 16 * 28 * 28 * 2 + 1024), h * 28 * 28 * 2, h * 28 * 28 * 2);
    }
    kvblock(0); kvblock(1);
  }
  unsigned codepf = 0;
  if (w_ == 6) {
    unsigned long long pc;
    asm volatile("s_getpc_b64 %0" : "=s"(pc));
    const unsigned off = (l * 32 + xi) * 128;
    asm volatile("s_nop 4\n\tbuffer_load_dword %0, %1, %2, 0 offen" : "=v"(codepf) : "v"(off), "s"(mk_srd((const void*)pc, CODEB)) : "memory");
  }
  unsigned epoch, key0, key1, keyk;
  asm volatile("s_load_dword %0, %1, 0x0" : "=s"(epoch) : "s"(p.ws));
  const rsrc_t rs_ws = __builtin_amdgcn_make_buffer_rsrc(p.ws, 0, W_END, 0x00020000);
  const v4u srdW = mk_srd(p.ws, W_END);
  unsigned vl16 = l * 16;
  asm volatile("" : "+v"(vl16));
  const int ft = 7 * (h & 3) + min(w_, 6), nx = 2 * ft + (g >> 1);
  const unsigned pubx = w_ == 7 ? OOB : (g & 1) * 14336 + (nx >> 2) * 1024 + ((nx & 3) * 16 + c) * 16;
  const unsigned tw = (t * 8 + w_) * 14336, tp = (t * 8 + 2 * (h >> 2)) * 14336;
  const unsigned ptA = W_PT + h * 86016 + w_ * 2048;
  int budget = 1 << 16;
  v4u wphi[2];
  v4u park[8];
  v4u xf[14];
  v4f k0, k1, v0, v1, ck0, ck1, cv0, cv1;
  float var;
  v4f res0, res1;
  auto hint_put = [&](int slot, int ahead = 0) INL {
    __builtin_amdgcn_raw_buffer_store_b64(v2u{clock32() + ahead, key0}, rs_ws, vl16 < 256 ? vl16 >> 1 : OOB, W_HINT + slot * 32768 + (t * 16 + h) * 128, SC1);
  };
  auto hint_get = [&](unsigned v, unsigned so) INL { SB(); const v2u r = __builtin_amdgcn_raw_buffer_load_b64(rs_ws, v, so, SC1); SB(); return r; };
  const unsigned q4 = (l & 15) >> 2, p4 = l & 3;
  const lptr l16 = (lptr)(size_t)vl16, lq16 = lbase(L_WQ + l * 16);
  const lptr stw = lbase(L_SCR + c * 32 + ((g ^ (c >> 2)) * 8));
  const lptr str = lbase(L_SCR + (4 * g + q4) * 32 + ((p4 ^ g) * 8));
  const lptr sl16 = lbase(L_SCR + l * 16), csg = lbase(L_CS + g * 16);
  const unsigned vcol = l < 16 ? l * 2048 : OOB, vrow = l < 16 ? l * 128 : OOB;
  const float sel3 = g3 ? 1.f : 0.f;
  v4u hwt = weights(l);
  if (g3) { hwt.z = 0u; hwt.w = 0u; }
  const v4u zu = {0u, 0u, 0u, 0u};

  v4u wof[14];
  const unsigned wos = W_WO + ft * 14336;
  auto kv_gemm = [&](const int s0, const int s1, auto step) INL {
    const v4f z4 = {0.f, 0.f, 0.f, 0.f};
    if (s0 == 0) { k0 = z4; k1 = z4; v0 = z4; v1 = z4; }
    v4u a[8];
#pragma unroll
    for (int i = 0; i < 2; ++i) { const int o = (s0 + i) * 2048; a[4 * i] = lrd(l16 + o); a[4 * i + 1] = lrd(l16 + o + 1024); a[4 * i + 2] = lrd(l16 + L_WV + o); a[4 * i + 3] = lrd(l16 + L_WV + o + 1024); SB(); }
#pragma unroll
    for (int s = s0; s < s1; ++s) {
      const int i = 4 * (s & 1), o = (s + 2) * 2048;
      if (s < s1 - 1) LGKNOWN(4); else LGKNOWN(0);
      SB();
      k1 = mfma(a[i + 1], xf[s], k1); SB();
      step(s);
      if (s + 2 < s1) { a[i + 1] = lrd(l16 + o + 1024); SB(); }
      k0 = mfma(a[i], xf[s], k0); SB(); if (s + 2 < s1) { a[i] = lrd(l16 + o); SB(); }
      v0 = mfma(a[i + 2], xf[s], v0); SB(); if (s + 2 < s1) { a[i + 2] = lrd(l16 + L_WV + o); SB(); }
      v1 = mfma(a[i + 3], xf[s], v1); SB(); if (s + 2 < s1) { a[i + 3] = lrd(l16 + L_WV + o + 1024); SB(); }
    }
  };
  v4u hwk = weights(l);
  hwk.w &= 0xffffu;
  asm volatile("" : "+v"(hwk.z), "+v"(hwk.w), "+v"(hwt));
  v4u fl0, fl1;
  auto flags_wait = [&](v2u f, unsigned v, unsigned so, unsigned long long lanes) INL {
    while (nonzero((f.x ^ key0) | (f.y ^ key1), lanes) && spend(budget))
      asm volatile("s_sleep 3\n\tbuffer_load_dwordx2 %0, %1, %2, %3 offen sc1\n\ts_waitcnt vmcnt(0)" : "+v"(f) : "v"(v), "s"(srdW), "s"(so) : "memory");
  };
  unsigned vres0;
  {
    unsigned a0 = (8 * g + (c >> 2)) * 64 + ((((l & 3) >> 1) ^ ((g & 1) << 1)) * 16) + (l & 1) * 8;
    unsigned a1 = (a0 ^ 32) ^ ((l & 2) << 2);
    unsigned sl3 = c >= 12 ? 4 * c - 45 : c + ((c * 11) >> 5);
    const unsigned one = 0x3f803f80u;
    v4u one8 = {one, one, one, one};
    const v4f z4 = {0.f, 0.f, 0.f, 0.f};
    auto turn = [&](lptr r0, lptr r1, bool pad, v4u& f0, v4u& f1) INL {
      f0 = cat(trd(r0), trd(r0 + 256)); f1 = cat(trd(r1), trd(r1 + 256));
      f1 = v4u{pad ? one : f1.x, pad ? one : f1.y, pad ? one : f1.z, pad ? one : f1.w};
    };
    const unsigned xa = xsb + c * 128 + (g ^ (c >> 1)) * 16;
    const lptr xa0 = lbase(xa), xa1 = lbase(xa ^ 64), cp = lbase(L_CSP + g * 16);
    unsigned vres = (t * 128 + (2 * (h >> 2) + (g & 1)) * 16 + c) * (DM * 2) + nx * 16;
    unsigned pubx_ = pubx, vcol_ = vcol, vrow_ = vrow;
    float dg0 = c == 4 * g ? 1.f : 0.f, dg1 = c == 4 * g + 1 ? 1.f : 0.f, dg2 = c == 4 * g + 2 ? 1.f : 0.f, dg3 = c == 4 * g + 3 ? 1.f : 0.f;
    asm volatile("" : "+v"(vres), "+v"(a0), "+v"(a1), "+v"(sl3), "+v"(one8), "+v"(dg0), "+v"(dg1), "+v"(dg2), "+v"(dg3) : "v"(pubx_), "v"(vcol_), "v"(vrow_), "v"(sel3));
    {
      const bool spread = w_ == 7 || (w_ < 4 && w_ != 2);
      const unsigned slot = spread ? sl3 : c;
      const lptr jr0 = lbase(jat + a0), jr1 = lbase(jat + ((w_ & 6) == 4 ? a0 ^ 32 : a1));
      unsigned vj1 = ((w_ & 6) == 4 ? 14336 : 1024) + (vl16 & 768) + slot * 16, vcs = g == 0 ? c * 4 : g == 1 ? 64 + slot * 4 : OOB;
      unsigned dst = !job ? W_END : w_ < 4 ? W_PT + ((w_ ? 16 : 0) + (h ^ 1)) * 86016 + ((w_ ? w_ - 1 : 2) * 14 + t) * 2048 : W_WO + ((w_ - 4) * 28 + 2 * h) * 14336 + t * 1024;
      unsigned dcs = (job && w_ < 4) ? W_CS + ((h ^ 1) * 56 + w_ * 14 + t) * 128 : W_END;
      asm volatile("" : "+v"(vj1), "+v"(vcs));
      asm volatile("" : "+s"(dst), "+s"(dcs));
      STAMP(51);
      if (w_ < 4) { xline(1, 0); xline(1, 1); } else { kvblock(2); kvblock(3); }
      asm volatile("s_waitcnt lgkmcnt(0)" : "+s"(epoch) :: "memory");
      epoch += 1u;
      keys(epoch, key0, key1);
      keyk = ((vl16 & 512) ? key1 : key0) >> ((vl16 & 256) ? 14 : 0);
      asm volatile("" : "+v"(keyk));
      VMWAIT(8);
      STAMP(47);
      v4u f0, f1;
      turn(jr0, jr1, spread && c >= 12, f0, f1);
      if (w_ == 7) { lwr(lq16 + 7 * 2048, f0); lwr(lbase(jat + vj1), f1); }
      __builtin_amdgcn_raw_buffer_store_b128(f0, rs_ws, vl16, dst, SC1);
      __builtin_amdgcn_raw_buffer_store_b128(f1, rs_ws, vj1, dst, SC1);
      keep(f0, f1);
      const v4f c0 = mfma(one8, f0, z4), c1 = mfma(one8, f1, z4);
      __builtin_amdgcn_raw_buffer_store_b32(asu(g ? c1.x : c0.x), rs_ws, vcs, dcs, SC1);
      fence();
      STAMP(52);
    }
    const unsigned kslot = kvp_ ? c : sl3;
    const lptr kr0 = lbase(kvat + a0), kr1 = lbase(kvat + a1), kw0 = lbase(kvat + vl16), kw1 = lbase(kvat + 1024 + (vl16 & 768) + kslot * 16);
    const lptr cw0 = lbase(L_CSP + (w_ & 3) * 128 + c * 4), cw1 = lbase(L_CSP + (w_ & 3) * 128 + 64 + kslot * 4);
    const bool kpad = !kvp_ && c >= 12;
    v4f cs0 = z4, cs1 = z4;
    auto kvturn = [&](int j) INL {
      v4u f0, f1;
      turn(kr0 + j * 4096, kr1 + j * 4096, kpad, f0, f1);
      lwr(kw0 + j * 4096, f0); lwr(kw1 + j * 4096, f1);
      cs0 = mfma(one8, f0, cs0); cs1 = mfma(one8, f1, cs1);
    };
    if (w_ >= 4) {
      VMWAIT(7); kvturn(0); kvturn(1);
      VMWAIT(3); kvturn(2); kvturn(3);
      kvblock(4); xline(4, 0); kvblock(5); kvblock(6);
      VMWAIT(8);
    } else {
      xline(2, 0); xline(2, 1); xline(3, 0); xline(3, 1);
      __builtin_amdgcn_s_setprio(0);
      xline(4, 0);
      VMWAIT(2);
    }
    asm volatile("" :: "v"(codepf));
    STAMP(48);
    wg_bar();
    STAMP(49);
#pragma unroll
    for (int j = 0; j < 2; ++j) { xf[2 * j] = lrd(xa0 + j * 2048); xf[2 * j + 1] = lrd(xa1 + j * 2048); }
    if (w_ == 6) __builtin_amdgcn_raw_buffer_store_b64(v2u{key0, key1}, rs_ws, vl16 < 256 ? vl16 >> 1 : OOB, W_FLAG + (t * 16 + h) * 128, SC1);
    v4f gram = z4, pa, pb;
    kv_gemm(0, 8, [&](int s) INL {
      gram = mfma(xf[s], xf[s], gram);
      if (s < 2) { xf[4 + 2 * s] = lrd(xa0 + (2 + s) * 2048); xf[5 + 2 * s] = lrd(xa1 + (2 + s) * 2048); }
      if (s == 1) xline(5, 0);
      if (s == 3) xline(6, 0);
      if (s == 5) { wphi[0] = lrd(lq16 + 7 * 2048); wphi[1] = lrd(lq16 + 7 * 2048 + 1024); }
      SB();
    });
    STAMP(50);
    v2u fc;
    const unsigned vfl = w_ == 7 ? (vl16 & 240) * 128 : (vl16 & 240) * 128 + (vl16 >> 8) * 128, sfl = W_FLAG + (w_ == 7 ? h ^ 1 : (7 * (h & 3)) >> 1) * 128;
    const bool now = w_ >= 6 && (int)(clock32() - (unsigned)born) >= P_F;
    if (now) asm volatile("s_nop 4\n\tbuffer_load_dwordx2 %0, %1, %2, %3 offen sc1\n\ts_waitcnt vmcnt(1)" : "=&v"(fc) : "v"(vfl), "s"(srdW), "s"(sfl) : "memory");
    else drain();
    if (w_ >= 4) { kvturn(4); kvturn(5); kvturn(6); *(LAS float*)(cw0) = cs0.x; *(LAS float*)(cw1) = cs1.x; }
#pragma unroll
    for (int j = 4; j < 7; ++j) { xf[2 * j] = lrd(xa0 + (j % 5) * 2048); xf[2 * j + 1] = lrd(xa1 + (j % 5) * 2048); }
    if (w_ >= 6) {
      asm volatile("s_waitcnt vmcnt(0)" : "+v"(fc) :: "memory");
      if (!now) { pause(P_F - 8 - (int)(clock32() - (unsigned)born)); fc = v2u{~key0, 0u}; }
      flags_wait(fc, vfl, sfl, ~0ull);
    }
    wg_bar();
    STAMP(54);
    kv_gemm(8, 14, [&](int s) INL {
      if (s < 11) { gram = mfma(xf[2 * s - 8], xf[2 * s - 8], gram); gram = mfma(xf[2 * s - 7], xf[2 * s - 7], gram); }
      if (s == 8) {
        const unsigned so = w_ >= 4 ? W_CS + (h * 56 + 14 * (w_ & 3)) * 128 : W_END;
        fl0 = ld16(rs_ws, vl16, so); fl1 = ld16(rs_ws, vl16 < 768 ? vl16 + 1024 : OOB, so);
        dma2s(L_WQ + w_ * 2048, vl16, srdW, ptA + L_WQ);
      }
      if (s == 9 && w_ < 6) dma2s(L_WQ + 16384 + w_ * 2048, vl16, srdW, ptA + L_WQ + 16384);
      if (s >= 10) {
#pragma unroll
        for (int n = NW0 * (s - 10); n < NW0 * (s - 9); ++n) wof[n] = ld16(rs_ws, w_ == 7 ? OOB : vl16, wos + n * 1024);
      }
      if (s >= 9 && s < 13) { const v4f u = pa + pb; if (s == 9) ck0 = u; else if (s == 10) ck1 = u; else if (s == 11) cv0 = u; else cv1 = u; }
      if (s < 12) { const int o = ((s - 8) >> 1) * 256 + ((s - 8) & 1) * 64; pa = *(LAS v4f*)(cp + o); pb = *(LAS v4f*)(cp + o + 128); }
      if (s == 12) var = xrow(1.f / DM, (gram.x * dg0 + gram.y * dg1) + (gram.z * dg2 + gram.w * dg3));
      SB();
    });
    if (w_ == 6) { *(LAS v4f*)(csg) = ck0; *(LAS v4f*)(csg + 64) = ck1; *(LAS v4f*)(csg + 128) = cv0; *(LAS v4f*)(csg + 192) = cv1; }
    vres0 = vres;
  }
  STAMP(56);
  int xwait;
  v4u kc = {key0, 0u, key1, 0u};
  PIN(kc);
  auto run_layer = [&](auto kind, auto role) INL {
    constexpr int layer = decltype(kind)::value, rep = layer >> 1;
    constexpr bool isA = !(layer & 1), first = layer == 0, more = layer < 3, lead = decltype(role)::value;
    int w = w_;
    asm volatile("" : "+s"(w));
    __builtin_assume(lead ? (w >= 4 && w < 8) : (w >= 0 && w < 4));
    const v4f z4 = {0.f, 0.f, 0.f, 0.f};
    const unsigned wob = (lead && w == 7) ? W_END - 12288 : wos + (isA ? 0 : 28 * 14336), wov = (lead && w == 7) ? OOB : vl16;
    auto load_wo = [&](int s0, int s1) INL {
#pragma unroll
      for (int s = s0; s < s1; ++s) wof[s] = ld16(rs_ws, wov + (s & 3) * 1024, wob + (s >> 2) * 4096);
    };
    v2u hh;
    unsigned vh;
    const unsigned shh = W_HINT + 2 * layer * 32768 + (t - 2) * 2048 + h * 128;
    if (!isA && lead) { const unsigned lo = lane_id(); vh = lo >= 16 ? OOB : (lo < 2 && (int)lo < t) ? (1 - lo) * 2048 : 4096; }
    const lptr cs = csg + (isA ? 0 : 384);
    float rstd, nmr;
    auto stats = [&]() INL {
      const float mean = k1.w * (1.f / DM);
      rstd = __builtin_amdgcn_rsqf(mx(fmaf(-mean, mean, var), 0.f) + 1e-5f); nmr = -mean * rstd;
      PIN(rstd); PIN(nmr);
    };
    unsigned hp = t < 15 ? W_HALO + rep * 4194304 + h * 262144 + (t * 8 + w) * 2048 : W_END;
    if (!isA) asm volatile("" : "+s"(hp));
    if (!first) {
      unsigned xs[4], sts = W_ST + layer * 131072 + (t * 8 + w) * 1024;
#pragma unroll
      for (int i = 0; i < 4; ++i) { xs[i] = W_X + layer * TILEBUF + tw + i * 4096; asm volatile("" : "+s"(xs[i])); }
      asm volatile("" : "+s"(sts));
      if (!isA && lead) PIN(vh);
      pause(xwait);
      auto ldx = [&](int s) INL { xf[s] = ld16(rs_ws, vl16 + (s & 3) * 1024, xs[s >> 2]); };
      for (;;) {
#pragma unroll
        for (int s = 0; s < WIN; ++s) ldx(s);
        STAMP(2 + layer * 12);
        v4u st;
        unsigned c0 = 0u - key0, c1 = 0u - key1;
        kv_gemm(0, 14, [&](int s) INL {
          const int n0 = WIN + s + (s < RAMP ? s : RAMP), n1 = WIN + s + 1 + (s + 1 < RAMP ? s + 1 : RAMP);
#pragma unroll
          for (int n = n0; n < n1; ++n) {
            if (n < 14) ldx(n);
            if (n == 14) st = ld16(rs_ws, vl16, sts);
            if (n == 15 && !isA && lead) hh = hint_get(vh, w == 4 ? shh : W_END);
          }
          c0 += xf[s].x + xf[s].z; c1 += xf[s].y + xf[s].w; PIN(c0); PIN(c1);
          if (s == 13) { var = xrow(1.f / DM, asf(st.x)); c0 += st.x + st.y; c1 += st.z; PIN(c0); PIN(c1); }
          if (s == 12) { ck0 = *(LAS v4f*)(cs); ck1 = *(LAS v4f*)(cs + 64); cv0 = *(LAS v4f*)(cs + 128); cv1 = *(LAS v4f*)(cs + 192); }
          SB();
        });
        c0 = xsum2u(c0, c1);
        stats();
        if (__builtin_expect(!nonzero(c0), 1) || !spend(budget)) break;
        __builtin_amdgcn_s_sleep(4);
      }
    }
    STAMP(3 + layer * 12);
    if (first) stats();
    auto ln7 = [&](v4f a0, v4f a1, v4f c0, v4f c1, float sc, float sh, float pad) INL {
      a0 = a0 * sc + c0 * sh;
      const v2f u = v2f{a1.x, a1.y} * sc + v2f{c1.x, c1.y} * sh;
      return v4u{pk(a0.x, a0.y), pk(a0.z, a0.w), pk(u.x, u.y), pk(fmaf(a1.z, sc, c1.z * sh), pad)};
    };
    auto q_gemm = [&](float sc, float pad) INL {
      v4f q0 = z4, q1 = z4, c0, c1;
      const lptr lq = lq16 + (isA ? 0 : L_WQB - L_WQ);
      v4u a[8];
#pragma unroll
      for (int i = 0; i < 8; ++i) { a[i] = lrd(lq + i * 1024); SB(); }
#pragma unroll
      for (int s = 0; s < 14; ++s) {
        const int i = (2 * s) & 7;
        if (!(s & 1)) { if (s < 12) LGKNOWN(4); else LGKNOWN(0); SB(); }
        q0 = mfma(a[i], xf[s], q0); SB(); if (s + 4 < 14) { a[i] = lrd(lq + (s + 4) * 2048); SB(); }
        q1 = mfma(a[i + 1], xf[s], q1); SB(); if (s + 4 < 14) { a[i + 1] = lrd(lq + (s + 4) * 2048 + 1024); SB(); }
        if (s == 12) { c0 = *(LAS v4f*)(cs + 256); c1 = *(LAS v4f*)(cs + 320); SB(); }
      }
      v4u r = ln7(q0, q1, c0, c1, rstd * sc, nmr * sc, pad);
      asm volatile("" : "+v"(r) :: "memory");
      return r;
    };
    constexpr float L2E = 1.4426950408889634f;
    const v4u kf = isA ? ln7(k0, k1, ck0, ck1, rstd * L2E, nmr * L2E, 0.f) : ln7(k0, k1, ck0, ck1, rstd, nmr, 0.f);
    auto v_pack = [&]() INL {
      const v4f a0 = v0 * rstd + cv0 * nmr, a1 = v1 * rstd + cv1 * nmr;
      return v4u{pk(a0.x, a0.y), pk(a0.z, a0.w), g3 ? 0x3f80u : pk(a1.x, a1.y), g3 ? 0u : pk(a1.z, a1.w)};
    };
    auto swap_kv = [&](int j0, int j1) INL {
#pragma unroll
      for (int j = j0; j < j1 && j < (lead ? 3 : 4); ++j) {
        const lptr a = lbase(w * 2048 + j * 16384 + vl16);
        v4u t0, t1;
        if (layer < 2) { t0 = lrd(a); t1 = lrd(a + 1024); }
        if (!first) { lwr(a, park[2 * j]); lwr(a + 1024, park[2 * j + 1]); }
        if (layer < 2) { park[2 * j] = t0; park[2 * j + 1] = t1; }
      }
      if (first) {
        asm volatile("s_waitcnt lgkmcnt(0)" ::: "memory");
#pragma unroll
        for (int j = j0; j < j1 && j < (lead ? 3 : 4); ++j) dma2s(w * 2048 + j * 16384, vl16, srdW, ptA + 16 * 86016 + j * 16384);
      }
    };
    v4u xres;
    v4f o0, o1;
    float den;
    const unsigned soh = W_HINT + (2 * layer + 1) * 32768 + t * 2048;
    v2u oh;

    if constexpr (isA) {
      if (lead && w == 7) hint_put(2 * layer);
      if (!first) { if (!lead) load_wo(6, 10); }
      else if (lead) {
        v4f cq = __builtin_bit_cast(v4f, fl0) + __builtin_bit_cast(v4f, fl1);
#pragma unroll
        for (int i = 0; i < 4; ++i) {
          float u = cq[i];
          u += asf(__builtin_amdgcn_update_dpp(0, asu(u), 0x128, 0xf, 0xf, false));
          cq[i] = xrow(1.f, u);
        }
        *(LAS v4f*)(lbase(L_CS + 256 + (w & 3) * 128 + (vl16 & 127))) = cq;
      }
      if (first) load_wo(4 * NW0, 4 * NW0 + 3);
      const v4u vf = v_pack();
      v4u pp, pm, qf;
      feature_map(kf, wphi, pp, pm, [&]() INL { SB(); if (first) load_wo(4 * NW0 + 3, 4 * NW0 + 6); else if (!lead) load_wo(10, 14); });
      {
        const lptr tb = stw + w * 3072;
        *(LAS v2u*)(tb) = v2u{pp.x, pp.y}; *(LAS v2u*)(tb + 512) = v2u{pp.z, pp.w};
        *(LAS v2u*)(tb + 1024) = v2u{pm.x, pm.y}; *(LAS v2u*)(tb + 1536) = v2u{pm.z, pm.w};
        *(LAS v2u*)(tb + 2048) = v2u{vf.x, vf.y}; *(LAS v2u*)(tb + 2560) = v2u{vf.z, vf.w};
      }
      if (first) VMWAIT(6);
      v4u hwk_ = {hwt.x, hwt.y, hwk.z, hwk.w};
      unsigned kvp = W_KVP + rep * 1048576 + h * 65536 + (w & 3) * 1024 + (lead ? t * 4096 : 0);
      const lptr pa = lbase((size_t)str + (w & 2) * 512), pb = lbase((size_t)str + 2048 + (w & 1) * 512);
      asm volatile("" : "+v"(hwk_));
      asm volatile("" : "+s"(kvp));
      wg_bar();
      STAMP(4 + layer * 12);
      auto residual = [&]() INL {
        xres = __builtin_amdgcn_raw_buffer_load_b128(__builtin_amdgcn_make_buffer_rsrc((void*)p.x, 0, SEQ * DM * 2, 0x00020000), vres0, 0, 0); SB();
      };
      if (lead) {
        __builtin_amdgcn_s_setprio(2);
        v4u bv[4], x0[4], x1[4];
#pragma unroll
        for (int kk = 0; kk < 4; ++kk) {
          bv[kk] = cat(trd(pb + kk * 6144), trd(pb + kk * 6144 + 3072));
          x0[kk] = cat(trd(pa + kk * 6144), trd(pa + kk * 6144 + 3072));
          x1[kk] = cat(trd(pa + kk * 6144 + 512), trd(pa + kk * 6144 + 3072 + 512));
          SB();
        }
        v4f d0 = z4, d1 = z4;
#pragma unroll
        for (int kk = 0; kk < 4; ++kk) { d0 = mfma(x0[kk], bv[kk], d0); d1 = mfma(x1[kk], bv[kk], d1); }
        v4u dv = {pk(d0.x, d0.y), pk(d0.z, d0.w), pk(d1.x, d1.y), pk(d1.z, 0.f)};
        const v4u hs = imma(hwk_, dv, zu);
        dv.w |= ((hs.x + keyk) & 0x3fffu) << 16;
        __builtin_amdgcn_raw_buffer_store_b128(dv, rs_ws, vl16, kvp, SC1);
        keep(dv);
        __builtin_amdgcn_s_setprio(0);
        STAMP(5 + layer * 12);
        if (first) {
          load_wo(4 * NW0 + 6, 14); residual();
#pragma unroll
          for (int j = 0; j < 4; ++j) if (j < 3 || w < 6) dma2s(L_WQB + (w & 3) * 2048 + j * 8192, vl16, srdW, ptA - 8192 + 16 * 86016 + L_WQ + j * 8192);
        } else load_wo(6, 14);
        qf = q_gemm(L2E, 0.f);
        feature_map(qf, wphi, pp, pm, []() {});
        PIN(pp); PIN(pm);
        if (first && KVE) swap_kv(0, 4);
      } else {
        const unsigned sh = W_HINT + 2 * layer * 32768 + h * 128;
        __builtin_amdgcn_s_setprio(1);
        __builtin_amdgcn_s_sleep(DLY_T);
        const v2u hv = hint_get(vcol, sh);
        if (first) { load_wo(4 * NW0 + 6, 14); residual(); }
        qf = q_gemm(L2E, 0.f);
        pause(lateness(hv, t, 150, key0, srdW, vcol, sh, budget) + T_KV);
        if (w == 0) hint_put(2 * layer + 1);
        fence();
        STAMP(6 + layer * 12);
        v4u kp[16];
#pragma unroll
        for (int i = 0; i < 8; ++i) kp[i] = ld16(rs_ws, vl16, kvp + i * 4096);
        feature_map(qf, wphi, pp, pm, [&]() INL {
          SB();
#pragma unroll
          for (int i = 8; i < 16; ++i) kp[i] = ld16(rs_ws, vl16, kvp + i * 4096);
        });
        PIN(pp); PIN(pm);
        const unsigned lo = lane_id();
        const unsigned bit = ((lo >> 4) == ((lo >> 2) & 3)) ? ((lo & 1) ? 0x3f800000u : 0x3f80u) : 0u;
        const unsigned u0 = (lo & 2) ? 0u : bit, u1 = (lo & 2) ? bit : 0u;
        const v4u sa = {u0, u1, 0u, 0u}, sb = {0u, 0u, u0, u1};
        const lptr kd = lbase((size_t)sl16 + (L_KVF - L_SCR) + w * 1024);
        auto add_up = [&](bool once) INL {
          v4f lo4 = z4, hi4 = z4;
          v4u hs = zu;
          unsigned ck = 0u - 16u * keyk;
          SB();
#pragma unroll
          for (int i = 0; i < 16; ++i) {
            hs = imma(hwk_, kp[i], hs); lo4 = mfma(sa, kp[i], lo4); hi4 = mfma(sb, kp[i], hi4);
            ck += kp[i].w >> 16; PIN(ck);
            SB();
          }
          const bool bad = nonzero((hs.x - ck) & 0x3fffu);
          SB();
          lwr(kd, v4u{pk(lo4.x, lo4.y), pk(lo4.z, lo4.w), pk(hi4.x, hi4.y), pk(hi4.z, 0.f)});
          return bad;
        };
        if (__builtin_expect(add_up(true), 0)) {
          do {
#pragma unroll
            for (int i = 0; i < 16; i += 4) reload4(kp[i], kp[i + 1], kp[i + 2], kp[i + 3], srdW, vl16, kvp + i * 4096, kvp + (i + 1) * 4096, kvp + (i + 2) * 4096, kvp + (i + 3) * 4096);
          } while (add_up(false) && spend(budget));
        }
      }
      __builtin_amdgcn_s_setprio(0);
      wg_bar();
      STAMP(7 + layer * 12);
      v4u kv[4];
      kv[0] = lrd(sl16 + (L_KVF - L_SCR) + 1024); SB(); kv[1] = lrd(sl16 + (L_KVF - L_SCR) + 3072); SB();
      kv[2] = lrd(sl16 + (L_KVF - L_SCR)); SB(); kv[3] = lrd(sl16 + (L_KVF - L_SCR) + 2048);
      SB();
      if (lead) oh = hint_get(vrow, w == 7 ? soh : W_END);
      o1 = mfma(kv[0], pp, z4); o1 = mfma(kv[1], pm, o1); SB();
      o0 = mfma(kv[2], pp, z4); o0 = mfma(kv[3], pm, o0); SB();
      { const v4f e6 = {1e-6f, 1e-6f, 1e-6f, 1e-6f}, r = __builtin_amdgcn_mfma_f32_16x16x4f32(sel3, o1.x, e6, 0, 0, 0); den = r.x; }
    } else {
      const unsigned hb = W_HALO + rep * 4194304 + h * 262144;
      __builtin_amdgcn_raw_buffer_store_b128(kf, rs_ws, vl16, hp, SC1); SB();
      const v4u hsk = imma(hwt, kf, v4u{kf.z + key0, 0u, kf.w + key1, 0u}); SB();
      v4u vf = v_pack();
      const v4u hs_v = imma(hwt, vf, hsk); SB();
      const lptr kw = sl16 + w * 2048, vw = stw + w * 2048, vr = str + w * 2048;
      *(LAS v2u*)(vw + 32768 + 1024) = v2u{vf.x, vf.y}; *(LAS v2u*)(vw + 32768 + 1536) = v2u{vf.z, vf.w};
      if (!lead || w < 7) lwr(kw + 32768, kf);
      SB();
      vf.z = g3 ? (hs_v.y << 13) + hs_v.x : vf.z; vf.w = g3 ? (hs_v.w << 13) + hs_v.z : vf.w;
      __builtin_amdgcn_raw_buffer_store_b128(vf, rs_ws, vl16 + 1024, hp, SC1); keep(vf);
      STAMP(4 + layer * 12);
      v4u hk[2], hv[2];
      unsigned hs[2];
#pragma unroll
      for (int i = 0; i < 2; ++i) hs[i] = t - 2 + i >= 0 ? hb + ((t - 2 + i) * 8 + w) * 2048 : W_END;
      int hlate;
      auto fetch = [&](int margin) INL {
        pause(__builtin_amdgcn_readfirstlane(hlate) + margin);
#pragma unroll
        for (int i = 0; i < 2; ++i) { hk[i] = ld16(rs_ws, vl16, hs[i]); hv[i] = ld16(rs_ws, vl16 + 1024, hs[i]); }
      };
      if (lead && w == 4) *(LAS int*)(csg + 896) = lateness(hh, 2, 200, key0, srdW, vh, shh, budget);
      if (lead) load_wo(6, 10);
      wg_bar();
      hlate = *(LAS int*)(csg + 896);
      if (!lead) { fetch(T_H); __builtin_amdgcn_s_setprio(1); }
      const v4u qf = q_gemm(0.18898223650461357f * L2E, 1.f);
      if (lead) { fetch(T_HL); load_wo(10, 14); } else load_wo(6, 10);
      const float NEG = -3.0e38f;
      v4f sd = mfma(kf, qf, z4);
#pragma unroll
      for (int r = 0; r < 4; ++r) sd[r] = (4 * g + r <= c) ? sd[r] : NEG;
      float m = xmax1(mx(mx3(sd.x, sd.y, sd.z), sd.w));
      auto diag = [&]() INL {
        const v2u a0 = trd(vr + 32768 + 1024), a1 = trd(vr + 32768 + 1536);
        const v4f e = sd - m;
        const v2u pd = {pk(ex2(e.x), ex2(e.y)), pk(ex2(e.z), ex2(e.w))};
        typedef short v4h __attribute__((ext_vector_type(4)));
        o0 = __builtin_amdgcn_mfma_f32_16x16x16bf16_1k(__builtin_bit_cast(v4h, a0), __builtin_bit_cast(v4h, pd), z4, 0, 0, 0);
        o1 = __builtin_amdgcn_mfma_f32_16x16x16bf16_1k(__builtin_bit_cast(v4h, a1), __builtin_bit_cast(v4h, pd), z4, 0, 0, 0);
      };
      diag();
      auto take_halo = [&]() INL {
        STAMP(6 + layer * 12);
        const unsigned nv = min(t, 2);
        auto stage = [&]() INL {
          v4u hs_ = zu;
          unsigned e0 = 0u - nv * key0, e1 = 0u - nv * key1;
#pragma unroll
          for (int i = 0; i < 2; ++i) {
            const unsigned none = t - 2 + i < 0 ? 0xfe000000u : 0u;
            hs_ = imma(hwt, hk[i], hs_); SB();
            hs_ = imma(hwt, hv[i], hs_); SB();
            e0 += hv[i].z - hk[i].z; e1 += hv[i].w - hk[i].w; PIN(e0); PIN(e1);
            lwr(kw + i * 16384, v4u{hk[i].x, hk[i].y, hk[i].z, hk[i].w | none});
            *(LAS v2u*)(vw + i * 16384 + 1024) = v2u{hv[i].x, hv[i].y};
            *(LAS v2u*)(vw + i * 16384 + 1536) = v2u{g3 ? 0x3f80u : hv[i].z, g3 ? 0u : hv[i].w};
            SB();
          }
          return row3_differs(hs_, e0, e1);
        };
        if (__builtin_expect(stage(), 0)) {
          do reload4(hk[0], hk[1], hv[0], hv[1], srdW, vl16, hs[0], hs[1], min(hs[0] + 1024, W_END), min(hs[1] + 1024, W_END));
          while (stage() && spend(budget));
        }
        wg_bar();
        STAMP(7 + layer * 12);
      };
      {
        const v4f cm = {-m, -m, -m, -m};
        v4f sa, sb; v4u a0, a1, b0, b1, pq;
#pragma unroll
        for (int j = 7; j >= 0; --j) {
          if (j >= 4 && (j < 6) == lead && (w >> 1) == 7 - j) take_halo();
          if (j == (lead ? 4 : 6)) VMKNOWN(4);
          if (!lead && j == 6 && w == 3) hint_put(2 * layer + 1);
          if (!lead && j <= 5 && j >= 4) load_wo(10 + 2 * (5 - j), 12 + 2 * (5 - j));
          if (!lead && j == PBACK) __builtin_amdgcn_s_setprio(0);
          if (!lead && j == POLL_OB) oh = hint_get(vrow, w == 3 ? soh : W_END);
          const v4u ka = lrd(kw + j * 4096), kb = lrd(kw + j * 4096 + 2048);
          const v4u n0 = cat(trd(vr + j * 4096 + 1024), trd(vr + j * 4096 + 3072)), n1 = cat(trd(vr + j * 4096 + 1536), trd(vr + j * 4096 + 3584));
          SB();
          if (j < 7) {
            const float e0 = ex2(sa.x), e1 = ex2(sa.y); SB();
            if (j < 6) { o0 = mfma(b0, pq, o0); SB(); }
            const float e2 = ex2(sa.z), e3 = ex2(sa.w); SB();
            if (j < 6) { o1 = mfma(b1, pq, o1); SB(); }
            const float e4 = ex2(sb.x), e5 = ex2(sb.y); SB();
            sa = mfma(ka, qf, cm); SB();
            const float e6 = ex2(sb.z), e7 = ex2(sb.w); SB();
            sb = mfma(kb, qf, cm); SB();
            pq = v4u{pk(e0, e1), pk(e2, e3), pk(e4, e5), pk(e6, e7)};
            b0 = a0; b1 = a1;
          } else {
            sa = mfma(ka, qf, cm); sb = mfma(kb, qf, cm);
          }
          a0 = n0; a1 = n1;
          SB();
        }
        o0 = mfma(b0, pq, o0); o1 = mfma(b1, pq, o1); SB();
#pragma unroll
        for (int r = 0; r < 4; ++r) sa[r] = (4 * g + r > c) ? sa[r] : NEG;
        const v4u pf = pexp(sa, sb);
        o1 = mfma(a1, pf, o1); o0 = mfma(a0, pf, o0);
      }
      den = xrow(sel3, o1.x);
      if (__builtin_expect(__any(!(den < 1.0e18f)), 0)) {
        float mt = m;
#pragma unroll 1
        for (int k = 0; k < 16; ++k) {
          v4f s = mfma(lrd(kw + k * 2048), qf, z4);
#pragma unroll
          for (int r = 0; r < 4; ++r) s[r] = (k > 0 || 4 * g + r > c) ? s[r] : NEG;
          mt = mx3(mt, mx(s.x, s.y), mx(s.z, s.w));
        }
        m = xmax1(mt);
        diag();
        const v4f cm = {-m, -m, -m, -m};
#pragma unroll 1
        for (int j = 7; j >= 0; --j) {
          v4f sa = mfma(lrd(kw + j * 4096), qf, cm);
          const v4f sb = mfma(lrd(kw + j * 4096 + 2048), qf, cm);
#pragma unroll
          for (int r = 0; r < 4; ++r) sa[r] = (j > 0 || 4 * g + r > c) ? sa[r] : NEG;
          const v4u pf = pexp(sa, sb);
          o0 = mfma(cat(trd(vr + j * 4096 + 1024), trd(vr + j * 4096 + 3072)), pf, o0);
          o1 = mfma(cat(trd(vr + j * 4096 + 1536), trd(vr + j * 4096 + 3584)), pf, o1);
        }
        den = xrow(sel3, o1.x);
      }
    }
    const unsigned ob = W_O + layer * 2097152 + t * 131072;
    {
      const float rd = __builtin_amdgcn_rcpf(den);
      o0 *= rd; o1 *= rd;
      v4u v = {pk(o0.x, o0.y), pk(o0.z, o0.w), pk(o1.x, o1.y), pk(o1.z, o1.w)};
      const v4u hs = imma(hwt, v, kc);
      v.z = g3 ? (hs.y << 13) + hs.x : v.z; v.w = g3 ? (hs.w << 13) + hs.z : v.w;
      __builtin_amdgcn_raw_buffer_store_b128(v, rs_ws, vl16, ob + (w * 16 + h) * 1024, SC1);
      keep(v);
    }
    __builtin_amdgcn_s_setprio(0);
    STAMP(8 + layer * 12);
    if (first) {
      const v4u xr = swap16(xres);
      res0 = v4f{asf(xr.x << 16), asf(xr.x & 0xffff0000u), asf(xr.y << 16), asf(xr.y & 0xffff0000u)};
      res1 = v4f{asf(xr.z << 16), asf(xr.z & 0xffff0000u), asf(xr.w << 16), asf(xr.w & 0xffff0000u)};
    }
    unsigned op = ob + (2 * (h >> 2) + (lead ? 0 : 1)) * 16384 + (w & 3) * 1024;
    asm volatile("" : "+s"(op));
    v4u of[4];
    if (isA && !(first && KVE && lead)) swap_kv(0, first && KVE ? 4 : 2);
    if (lead == isA && w == (isA ? 7 : 3)) *(LAS int*)(csg + 768) = lateness(oh, h, isA ? 165 : 125, key0, srdW, vrow, soh, budget);
    wg_bar();
    const int olate = __builtin_amdgcn_readfirstlane(*(LAS int*)(csg + 768));
    if (!isA && more) swap_kv(0, 2);
    pause(olate + (isA ? T_OA : T_OB) + (more ? 0 : 6));
#pragma unroll
    for (int i = 0; i < 4; ++i) of[i] = ld16(rs_ws, vl16, op + i * 4096);
    if (more && !first) swap_kv(2, 4);
    const unsigned qd = 7 * (w & 3) + (vl16 >> 8), qa = L_SCR + (lead ? 0 : 14336) + (qd >> 1) * 256 + (qd & 1) * 8 + (vl16 & 255);
    const lptr lo = lbase(qa), hi = lbase(g3 ? L_SCR + 32768 + vl16 : qa + 512);
    STAMP(9 + layer * 12);
    auto stage_o = [&]() INL {
      auto put = [&]() INL {
        v4u hs = zu;
        unsigned e0 = 0u - 4u * key0, e1 = 0u - 4u * key1;
#pragma unroll
        for (int i = 0; i < 4; ++i) {
          hs = imma(hwt, of[i], hs); SB();
          *(LAS v2u*)(lo + i * 3584) = v2u{of[i].x, of[i].y};
          *(LAS v2u*)(hi + i * 3584) = v2u{of[i].z, of[i].w};
          e0 += of[i].z; e1 += of[i].w; PIN(e0); PIN(e1);
          SB();
        }
        return row3_differs(hs, e0, e1);
      };
      if (__builtin_expect(put(), 0)) {
        do reload4(of[0], of[1], of[2], of[3], srdW, vl16, op, op + 4096, op + 8192, op + 12288);
        while (put() && spend(budget));
      }
    };
    stage_o();
    wg_bar();
    if (first && !KVE) swap_kv(2, 4);
    const unsigned sx = W_HINT + (8 + layer) * 32768 + t * 2048;
    if (lead && w == 7) {
      if (more) {
        hint_put(8 + layer);
        __builtin_amdgcn_s_sleep(P_X);
        *(LAS int*)(csg + 832) = lateness(hint_get(vrow, sx), h, 70, key0, srdW, vrow, sx, budget);
      }
    }
    int w2 = w;
    asm volatile("" : "+s"(w2));
    if (!lead || w2 != 7) {
      const unsigned won = wos + (isA ? 28 * 14336 : 0);
      v4u bq[8];
#pragma unroll
      for (int i = 0; i < 4; ++i) { bq[2 * i] = lrd(sl16 + i * 1024); SB(); bq[2 * i + 1] = lrd(sl16 + 14336 + i * 1024); SB(); }
#pragma unroll
      for (int s = 0; s < 14; ++s) {
        const int i = (2 * s) & 7;
        if (!(s & 1)) { if (s < 12) LGKNOWN(4); else LGKNOWN(0); SB(); }
        res0 = mfma(wof[s], bq[i], res0); SB(); if (s + 4 < 14) { bq[i] = lrd(sl16 + (s + 4) * 1024); SB(); }
        res1 = mfma(wof[s], bq[i + 1], res1); SB(); if (s + 4 < 14) { bq[i + 1] = lrd(sl16 + 14336 + (s + 4) * 1024); SB(); }
        if (more && s >= 1 && (s - 1) % WOS == 0 && (s - 1) / WOS < 6) { const int n = (s - 1) / WOS; wof[n] = ld16(rs_ws, vl16 + (n & 3) * 1024, won + (n >> 2) * 4096); }
      }
      const v4u xo = swap16(v4u{pk(res0.x, res0.y), pk(res0.z, res0.w), pk(res1.x, res1.y), pk(res1.z, res1.w)});
      if (more) {
        __builtin_amdgcn_raw_buffer_store_b128(xo, rs_ws, pubx, W_X + (layer + 1) * TILEBUF + tp, SC1);
        keep(xo);
        auto r = __builtin_amdgcn_permlane32_swap(xo.x + xo.z, xo.y + xo.w, false, false);
        const unsigned r0 = r[0], r1 = r[1];
        *(LAS v2u*)(lbase(L_SCR + 28672 + w * 512 + (vl16 >> 1))) = v2u{r0 + r1, asu(sumsq8(xo))};
      } else {
        const unsigned lo = lane_id();
        __builtin_amdgcn_raw_buffer_store_b128(xo, __builtin_amdgcn_make_buffer_rsrc(p.out, 0, SEQ * DM * 2, 0x00020000),
                                               (lo & 31) * (DM * 2) + (2 * ft + (lo >> 5)) * 16, (t * 128 + (h >> 2) * 32) * (DM * 2), 0);
        keep(xo);
      }
    }
    STAMP(10 + layer * 12);
    if (more) {
      if (first) VMWAIT(7);
      wg_bar();
      const int late = __builtin_amdgcn_readfirstlane(*(LAS int*)(csg + 832));
      if (lead && w2 == 7) {
        float q = 0.f;
        unsigned a = 0u;
        const lptr sp = lbase(L_SCR + 28672 + (vl16 >> 1));
#pragma unroll
        for (int i = 0; i < 7; ++i) { const v2u v = *(LAS v2u*)(sp + i * 512); a += v.x; q += asf(v.y); }
        auto r = __builtin_amdgcn_permlane32_swap(asu(q), asu(q), false, false);
        auto ra = __builtin_amdgcn_permlane32_swap(a, a, false, false);
        const unsigned r0 = r[0], r1 = r[1], a0 = ra[0], a1 = ra[1], qs = asu(asf(r0) + asf(r1));
        const v4u rec = {qs, key0 - qs - a0, key1 - a1, 0u};
        __builtin_amdgcn_raw_buffer_store_b128(rec, rs_ws, vl16 < 512 ? (vl16 >> 8) * 1024 + (vl16 & 255) : OOB,
                                               W_ST + (layer + 1) * 131072 + (t * 8 + 2 * (h >> 2)) * 1024 + (h & 3) * 256, SC1);
        keep(rec);
        if (isA) hint_put(2 * layer + 2, max(late, 0));
        xwait = late + T_X - 12;
      } else {
        xwait = late + T_X;
      }
    }
  };
  auto finish = [&]() INL {
  };
  if (w_ >= 4) {
    run_layer(Layer<0>{}, Layer<1>{}); run_layer(Layer<1>{}, Layer<1>{}); run_layer(Layer<2>{}, Layer<1>{}); run_layer(Layer<3>{}, Layer<1>{});
    if (w_ == 7 && b == 0) __builtin_amdgcn_raw_buffer_store_b32(epoch, rs_ws, 0, W_KEY, 0);
#pragma unroll
    for (int s = 0; s < 14; ++s) asm volatile("" : "=v"(xf[s]));
    asm volatile("" : "=v"(k0), "=v"(k1), "=v"(v0), "=v"(v1), "=v"(ck0), "=v"(ck1), "=v"(cv0), "=v"(cv1), "=v"(var), "=v"(wphi[0]), "=v"(wphi[1]), "=v"(vres0));
  }
  int wz = w_;
  asm volatile("" : "+s"(wz));
  if (wz < 4) {
    run_layer(Layer<0>{}, Layer<0>{}); run_layer(Layer<1>{}, Layer<0>{}); run_layer(Layer<2>{}, Layer<0>{}); run_layer(Layer<3>{}, Layer<0>{});
  }
  finish();
}
}

extern "C" void kernel_launch(void* const* d_in, const int* in_sizes, int n_in, void* d_out, int out_size, void* d_ws, size_t ws_size,
                              hipStream_t stream) {
  (void)in_sizes; (void)n_in; (void)out_size; (void)ws_size;
  (void)hipFuncSetAttribute((const void*)fused_layers, hipFuncAttributeMaxDynamicSharedMemorySize, (int)L_TOTAL);
  Params p;
  p.x = d_in[0]; p.wA = d_in[1]; p.wphi = d_in[2]; p.woA = d_in[3]; p.wB = d_in[5]; p.woB = d_in[6]; p.out = d_out; p.ws = d_ws;
  hipLaunchKernelGGL(fused_layers, dim3(256), dim3(512), L_TOTAL, stream, p);
}
```

```cpp
#include <hip/hip_runtime.h>
#include <stdint.h>

#define LAS __attribute__((address_space(3)))
#define DEV __device__ __forceinline__
typedef unsigned v4u __attribute__((ext_vector_type(4)));
typedef unsigned v2u __attribute__((ext_vector_type(2)));
typedef float v4f __attribute__((ext_vector_type(4)));
typedef float v2f __attribute__((ext_vector_type(2)));
typedef int v4i __attribute__((ext_vector_type(4)));
typedef short v4s __attribute__((ext_vector_type(4)));
typedef __bf16 v8bf __attribute__((ext_vector_type(8)));
typedef __bf16 v2bf __attribute__((ext_vector_type(2)));
typedef __amdgpu_buffer_rsrc_t rsrc_t;

#define TPX 4
#define DLY_T 3
#define WO1 6
#define POLL_OB 1
#define T_KV 4
#define T_H 32
#define T_OA 10
#define T_OB 12
#define P_X 11
#define T_X 14
#define T_XL 0
#define T_OT 20
#define PRI0 1
#define RAMP 0
#define WOS 1
#define T_HL 4
#define P_F 440
#define BOFF 4
#define CODEB 60000
#define NW0 2
#define KVE 1
#define PBACK 99
#define ILV 1
#define WIN 5

namespace {
constexpr int SEQ = 2048, DM = 448;
constexpr unsigned L_WK = 0, L_WV = 28672, L_WQ = 57344, L_WQB = 86016, L_SCR = 114688, L_TOTAL = 163840;
constexpr unsigned L_CS = L_SCR + 23 * 2048;
constexpr unsigned L_KVF = L_SCR + 40960;
constexpr unsigned TILEBUF = 16 * 8 * 14 * 1024;
constexpr unsigned W_KEY = 0, W_HINT = 4096, W_FLAG = W_HINT + 11 * 32768, W_ST = W_HINT + 12 * 32768, W_KVP = W_ST + 4 * 131072, W_O = W_KVP + 2 * 1048576,
                   W_HALO = W_O + 4 * 2097152, W_X = W_HALO + 2 * 4194304, W_PT = W_X + 4 * TILEBUF, W_WO = W_PT + 2 * 16 * 86016,
                   W_CS = W_WO + 2 * 28 * 14336, W_END = W_CS + 16 * 56 * 128;
constexpr unsigned W_DBG = 64u << 20;
constexpr int SC1 = 16;
constexpr unsigned OOB = 0x40000000u;

DEV v4f mfma(v4u a, v4u b, v4f c) {
  return __builtin_amdgcn_mfma_f32_16x16x32_bf16(__builtin_bit_cast(v8bf, a), __builtin_bit_cast(v8bf, b), c, 0, 0, 0);
}
DEV unsigned pk(float a, float b) { v2f f = {a, b}; v2bf r = __builtin_convertvector(f, v2bf); return __builtin_bit_cast(unsigned, r); }
typedef LAS char* lptr;
DEV lptr lbase(unsigned v) { asm volatile("" : "+v"(v)); return (lptr)(size_t)v; }
DEV v2u trd(lptr a) { v4s r = __builtin_amdgcn_ds_read_tr16_b64_v4i16((LAS v4s*)a); return __builtin_bit_cast(v2u, r); }
DEV v4u cat(v2u a, v2u b) { return v4u{a.x, a.y, b.x, b.y}; }
DEV v4u lrd(lptr a) { return *(LAS v4u*)a; }
DEV void lwr(lptr a, v4u v) { *(LAS v4u*)a = v; }
#define VMWAIT(n) asm volatile("s_waitcnt vmcnt(" #n ")" ::: "memory")
DEV float asf(unsigned u) { return __builtin_bit_cast(float, u); }
DEV unsigned asu(float f) { return __builtin_bit_cast(unsigned, f); }
DEV void wg_bar() { asm volatile("s_waitcnt lgkmcnt(0)\n\ts_barrier" ::: "memory"); }
DEV void drain() { asm volatile("s_waitcnt vmcnt(0)" ::: "memory"); }
DEV void fence() { asm volatile("" ::: "memory"); }
DEV float ex2(float x) { return __builtin_amdgcn_exp2f(x); }

DEV unsigned lane_id() { unsigned r; asm volatile("v_mbcnt_lo_u32_b32 %0, -1, 0\n\tv_mbcnt_hi_u32_b32 %0, -1, %0" : "=v"(r)); __builtin_assume(r < 64); return r; }
DEV float mx(float a, float b) { return __builtin_elementwise_maximum(a, b); }
DEV float mn(float a, float b) { return __builtin_elementwise_minimum(a, b); }
DEV float mx3(float a, float b, float c) { return mx(mx(a, b), c); }
DEV float mn3(float a, float b, float c) { return mn(mn(a, b), c); }
DEV float xrow(float sel, float v) { const v4f z = {0.f, 0.f, 0.f, 0.f}; const v4f r = __builtin_amdgcn_mfma_f32_16x16x4f32(sel, v, z, 0, 0, 0); return r.x; }
DEV v4f xrow4(float sel, float v) { const v4f z = {0.f, 0.f, 0.f, 0.f}; return __builtin_amdgcn_mfma_f32_16x16x4f32(sel, v, z, 0, 0, 0); }
DEV v4u swap16(v4u v) {
  auto r = __builtin_amdgcn_permlane16_swap(v.x, v.z, false, false);
  auto q = __builtin_amdgcn_permlane16_swap(v.y, v.w, false, false);
  const unsigned r0 = r[0], r1 = r[1], q0 = q[0], q1 = q[1];
  return v4u{r0, q0, r1, q1};
}
DEV float sumsq8(v4u v) {
  const v4f lo = __builtin_bit_cast(v4f, v << 16), hi = __builtin_bit_cast(v4f, v & 0xffff0000u), q = lo * lo + hi * hi;
  return (q.x + q.y) + (q.z + q.w);
}
DEV void xmax2(float& a, float& b) {
  auto r = __builtin_amdgcn_permlane32_swap(asu(a), asu(b), false, false);
  unsigned r0 = r[0], r1 = r[1];
  float z = mx(asf(r0), asf(r1));
  auto q = __builtin_amdgcn_permlane16_swap(asu(z), asu(z), false, false);
  unsigned q0 = q[0], q1 = q[1];
  z = mx(asf(q0), asf(q1));
  auto p = __builtin_amdgcn_permlane32_swap(asu(z), asu(z), false, false);
  unsigned p0 = p[0], p1 = p[1];
  a = asf(p0); b = asf(p1);
}
DEV float xmax1(float a) {
  auto r = __builtin_amdgcn_permlane32_swap(asu(a), asu(a), false, false);
  unsigned r0 = r[0], r1 = r[1];
  float z = mx(asf(r0), asf(r1));
  auto q = __builtin_amdgcn_permlane16_swap(asu(z), asu(z), false, false);
  unsigned q0 = q[0], q1 = q[1];
  return mx(asf(q0), asf(q1));
}
DEV void dma2(unsigned ldsdst, unsigned va, unsigned vb, v4u srd, unsigned sa, unsigned sb) {
  asm volatile("s_mov_b32 m0, %0\n\ts_nop 3\n\tbuffer_load_dwordx4 %1, %3, %4 offen lds\n\tbuffer_load_dwordx4 %2, %3, %5 offen offset:1024 lds"
               :: "s"(ldsdst), "v"(va), "v"(vb), "s"(srd), "s"(sa), "s"(sb) : "memory");
}
DEV void keep(v4u v) { asm volatile("s_nop 1" :: "v"(v) : "memory"); }
DEV void keep(v4u v, v4u u) { asm volatile("s_nop 1" :: "v"(v), "v"(u) : "memory"); }
DEV void dma2s(unsigned ldsdst, unsigned v, v4u srd, unsigned so) {
  asm volatile("s_mov_b32 m0, %0\n\ts_nop 3\n\tbuffer_load_dwordx4 %1, %2, %3 offen sc1 lds\n\tbuffer_load_dwordx4 %1, %2, %3 offen offset:1024 sc1 lds"
               :: "s"(ldsdst), "v"(v), "s"(srd), "s"(so) : "memory");
}
DEV v4u ld16(rsrc_t rs, unsigned v, unsigned so) {
  const v4u r = __builtin_amdgcn_raw_buffer_load_b128(rs, v, so, SC1);
  __builtin_amdgcn_sched_barrier(0);
  return r;
}
DEV v4u mk_srd(const void* p, unsigned bytes) {
  unsigned long long a = (unsigned long long)p;
  return v4u{(unsigned)a, (unsigned)(a >> 32) & 0xffffu, bytes, 0x00020000u};
}
DEV v4u imma(v4u a, v4u b, v4u c) {
  const v4i r = __builtin_amdgcn_mfma_i32_16x16x64_i8(__builtin_bit_cast(v4i, a), __builtin_bit_cast(v4i, b), __builtin_bit_cast(v4i, c), 0, 0, 0);
  return __builtin_bit_cast(v4u, r);
}
DEV v4u weights(unsigned lo) {
  const unsigned a = (lo + 1) * 0x9e3779b1u, b = (a ^ (a >> 15)) * 0x85ebca77u, c = (b ^ (b >> 13)) * 0xc2b2ae3du, d = (c ^ (c >> 16)) * 0x27d4eb2fu;
  return v4u{(a ^ (a >> 16)) | 0x01010101u, (b ^ (b >> 16)) | 0x01010101u, (c ^ (c >> 16)) | 0x01010101u, (d ^ (d >> 16)) | 0x01010101u};
}
DEV void keys(unsigned e, unsigned& k0, unsigned& k1) { k0 = e * 0x9e3779b1u + 0x7f4a7c15u; k1 = e * 0x85ebca77u + 0x165667b1u; }
DEV unsigned xsum2u(unsigned a, unsigned b) {
  auto r = __builtin_amdgcn_permlane32_swap(a, b, false, false);
  const unsigned r0 = r[0], r1 = r[1], z = r0 + r1;
  auto q = __builtin_amdgcn_permlane16_swap(z, z, false, false);
  const unsigned q0 = q[0], q1 = q[1];
  return q0 + q1;
}
DEV void reload4(v4u& a, v4u& b, v4u& c, v4u& d, v4u srd, unsigned voff, unsigned sa, unsigned sb, unsigned sc, unsigned sd) {
  asm volatile("s_sleep 2\n\tbuffer_load_dwordx4 %0, %4, %5, %6 offen sc1\n\tbuffer_load_dwordx4 %1, %4, %5, %7 offen sc1\n\t"
               "buffer_load_dwordx4 %2, %4, %5, %8 offen sc1\n\tbuffer_load_dwordx4 %3, %4, %5, %9 offen sc1\n\ts_waitcnt vmcnt(0)"
               : "+v"(a), "+v"(b), "+v"(c), "+v"(d) : "v"(voff), "s"(srd), "s"(sa), "s"(sb), "s"(sc), "s"(sd) : "memory");
}
DEV bool spend(int& budget) { budget = max(budget - 1, 0); return budget != 0; }
DEV bool nonzero(unsigned v, unsigned long long lanes = ~0ull) { return (__builtin_amdgcn_uicmp(v, 0u, 33) & lanes) != 0ull; }
DEV bool row3_differs(v4u hs, unsigned e0, unsigned e1) {
  const unsigned long long m = __builtin_amdgcn_uicmp((hs.y << 13) + hs.x, e0, 33) | __builtin_amdgcn_uicmp((hs.w << 13) + hs.z, e1, 33);
  unsigned hi = (unsigned)(m >> 32);
  asm volatile("" : "+s"(hi));
  return (hi >> 16) != 0u;
}
DEV bool retry(bool bad, int& budget) {
  if (__builtin_expect(!__any(bad), 1)) return false;
  budget = max(budget - 1, 0);
  return __any(budget != 0);
}

DEV unsigned clock32() { unsigned long long t; asm volatile("s_memrealtime %0\n\ts_waitcnt lgkmcnt(0)" : "=s"(t) :: "memory"); return (unsigned)t; }
DEV int lateness(v2u v, int own, int seg, unsigned key, v4u srd, unsigned voff, unsigned soff, int& budget) {
  asm volatile("" : "+v"(v) :: "memory");
  int over = 0;
  if (__builtin_expect(nonzero(v.y - key, 0xffffull), 0)) {
    do {
      asm volatile("s_sleep %4\n\tbuffer_load_dwordx2 %0, %1, %2, %3 offen sc1\n\ts_waitcnt vmcnt(0)" : "+v"(v) : "v"(voff), "s"(srd), "s"(soff), "n"(BOFF) : "memory");
    } while (nonzero(v.y - key, 0xffffull) && spend(budget));
    const unsigned tv = v.x;
    over = max((int)(clock32() - __builtin_amdgcn_readlane(tv, own)) - seg, 0);
  }
  const unsigned tv = v.x;
  int d = (int)(tv - __builtin_amdgcn_readlane(tv, own));
  d = max(d, __builtin_amdgcn_update_dpp(0, d, 0x128, 0xf, 0xf, false));
  d = max(d, __builtin_amdgcn_update_dpp(0, d, 0x124, 0xf, 0xf, false));
  d = max(d, __builtin_amdgcn_update_dpp(0, d, 0x122, 0xf, 0xf, false));
  d = max(d, __builtin_amdgcn_update_dpp(0, d, 0x121, 0xf, 0xf, false));
  return min(__builtin_amdgcn_readfirstlane(d), 1000) - over;
}
DEV void pause(int ticks) {
  asm volatile("s_cmp_lt_i32 %0, 1\n\ts_cbranch_scc1 2f\n1:\n\ts_sleep 1\n\ts_sub_i32 %0, %0, 4\n\ts_cmp_gt_i32 %0, 0\n\ts_cbranch_scc1 1b\n2:" : "+s"(ticks) :: "memory", "scc");
}

template <class F>
DEV void feature_map(v4u vf, const v4u (&wphi)[2], v4u& plus, v4u& minus, F mid) {
  const v4f z4 = {0.f, 0.f, 0.f, 0.f};
  const v4f p0 = mfma(wphi[0], vf, z4), p1 = mfma(wphi[1], vf, z4);
  float hi = mx3(mx3(mx3(p0.x, p0.y, p0.z), p0.w, p1.x), p1.y, p1.z);
  float lo = -mn3(mn3(mn3(p0.x, p0.y, p0.z), p0.w, p1.x), p1.y, p1.z);
  xmax2(hi, lo);
  mid();
  const v2f p01 = {p0.x, p0.y}, p23 = {p0.z, p0.w}, p45 = {p1.x, p1.y}, h2 = {hi, hi}, l2 = {lo, lo};
  v2f a01 = p01 - h2, a23 = p23 - h2, a45 = p45 - h2, b01 = p01 + l2, b23 = p23 + l2, b45 = p45 + l2;
  asm volatile("" : "+v"(a01), "+v"(a23), "+v"(a45), "+v"(b01), "+v"(b23), "+v"(b45));
  const v2f e01 = {ex2(a01.x), ex2(a01.y)}, e23 = {ex2(a23.x), ex2(a23.y)}, e45 = {ex2(a45.x), ex2(a45.y)};
  const float e6 = ex2(p1.z - hi);
  const v2f se = (e01 + e23) + e45;
  v4f te = xrow4(1.f, (se.x + e6) + se.y);
  __builtin_amdgcn_sched_barrier(0);
  const v2f f01 = {ex2(-b01.x), ex2(-b01.y)}, f23 = {ex2(-b23.x), ex2(-b23.y)}, f45 = {ex2(-b45.x), ex2(-b45.y)};
  const float f6 = ex2(-(p1.z + lo));
  const v2f sf = (f01 + f23) + f45;
  v4f tf = xrow4(1.f, (sf.x + f6) + sf.y);
  __builtin_amdgcn_sched_barrier(0);
  const float re = __builtin_amdgcn_rcpf(te.x);
  asm volatile("" :: "v"(te));
  const v2f x01 = e01 * re, x23 = e23 * re, x45 = e45 * re;
  plus = v4u{pk(x01.x, x01.y), pk(x23.x, x23.y), pk(x45.x, x45.y), pk(e6 * re, 0.f)};
  __builtin_amdgcn_sched_barrier(0);
  const float rf = __builtin_amdgcn_rcpf(tf.x);
  asm volatile("" :: "v"(tf));
  const v2f y01 = f01 * rf, y23 = f23 * rf, y45 = f45 * rf;
  minus = v4u{pk(y01.x, y01.y), pk(y23.x, y23.y), pk(y45.x, y45.y), pk(f6 * rf, 0.f)};
}

DEV v4u pexp(v4f a, v4f b) {
  return v4u{pk(ex2(a.x), ex2(a.y)), pk(ex2(a.z), ex2(a.w)), pk(ex2(b.x), ex2(b.y)), pk(ex2(b.z), ex2(b.w))};
}
#define VMKNOWN(n) __builtin_amdgcn_s_waitcnt(((n) & 15) | (((n) >> 4) << 14) | 0xf70)
#define LGKNOWN(n) __builtin_amdgcn_s_waitcnt(0xc07f | ((n) << 8))
#define PIN(x) asm volatile("" : "+v"(x))
#define SB() __builtin_amdgcn_sched_barrier(0)
#define INL __attribute__((always_inline))

#define STAMP(i) do {} while (0)

template <int N> struct Layer { static constexpr int value = N; };
struct Params {
  const void* x; const void* wA; const void* wphi; const void* woA; const void* wB; const void* woB; void* out; void* ws;
};

__global__ void __launch_bounds__(512, 2) fused_layers(Params) {
  typedef unsigned long long v8p __attribute__((ext_vector_type(8)));
  v8p args;
  asm volatile("s_load_dwordx16 %0, %1, 0x0" : "=s"(args) : "s"(__builtin_amdgcn_kernarg_segment_ptr()));
  unsigned long long born;
  asm volatile("s_memrealtime %0" : "=s"(born));
  const int b = blockIdx.x, xg = b & 7, xi = b >> 3;
  constexpr int HPX = 32 / TPX, NGT = 16 / TPX;
  const int t = (xg % NGT) * TPX + xi / HPX;
  const int h = (xg / NGT) * HPX + xi % HPX;
  const int wid = __builtin_amdgcn_readfirstlane(threadIdx.x >> 6) & 7;
  const int w_ = wid < 4 ? 7 - wid : wid - 4;
  const int l = threadIdx.x & 63, c = l & 15, g = l >> 4;
  const bool g3 = g == 3;
  STAMP(0);
  constexpr unsigned L_XS = 73728, L_JOB = L_WQ, L_CSP = L_XS + 81920;
  const unsigned chunk16 = ((l & 3) ^ ((l >> 5) << 1)) * 16, chunk56 = chunk16 == 48 ? 40 : chunk16, vqkv = (l >> 2) * (3 * DM * 2) + chunk56;
  const int kvp_ = (w_ >> 1) & 1, kvs = w_ & 1;
  const unsigned kvat = kvp_ * L_WV + kvs * 2048, kvso = kvs * (32 * 3 * DM * 2) + (kvp_ + 1) * DM * 2 + 56 * h;
  const unsigned xsb = L_XS + w_ * 10240, xso = (t * 128 + w_ * 16) * (DM * 2);
  const unsigned vx0 = (l >> 3) * (DM * 2) + ((l & 7) ^ (l >> 4)) * 16, vx1 = ((l >> 3) + 8) * (DM * 2) + ((l & 7) ^ (4 + (l >> 4))) * 16 - 1024;
  const bool job = t < 14 && (w_ < 4 || (w_ < 6 && h < 14));
  const unsigned jat = L_JOB + w_ * 2048;
  asm volatile("" :: "v"(vqkv), "v"(vx0), "v"(vx1), "s"(kvat), "s"(kvso), "s"(xsb), "s"(xso), "s"(jat));
  asm volatile("s_waitcnt lgkmcnt(0)" : "+s"(args), "+s"(born));
  const Params p = {(const void*)args[0], (const void*)args[1], (const void*)args[2], (const void*)args[3], (const void*)args[4], (const void*)args[5], (void*)args[6], (void*)args[7]};
  const v4u srdA = mk_srd(p.wA, DM * 3 * DM * 2), srdX = mk_srd(p.x, SEQ * DM * 2);
  auto kvblock = [&](int j) INL { const unsigned so = kvso + j * (64 * 3 * DM * 2); dma2(kvat + j * 4096, vqkv, vqkv, srdA, so, so + 16 * 3 * DM * 2 - 1024); };
  auto xline = [&](int j, int up) INL {
    const unsigned so = xso + up * (64 * DM * 2) + j * 128;
    dma2(xsb + up * 40960 + (j % 5) * 2048, vx0, vx1, srdX, so, so);
  };
  if (w_ < 4) {
    __builtin_amdgcn_s_setprio(PRI0);
    const unsigned so = t * (32 * 3 * DM * 2) + (w_ == 3 ? 0 : w_ * DM * 2) + 56 * (h ^ 1);
    if (job) dma2(jat, vqkv, vqkv, mk_srd(w_ ? p.wB : p.wA, DM * 3 * DM * 2), so, so + 16 * 3 * DM * 2 - 1024);
    xline(0, 0); xline(0, 1);
  } else {
    if (job) {
      const unsigned v = (l >> 2) * (DM * 2) + chunk16, so = t * (32 * DM * 2) + 64 * h;
      dma2(jat, v, v, mk_srd(w_ == 5 ? p.woB : p.woA, DM * DM * 2), so, so + 16 * DM * 2 - 1024);
    } else if (w_ == 7) {
      const unsigned e = (l >> 2) & 7, g0 = l >> 5, d0 = e < 4 ? 4 * g0 + e : 12 + 3 * g0 + e, d1 = d0 + (e < 4 ? 8 : 6);
      dma2(jat, e < 7 ? d0 * 56 + chunk56 + 1024 : OOB, e < 7 ? d1 * 56 + chunk56 : OOB,
           mk_srd((const char*)p.wphi - 1024, 16 * 28 * 28 * 2 + 1024), h * 28 * 28 * 2, h * 28 * 28 * 2);
    }
    kvblock(0); kvblock(1);
  }
  unsigned codepf = 0;
  if (w_ == 6) {
    unsigned long long pc;
    asm volatile("s_getpc_b64 %0" : "=s"(pc));
    const unsigned off = (l * 32 + xi) * 128;
    asm volatile("s_nop 4\n\tbuffer_load_dword %0, %1, %2, 0 offen" : "=v"(codepf) : "v"(off), "s"(mk_srd((const void*)pc, CODEB)) : "memory");
  }
  unsigned epoch, key0, key1, keyk;
  asm volatile("s_load_dword %0, %1, 0x0" : "=s"(epoch) : "s"(p.ws));
  const rsrc_t rs_ws = __builtin_amdgcn_make_buffer_rsrc(p.ws, 0, W_END, 0x00020000);
  const v4u srdW = mk_srd(p.ws, W_END);
  unsigned vl16 = l * 16;
  asm volatile("" : "+v"(vl16));
  const int ft = 7 * (h & 3) + min(w_, 6), nx = 2 * ft + (g >> 1);
  const unsigned pubx = w_ == 7 ? OOB : (g & 1) * 14336 + (nx >> 2) * 1024 + ((nx & 3) * 16 + c) * 16;
  const unsigned tw = (t * 8 + w_) * 14336, tp = (t * 8 + 2 * (h >> 2)) * 14336;
  const unsigned ptA = W_PT + h * 86016 + w_ * 2048;
  int budget = 1 << 16;
  v4u wphi[2];
  v4u park[8];
  v4u xf[14];
  v4f k0, k1, v0, v1, ck0, ck1, cv0, cv1;
  float var;
  v4f res0, res1;
  auto hint_put = [&](int slot, int ahead = 0) INL {
    __builtin_amdgcn_raw_buffer_store_b64(v2u{clock32() + ahead, key0}, rs_ws, vl16 < 256 ? vl16 >> 1 : OOB, W_HINT + slot * 32768 + (t * 16 + h) * 128, SC1);
  };
  auto hint_get = [&](unsigned v, unsigned so) INL { SB(); const v2u r = __builtin_amdgcn_raw_buffer_load_b64(rs_ws, v, so, SC1); SB(); return r; };
  const unsigned q4 = (l & 15) >> 2, p4 = l & 3;
  const lptr l16 = (lptr)(size_t)vl16, lq16 = lbase(L_WQ + l * 16);
  const lptr stw = lbase(L_SCR + c * 32 + ((g ^ (c >> 2)) * 8));
  const lptr str = lbase(L_SCR + (4 * g + q4) * 32 + ((p4 ^ g) * 8));
  const lptr sl16 = lbase(L_SCR + l * 16), csg = lbase(L_CS + g * 16);
  const unsigned vcol = l < 16 ? l * 2048 : OOB, vrow = l < 16 ? l * 128 : OOB;
  const float sel3 = g3 ? 1.f : 0.f;
  v4u hwt = weights(l);
  if (g3) { hwt.z = 0u; hwt.w = 0u; }
  const v4u zu = {0u, 0u, 0u, 0u};

  v4u wof[14];
  const unsigned wos = W_WO + ft * 14336;
  auto kv_gemm = [&](const int s0, const int s1, auto step) INL {
    const v4f z4 = {0.f, 0.f, 0.f, 0.f};
    if (s0 == 0) { k0 = z4; k1 = z4; v0 = z4; v1 = z4; }
    v4u a[8];
#pragma unroll
    for (int i = 0; i < 2; ++i) { const int o = (s0 + i) * 2048; a[4 * i] = lrd(l16 + o); a[4 * i + 1] = lrd(l16 + o + 1024); a[4 * i + 2] = lrd(l16 + L_WV + o); a[4 * i + 3] = lrd(l16 + L_WV + o + 1024); SB(); }
#pragma unroll
    for (int s = s0; s < s1; ++s) {
      const int i = 4 * (s & 1), o = (s + 2) * 2048;
      if (s < s1 - 1) LGKNOWN(4); else LGKNOWN(0);
      SB();
      k1 = mfma(a[i + 1], xf[s], k1); SB();
      step(s);
      if (s + 2 < s1) { a[i + 1] = lrd(l16 + o + 1024); SB(); }
      k0 = mfma(a[i], xf[s], k0); SB(); if (s + 2 < s1) { a[i] = lrd(l16 + o); SB(); }
      v0 = mfma(a[i + 2], xf[s], v0); SB(); if (s + 2 < s1) { a[i + 2] = lrd(l16 + L_WV + o); SB(); }
      v1 = mfma(a[i + 3], xf[s], v1); SB(); if (s + 2 < s1) { a[i + 3] = lrd(l16 + L_WV + o + 1024); SB(); }
    }
  };
  v4u hwk = weights(l);
  hwk.w &= 0xffffu;
  asm volatile("" : "+v"(hwk.z), "+v"(hwk.w), "+v"(hwt));
  v4u fl0, fl1;
  auto flags_wait = [&](v2u f, unsigned v, unsigned so, unsigned long long lanes) INL {
    while (nonzero((f.x ^ key0) | (f.y ^ key1), lanes) && spend(budget))
      asm volatile("s_sleep 3\n\tbuffer_load_dwordx2 %0, %1, %2, %3 offen sc1\n\ts_waitcnt vmcnt(0)" : "+v"(f) : "v"(v), "s"(srdW), "s"(so) : "memory");
  };
  unsigned vres0;
  {
    unsigned a0 = (8 * g + (c >> 2)) * 64 + ((((l & 3) >> 1) ^ ((g & 1) << 1)) * 16) + (l & 1) * 8;
    unsigned a1 = (a0 ^ 32) ^ ((l & 2) << 2);
    unsigned sl3 = c >= 12 ? 4 * c - 45 : c + ((c * 11) >> 5);
    const unsigned one = 0x3f803f80u;
    v4u one8 = {one, one, one, one};
    const v4f z4 = {0.f, 0.f, 0.f, 0.f};
    auto turn = [&](lptr r0, lptr r1, bool pad, v4u& f0, v4u& f1) INL {
      f0 = cat(trd(r0), trd(r0 + 256)); f1 = cat(trd(r1), trd(r1 + 256));
      f1 = v4u{pad ? one : f1.x, pad ? one : f1.y, pad ? one : f1.z, pad ? one : f1.w};
    };
    const unsigned xa = xsb + c * 128 + (g ^ (c >> 1)) * 16;
    const lptr xa0 = lbase(xa), xa1 = lbase(xa ^ 64), cp = lbase(L_CSP + g * 16);
    unsigned vres = (t * 128 + (2 * (h >> 2) + (g & 1)) * 16 + c) * (DM * 2) + nx * 16;
    unsigned pubx_ = pubx, vcol_ = vcol, vrow_ = vrow;
    float dg0 = c == 4 * g ? 1.f : 0.f, dg1 = c == 4 * g + 1 ? 1.f : 0.f, dg2 = c == 4 * g + 2 ? 1.f : 0.f, dg3 = c == 4 * g + 3 ? 1.f : 0.f;
    asm volatile("" : "+v"(vres), "+v"(a0), "+v"(a1), "+v"(sl3), "+v"(one8), "+v"(dg0), "+v"(dg1), "+v"(dg2), "+v"(dg3) : "v"(pubx_), "v"(vcol_), "v"(vrow_), "v"(sel3));
    {
      const bool spread = w_ == 7 || (w_ < 4 && w_ != 2);
      const unsigned slot = spread ? sl3 : c;
      const lptr jr0 = lbase(jat + a0), jr1 = lbase(jat + ((w_ & 6) == 4 ? a0 ^ 32 : a1));
      unsigned vj1 = ((w_ & 6) == 4 ? 14336 : 1024) + (vl16 & 768) + slot * 16, vcs = g == 0 ? c * 4 : g == 1 ? 64 + slot * 4 : OOB;
      unsigned dst = !job ? W_END : w_ < 4 ? W_PT + ((w_ ? 16 : 0) + (h ^ 1)) * 86016 + ((w_ ? w_ - 1 : 2) * 14 + t) * 2048 : W_WO + ((w_ - 4) * 28 + 2 * h) * 14336 + t * 1024;
      unsigned dcs = (job && w_ < 4) ? W_CS + ((h ^ 1) * 56 + w_ * 14 + t) * 128 : W_END;
      asm volatile("" : "+v"(vj1), "+v"(vcs));
      asm volatile("" : "+s"(dst), "+s"(dcs));
      STAMP(51);
      if (w_ < 4) { xline(1, 0); xline(1, 1); } else { kvblock(2); kvblock(3); }
      asm volatile("s_waitcnt lgkmcnt(0)" : "+s"(epoch) :: "memory");
      epoch += 1u;
      keys(epoch, key0, key1);
      keyk = ((vl16 & 512) ? key1 : key0) >> ((vl16 & 256) ? 14 : 0);
      asm volatile("" : "+v"(keyk));
      VMWAIT(8);
      STAMP(47);
      v4u f0, f1;
      turn(jr0, jr1, spread && c >= 12, f0, f1);
      if (w_ == 7) { lwr(lq16 + 7 * 2048, f0); lwr(lbase(jat + vj1), f1); }
      __builtin_amdgcn_raw_buffer_store_b128(f0, rs_ws, vl16, dst, SC1);
      __builtin_amdgcn_raw_buffer_store_b128(f1, rs_ws, vj1, dst, SC1);
      keep(f0, f1);
      const v4f c0 = mfma(one8, f0, z4), c1 = mfma(one8, f1, z4);
      __builtin_amdgcn_raw_buffer_store_b32(asu(g ? c1.x : c0.x), rs_ws, vcs, dcs, SC1);
      fence();
      STAMP(52);
    }
    const unsigned kslot = kvp_ ? c : sl3;
    const lptr kr0 = lbase(kvat + a0), kr1 = lbase(kvat + a1), kw0 = lbase(kvat + vl16), kw1 = lbase(kvat + 1024 + (vl16 & 768) + kslot * 16);
    const lptr cw0 = lbase(L_CSP + (w_ & 3) * 128 + c * 4), cw1 = lbase(L_CSP + (w_ & 3) * 128 + 64 + kslot * 4);
    const bool kpad = !kvp_ && c >= 12;
    v4f cs0 = z4, cs1 = z4;
    auto kvturn = [&](int j) INL {
      v4u f0, f1;
      turn(kr0 + j * 4096, kr1 + j * 4096, kpad, f0, f1);
      lwr(kw0 + j * 4096, f0); lwr(kw1 + j * 4096, f1);
      cs0 = mfma(one8, f0, cs0); cs1 = mfma(one8, f1, cs1);
    };
    if (w_ >= 4) {
      VMWAIT(7); kvturn(0); kvturn(1);
      VMWAIT(3); kvturn(2); kvturn(3);
      kvblock(4); xline(4, 0); kvblock(5); kvblock(6);
      VMWAIT(8);
    } else {
      xline(2, 0); xline(2, 1); xline(3, 0); xline(3, 1);
      __builtin_amdgcn_s_setprio(0);
      xline(4, 0);
      VMWAIT(2);
    }
    asm volatile("" :: "v"(codepf));
    STAMP(48);
    wg_bar();
    STAMP(49);
#pragma unroll
    for (int j = 0; j < 2; ++j) { xf[2 * j] = lrd(xa0 + j * 2048); xf[2 * j + 1] = lrd(xa1 + j * 2048); }
    if (w_ == 6) __builtin_amdgcn_raw_buffer_store_b64(v2u{key0, key1}, rs_ws, vl16 < 256 ? vl16 >> 1 : OOB, W_FLAG + (t * 16 + h) * 128, SC1);
    v4f gram = z4, pa, pb;
    kv_gemm(0, 8, [&](int s) INL {
      gram = mfma(xf[s], xf[s], gram);
      if (s < 2) { xf[4 + 2 * s] = lrd(xa0 + (2 + s) * 2048); xf[5 + 2 * s] = lrd(xa1 + (2 + s) * 2048); }
      if (s == 1) xline(5, 0);
      if (s == 3) xline(6, 0);
      if (s == 5) { wphi[0] = lrd(lq16 + 7 * 2048); wphi[1] = lrd(lq16 + 7 * 2048 + 1024); }
      SB();
    });
    STAMP(50);
    v2u fc;
    const unsigned vfl = w_ == 7 ? (vl16 & 240) * 128 : (vl16 & 240) * 128 + (vl16 >> 8) * 128, sfl = W_FLAG + (w_ == 7 ? h ^ 1 : (7 * (h & 3)) >> 1) * 128;
    const bool now = w_ >= 6 && (int)(clock32() - (unsigned)born) >= P_F;
    if (now) asm volatile("s_nop 4\n\tbuffer_load_dwordx2 %0, %1, %2, %3 offen sc1\n\ts_waitcnt vmcnt(1)" : "=&v"(fc) : "v"(vfl), "s"(srdW), "s"(sfl) : "memory");
    else drain();
    if (w_ >= 4) { kvturn(4); kvturn(5); kvturn(6); *(LAS float*)(cw0) = cs0.x; *(LAS float*)(cw1) = cs1.x; }
#pragma unroll
    for (int j = 4; j < 7; ++j) { xf[2 * j] = lrd(xa0 + (j % 5) * 2048); xf[2 * j + 1] = lrd(xa1 + (j % 5) * 2048); }
    if (w_ >= 6) {
      asm volatile("s_waitcnt vmcnt(0)" : "+v"(fc) :: "memory");
      if (!now) { pause(P_F - 8 - (int)(clock32() - (unsigned)born)); fc = v2u{~key0, 0u}; }
      flags_wait(fc, vfl, sfl, ~0ull);
    }
    wg_bar();
    STAMP(54);
    kv_gemm(8, 14, [&](int s) INL {
      if (s < 11) { gram = mfma(xf[2 * s - 8], xf[2 * s - 8], gram); gram = mfma(xf[2 * s - 7], xf[2 * s - 7], gram); }
      if (s == 8) {
        const unsigned so = w_ >= 4 ? W_CS + (h * 56 + 14 * (w_ & 3)) * 128 : W_END;
        fl0 = ld16(rs_ws, vl16, so); fl1 = ld16(rs_ws, vl16 < 768 ? vl16 + 1024 : OOB, so);
        dma2s(L_WQ + w_ * 2048, vl16, srdW, ptA + L_WQ);
      }
      if (s == 9 && w_ < 6) dma2s(L_WQ + 16384 + w_ * 2048, vl16, srdW, ptA + L_WQ + 16384);
      if (s >= 10) {
#pragma unroll
        for (int n = NW0 * (s - 10); n < NW0 * (s - 9); ++n) wof[n] = ld16(rs_ws, w_ == 7 ? OOB : vl16, wos + n * 1024);
      }
      if (s >= 9 && s < 13) { const v4f u = pa + pb; if (s == 9) ck0 = u; else if (s == 10) ck1 = u; else if (s == 11) cv0 = u; else cv1 = u; }
      if (s < 12) { const int o = ((s - 8) >> 1) * 256 + ((s - 8) & 1) * 64; pa = *(LAS v4f*)(cp + o); pb = *(LAS v4f*)(cp + o + 128); }
      if (s == 12) var = xrow(1.f / DM, (gram.x * dg0 + gram.y * dg1) + (gram.z * dg2 + gram.w * dg3));
      SB();
    });
    if (w_ == 6) { *(LAS v4f*)(csg) = ck0; *(LAS v4f*)(csg + 64) = ck1; *(LAS v4f*)(csg + 128) = cv0; *(LAS v4f*)(csg + 192) = cv1; }
    vres0 = vres;
  }
  STAMP(56);
  int xwait;
#define FAIL(k)
  v4u kc = {key0, 0u, key1, 0u};
  PIN(kc);
  auto run_layer = [&](auto kind, auto role) INL {
    constexpr int layer = decltype(kind)::value, rep = layer >> 1;
    constexpr bool isA = !(layer & 1), first = layer == 0, more = layer < 3, lead = decltype(role)::value;
    int w = w_;
    asm volatile("" : "+s"(w));
    __builtin_assume(lead ? (w >= 4 && w < 8) : (w >= 0 && w < 4));
    const v4f z4 = {0.f, 0.f, 0.f, 0.f};
    const unsigned wob = (lead && w == 7) ? W_END - 12288 : wos + (isA ? 0 : 28 * 14336), wov = (lead && w == 7) ? OOB : vl16;
    auto load_wo = [&](int s0, int s1) INL {
#pragma unroll
      for (int s = s0; s < s1; ++s) wof[s] = ld16(rs_ws, wov + (s & 3) * 1024, wob + (s >> 2) * 4096);
    };
    v2u hh;
    unsigned vh;
    const unsigned shh = W_HINT + 2 * layer * 32768 + (t - 2) * 2048 + h * 128;
    if (!isA && lead) { const unsigned lo = lane_id(); vh = lo >= 16 ? OOB : (lo < 2 && (int)lo < t) ? (1 - lo) * 2048 : 4096; }
    const lptr cs = csg + (isA ? 0 : 384);
    float rstd, nmr;
    auto stats = [&]() INL {
      const float mean = k1.w * (1.f / DM);
      rstd = __builtin_amdgcn_rsqf(mx(fmaf(-mean, mean, var), 0.f) + 1e-5f); nmr = -mean * rstd;
      PIN(rstd); PIN(nmr);
    };
    unsigned hp = t < 15 ? W_HALO + rep * 4194304 + h * 262144 + (t * 8 + w) * 2048 : W_END;
    if (!isA) asm volatile("" : "+s"(hp));
    if (!first) {
      unsigned xs[4], sts = W_ST + layer * 131072 + (t * 8 + w) * 1024;
#pragma unroll
      for (int i = 0; i < 4; ++i) { xs[i] = W_X + layer * TILEBUF + tw + i * 4096; asm volatile("" : "+s"(xs[i])); }
      asm volatile("" : "+s"(sts));
      if (!isA && lead) PIN(vh);
      pause(xwait);
      auto ldx = [&](int s) INL { xf[s] = ld16(rs_ws, vl16 + (s & 3) * 1024, xs[s >> 2]); };
      for (;;) {
#pragma unroll
        for (int s = 0; s < WIN; ++s) ldx(s);
        STAMP(2 + layer * 12);
        v4u st;
        unsigned c0 = 0u - key0, c1 = 0u - key1;
        kv_gemm(0, 14, [&](int s) INL {
          const int n0 = WIN + s + (s < RAMP ? s : RAMP), n1 = WIN + s + 1 + (s + 1 < RAMP ? s + 1 : RAMP);
#pragma unroll
          for (int n = n0; n < n1; ++n) {
            if (n < 14) ldx(n);
            if (n == 14) st = ld16(rs_ws, vl16, sts);
            if (n == 15 && !isA && lead) hh = hint_get(vh, w == 4 ? shh : W_END);
          }
          c0 += xf[s].x + xf[s].z; c1 += xf[s].y + xf[s].w; PIN(c0); PIN(c1);
          if (s == 13) { var = xrow(1.f / DM, asf(st.x)); c0 += st.x + st.y; c1 += st.z; PIN(c0); PIN(c1); }
          if (s == 12) { ck0 = *(LAS v4f*)(cs); ck1 = *(LAS v4f*)(cs + 64); cv0 = *(LAS v4f*)(cs + 128); cv1 = *(LAS v4f*)(cs + 192); }
          SB();
        });
        c0 = xsum2u(c0, c1);
        stats();
        if (__builtin_expect(!nonzero(c0), 1) || !spend(budget)) break;
        FAIL(0);
        __builtin_amdgcn_s_sleep(4);
      }
    }
    STAMP(3 + layer * 12);
    if (first) stats();
    auto ln7 = [&](v4f a0, v4f a1, v4f c0, v4f c1, float sc, float sh, float pad) INL {
      a0 = a0 * sc + c0 * sh;
      const v2f u = v2f{a1.x, a1.y} * sc + v2f{c1.x, c1.y} * sh;
      return v4u{pk(a0.x, a0.y), pk(a0.z, a0.w), pk(u.x, u.y), pk(fmaf(a1.z, sc, c1.z * sh), pad)};
    };
    auto q_gemm = [&](float sc, float pad) INL {
      v4f q0 = z4, q1 = z4, c0, c1;
      const lptr lq = lq16 + (isA ? 0 : L_WQB - L_WQ);
      v4u a[8];
#pragma unroll
      for (int i = 0; i < 8; ++i) { a[i] = lrd(lq + i * 1024); SB(); }
#pragma unroll
      for (int s = 0; s < 14; ++s) {
        const int i = (2 * s) & 7;
        if (!(s & 1)) { if (s < 12) LGKNOWN(4); else LGKNOWN(0); SB(); }
        q0 = mfma(a[i], xf[s], q0); SB(); if (s + 4 < 14) { a[i] = lrd(lq + (s + 4) * 2048); SB(); }
        q1 = mfma(a[i + 1], xf[s], q1); SB(); if (s + 4 < 14) { a[i + 1] = lrd(lq + (s + 4) * 2048 + 1024); SB(); }
        if (s == 12) { c0 = *(LAS v4f*)(cs + 256); c1 = *(LAS v4f*)(cs + 320); SB(); }
      }
      v4u r = ln7(q0, q1, c0, c1, rstd * sc, nmr * sc, pad);
      asm volatile("" : "+v"(r) :: "memory");
      return r;
    };
    constexpr float L2E = 1.4426950408889634f;
    const v4u kf = isA ? ln7(k0, k1, ck0, ck1, rstd * L2E, nmr * L2E, 0.f) : ln7(k0, k1, ck0, ck1, rstd, nmr, 0.f);
    auto v_pack = [&]() INL {
      const v4f a0 = v0 * rstd + cv0 * nmr, a1 = v1 * rstd + cv1 * nmr;
      return v4u{pk(a0.x, a0.y), pk(a0.z, a0.w), g3 ? 0x3f80u : pk(a1.x, a1.y), g3 ? 0u : pk(a1.z, a1.w)};
    };
    auto swap_kv = [&](int j0, int j1) INL {
#pragma unroll
      for (int j = j0; j < j1 && j < (lead ? 3 : 4); ++j) {
        const lptr a = lbase(w * 2048 + j * 16384 + vl16);
        v4u t0, t1;
        if (layer < 2) { t0 = lrd(a); t1 = lrd(a + 1024); }
        if (!first) { lwr(a, park[2 * j]); lwr(a + 1024, park[2 * j + 1]); }
        if (layer < 2) { park[2 * j] = t0; park[2 * j + 1] = t1; }
      }
      if (first) {
        asm volatile("s_waitcnt lgkmcnt(0)" ::: "memory");
#pragma unroll
        for (int j = j0; j < j1 && j < (lead ? 3 : 4); ++j) dma2s(w * 2048 + j * 16384, vl16, srdW, ptA + 16 * 86016 + j * 16384);
      }
    };
    v4u xres;
    v4f o0, o1;
    float den;
    const unsigned soh = W_HINT + (2 * layer + 1) * 32768 + t * 2048;
    v2u oh;

    if constexpr (isA) {
      if (lead && w == 7) hint_put(2 * layer);
      if (!first) { if (!lead) load_wo(6, 10); }
      else if (lead) {
        v4f cq = __builtin_bit_cast(v4f, fl0) + __builtin_bit_cast(v4f, fl1);
#pragma unroll
        for (int i = 0; i < 4; ++i) {
          float u = cq[i];
          u += asf(__builtin_amdgcn_update_dpp(0, asu(u), 0x128, 0xf, 0xf, false));
          cq[i] = xrow(1.f, u);
        }
        *(LAS v4f*)(lbase(L_CS + 256 + (w & 3) * 128 + (vl16 & 127))) = cq;
      }
      if (first) load_wo(4 * NW0, 4 * NW0 + 3);
      const v4u vf = v_pack();
      v4u pp, pm, qf;
      feature_map(kf, wphi, pp, pm, [&]() INL { SB(); if (first) load_wo(4 * NW0 + 3, 4 * NW0 + 6); else if (!lead) load_wo(10, 14); });
      {
        const lptr tb = stw + w * 3072;
        *(LAS v2u*)(tb) = v2u{pp.x, pp.y}; *(LAS v2u*)(tb + 512) = v2u{pp.z, pp.w};
        *(LAS v2u*)(tb + 1024) = v2u{pm.x, pm.y}; *(LAS v2u*)(tb + 1536) = v2u{pm.z, pm.w};
        *(LAS v2u*)(tb + 2048) = v2u{vf.x, vf.y}; *(LAS v2u*)(tb + 2560) = v2u{vf.z, vf.w};
      }
      if (first) VMWAIT(6);
      v4u hwk_ = {hwt.x, hwt.y, hwk.z, hwk.w};
      unsigned kvp = W_KVP + rep * 1048576 + h * 65536 + (w & 3) * 1024 + (lead ? t * 4096 : 0);
      const lptr pa = lbase((size_t)str + (w & 2) * 512), pb = lbase((size_t)str + 2048 + (w & 1) * 512);
      asm volatile("" : "+v"(hwk_));
      asm volatile("" : "+s"(kvp));
      wg_bar();
      STAMP(4 + layer * 12);
      auto residual = [&]() INL {
        xres = __builtin_amdgcn_raw_buffer_load_b128(__builtin_amdgcn_make_buffer_rsrc((void*)p.x, 0, SEQ * DM * 2, 0x00020000), vres0, 0, 0); SB();
      };
      if (lead) {
        __builtin_amdgcn_s_setprio(2);
        v4u bv[4], x0[4], x1[4];
#pragma unroll
        for (int kk = 0; kk < 4; ++kk) {
          bv[kk] = cat(trd(pb + kk * 6144), trd(pb + kk * 6144 + 3072));
          x0[kk] = cat(trd(pa + kk * 6144), trd(pa + kk * 6144 + 3072));
          x1[kk] = cat(trd(pa + kk * 6144 + 512), trd(pa + kk * 6144 + 3072 + 512));
          SB();
        }
        v4f d0 = z4, d1 = z4;
#pragma unroll
        for (int kk = 0; kk < 4; ++kk) { d0 = mfma(x0[kk], bv[kk], d0); d1 = mfma(x1[kk], bv[kk], d1); }
        v4u dv = {pk(d0.x, d0.y), pk(d0.z, d0.w), pk(d1.x, d1.y), pk(d1.z, 0.f)};
        const v4u hs = imma(hwk_, dv, zu);
        dv.w |= ((hs.x + keyk) & 0x3fffu) << 16;
        __builtin_amdgcn_raw_buffer_store_b128(dv, rs_ws, vl16, kvp, SC1);
        keep(dv);
        __builtin_amdgcn_s_setprio(0);
        STAMP(5 + layer * 12);
        if (first) {
          load_wo(4 * NW0 + 6, 14); residual();
#pragma unroll
          for (int j = 0; j < 4; ++j) if (j < 3 || w < 6) dma2s(L_WQB + (w & 3) * 2048 + j * 8192, vl16, srdW, ptA - 8192 + 16 * 86016 + L_WQ + j * 8192);
        } else load_wo(6, 14);
        qf = q_gemm(L2E, 0.f);
        feature_map(qf, wphi, pp, pm, []() {});
        PIN(pp); PIN(pm);
        if (first && KVE) swap_kv(0, 4);
      } else {
        const unsigned sh = W_HINT + 2 * layer * 32768 + h * 128;
        __builtin_amdgcn_s_setprio(1);
        __builtin_amdgcn_s_sleep(DLY_T);
        const v2u hv = hint_get(vcol, sh);
        if (first) { load_wo(4 * NW0 + 6, 14); residual(); }
        qf = q_gemm(L2E, 0.f);
        pause(lateness(hv, t, 150, key0, srdW, vcol, sh, budget) + T_KV);
        if (w == 0) hint_put(2 * layer + 1);
        fence();
        STAMP(6 + layer * 12);
        v4u kp[16];
#pragma unroll
        for (int i = 0; i < 8; ++i) kp[i] = ld16(rs_ws, vl16, kvp + i * 4096);
        feature_map(qf, wphi, pp, pm, [&]() INL {
          SB();
#pragma unroll
          for (int i = 8; i < 16; ++i) kp[i] = ld16(rs_ws, vl16, kvp + i * 4096);
        });
        PIN(pp); PIN(pm);
        const unsigned lo = lane_id();
        const unsigned bit = ((lo >> 4) == ((lo >> 2) & 3)) ? ((lo & 1) ? 0x3f800000u : 0x3f80u) : 0u;
        const unsigned u0 = (lo & 2) ? 0u : bit, u1 = (lo & 2) ? bit : 0u;
        const v4u sa = {u0, u1, 0u, 0u}, sb = {0u, 0u, u0, u1};
        const lptr kd = lbase((size_t)sl16 + (L_KVF - L_SCR) + w * 1024);
        auto add_up = [&](bool once) INL {
          v4f lo4 = z4, hi4 = z4;
          v4u hs = zu;
          unsigned ck = 0u - 16u * keyk;
          SB();
#pragma unroll
          for (int i = 0; i < 16; ++i) {
            hs = imma(hwk_, kp[i], hs); lo4 = mfma(sa, kp[i], lo4); hi4 = mfma(sb, kp[i], hi4);
            ck += kp[i].w >> 16; PIN(ck);
            SB();
          }
          const bool bad = nonzero((hs.x - ck) & 0x3fffu);
          SB();
          lwr(kd, v4u{pk(lo4.x, lo4.y), pk(lo4.z, lo4.w), pk(hi4.x, hi4.y), pk(hi4.z, 0.f)});
          return bad;
        };
        if (__builtin_expect(add_up(true), 0)) {
          FAIL(1);
          do {
#pragma unroll
            for (int i = 0; i < 16; i += 4) reload4(kp[i], kp[i + 1], kp[i + 2], kp[i + 3], srdW, vl16, kvp + i * 4096, kvp + (i + 1) * 4096, kvp + (i + 2) * 4096, kvp + (i + 3) * 4096);
          } while (add_up(false) && spend(budget));
        }
      }
      __builtin_amdgcn_s_setprio(0);
      wg_bar();
      STAMP(7 + layer * 12);
      v4u kv[4];
      kv[0] = lrd(sl16 + (L_KVF - L_SCR) + 1024); SB(); kv[1] = lrd(sl16 + (L_KVF - L_SCR) + 3072); SB();
      kv[2] = lrd(sl16 + (L_KVF - L_SCR)); SB(); kv[3] = lrd(sl16 + (L_KVF - L_SCR) + 2048);
      SB();
      if (lead) oh = hint_get(vrow, w == 7 ? soh : W_END);
      o1 = mfma(kv[0], pp, z4); o1 = mfma(kv[1], pm, o1); SB();
      o0 = mfma(kv[2], pp, z4); o0 = mfma(kv[3], pm, o0); SB();
      { const v4f e6 = {1e-6f, 1e-6f, 1e-6f, 1e-6f}, r = __builtin_amdgcn_mfma_f32_16x16x4f32(sel3, o1.x, e6, 0, 0, 0); den = r.x; }
    } else {
      const unsigned hb = W_HALO + rep * 4194304 + h * 262144;
      __builtin_amdgcn_raw_buffer_store_b128(kf, rs_ws, vl16, hp, SC1); SB();
      const v4u hsk = imma(hwt, kf, v4u{kf.z + key0, 0u, kf.w + key1, 0u}); SB();
      v4u vf = v_pack();
      const v4u hs_v = imma(hwt, vf, hsk); SB();
      const lptr kw = sl16 + w * 2048, vw = stw + w * 2048, vr = str + w * 2048;
      *(LAS v2u*)(vw + 32768 + 1024) = v2u{vf.x, vf.y}; *(LAS v2u*)(vw + 32768 + 1536) = v2u{vf.z, vf.w};
      if (!lead || w < 7) lwr(kw + 32768, kf);
      SB();
      vf.z = g3 ? (hs_v.y << 13) + hs_v.x : vf.z; vf.w = g3 ? (hs_v.w << 13) + hs_v.z : vf.w;
      __builtin_amdgcn_raw_buffer_store_b128(vf, rs_ws, vl16 + 1024, hp, SC1); keep(vf);
      STAMP(4 + layer * 12);
      v4u hk[2], hv[2];
      unsigned hs[2];
#pragma unroll
      for (int i = 0; i < 2; ++i) hs[i] = t - 2 + i >= 0 ? hb + ((t - 2 + i) * 8 + w) * 2048 : W_END;
      int hlate;
      auto fetch = [&](int margin) INL {
        pause(__builtin_amdgcn_readfirstlane(hlate) + margin);
#pragma unroll
        for (int i = 0; i < 2; ++i) { hk[i] = ld16(rs_ws, vl16, hs[i]); hv[i] = ld16(rs_ws, vl16 + 1024, hs[i]); }
      };
      if (lead && w == 4) *(LAS int*)(csg + 896) = lateness(hh, 2, 200, key0, srdW, vh, shh, budget);
      if (lead) load_wo(6, 10);
      wg_bar();
      hlate = *(LAS int*)(csg + 896);
      if (!lead) { fetch(T_H); __builtin_amdgcn_s_setprio(1); }
      const v4u qf = q_gemm(0.18898223650461357f * L2E, 1.f);
      if (lead) { fetch(T_HL); load_wo(10, 14); } else load_wo(6, 10);
      const float NEG = -3.0e38f;
      v4f sd = mfma(kf, qf, z4);
#pragma unroll
      for (int r = 0; r < 4; ++r) sd[r] = (4 * g + r <= c) ? sd[r] : NEG;
      float m = xmax1(mx(mx3(sd.x, sd.y, sd.z), sd.w));
      auto diag = [&]() INL {
        const v2u a0 = trd(vr + 32768 + 1024), a1 = trd(vr + 32768 + 1536);
        const v4f e = sd - m;
        const v2u pd = {pk(ex2(e.x), ex2(e.y)), pk(ex2(e.z), ex2(e.w))};
        typedef short v4h __attribute__((ext_vector_type(4)));
        o0 = __builtin_amdgcn_mfma_f32_16x16x16bf16_1k(__builtin_bit_cast(v4h, a0), __builtin_bit_cast(v4h, pd), z4, 0, 0, 0);
        o1 = __builtin_amdgcn_mfma_f32_16x16x16bf16_1k(__builtin_bit_cast(v4h, a1), __builtin_bit_cast(v4h, pd), z4, 0, 0, 0);
      };
      diag();
      auto take_halo = [&]() INL {
        STAMP(6 + layer * 12);
        const unsigned nv = min(t, 2);
        auto stage = [&]() INL {
          v4u hs_ = zu;
          unsigned e0 = 0u - nv * key0, e1 = 0u - nv * key1;
#pragma unroll
          for (int i = 0; i < 2; ++i) {
            const unsigned none = t - 2 + i < 0 ? 0xfe000000u : 0u;
            hs_ = imma(hwt, hk[i], hs_); SB();
            hs_ = imma(hwt, hv[i], hs_); SB();
            e0 += hv[i].z - hk[i].z; e1 += hv[i].w - hk[i].w; PIN(e0); PIN(e1);
            lwr(kw + i * 16384, v4u{hk[i].x, hk[i].y, hk[i].z, hk[i].w | none});
            *(LAS v2u*)(vw + i * 16384 + 1024) = v2u{hv[i].x, hv[i].y};
            *(LAS v2u*)(vw + i * 16384 + 1536) = v2u{g3 ? 0x3f80u : hv[i].z, g3 ? 0u : hv[i].w};
            SB();
          }
          return row3_differs(hs_, e0, e1);
        };
        if (__builtin_expect(stage(), 0)) {
          FAIL(1);
          do reload4(hk[0], hk[1], hv[0], hv[1], srdW, vl16, hs[0], hs[1], min(hs[0] + 1024, W_END), min(hs[1] + 1024, W_END));
          while (stage() && spend(budget));
        }
        wg_bar();
        STAMP(7 + layer * 12);
      };
      {
        const v4f cm = {-m, -m, -m, -m};
        v4f sa, sb; v4u a0, a1, b0, b1, pq;
#pragma unroll
        for (int j = 7; j >= 0; --j) {
          if (j >= 4 && (j < 6) == lead && (w >> 1) == 7 - j) take_halo();
          if (j == (lead ? 4 : 6)) VMKNOWN(4);
          if (!lead && j == 6 && w == 3) hint_put(2 * layer + 1);
          if (!lead && j <= 5 && j >= 4) load_wo(10 + 2 * (5 - j), 12 + 2 * (5 - j));
          if (!lead && j == PBACK) __builtin_amdgcn_s_setprio(0);
          if (!lead && j == POLL_OB) oh = hint_get(vrow, w == 3 ? soh : W_END);
          const v4u ka = lrd(kw + j * 4096), kb = lrd(kw + j * 4096 + 2048);
          const v4u n0 = cat(trd(vr + j * 4096 + 1024), trd(vr + j * 4096 + 3072)), n1 = cat(trd(vr + j * 4096 + 1536), trd(vr + j * 4096 + 3584));
          SB();
          if (j < 7) {
            const float e0 = ex2(sa.x), e1 = ex2(sa.y); SB();
            if (j < 6) { o0 = mfma(b0, pq, o0); SB(); }
            const float e2 = ex2(sa.z), e3 = ex2(sa.w); SB();
            if (j < 6) { o1 = mfma(b1, pq, o1); SB(); }
            const float e4 = ex2(sb.x), e5 = ex2(sb.y); SB();
            sa = mfma(ka, qf, cm); SB();
            const float e6 = ex2(sb.z), e7 = ex2(sb.w); SB();
            sb = mfma(kb, qf, cm); SB();
            pq = v4u{pk(e0, e1), pk(e2, e3), pk(e4, e5), pk(e6, e7)};
            b0 = a0; b1 = a1;
          } else {
            sa = mfma(ka, qf, cm); sb = mfma(kb, qf, cm);
          }
          a0 = n0; a1 = n1;
          SB();
        }
        o0 = mfma(b0, pq, o0); o1 = mfma(b1, pq, o1); SB();
#pragma unroll
        for (int r = 0; r < 4; ++r) sa[r] = (4 * g + r > c) ? sa[r] : NEG;
        const v4u pf = pexp(sa, sb);
        o1 = mfma(a1, pf, o1); o0 = mfma(a0, pf, o0);
      }
      den = xrow(sel3, o1.x);
      if (__builtin_expect(__any(!(den < 1.0e18f)), 0)) {
        float mt = m;
#pragma unroll 1
        for (int k = 0; k < 16; ++k) {
          v4f s = mfma(lrd(kw + k * 2048), qf, z4);
#pragma unroll
          for (int r = 0; r < 4; ++r) s[r] = (k > 0 || 4 * g + r > c) ? s[r] : NEG;
          mt = mx3(mt, mx(s.x, s.y), mx(s.z, s.w));
        }
        m = xmax1(mt);
        diag();
        const v4f cm = {-m, -m, -m, -m};
#pragma unroll 1
        for (int j = 7; j >= 0; --j) {
          v4f sa = mfma(lrd(kw + j * 4096), qf, cm);
          const v4f sb = mfma(lrd(kw + j * 4096 + 2048), qf, cm);
#pragma unroll
          for (int r = 0; r < 4; ++r) sa[r] = (j > 0 || 4 * g + r > c) ? sa[r] : NEG;
          const v4u pf = pexp(sa, sb);
          o0 = mfma(cat(trd(vr + j * 4096 + 1024), trd(vr + j * 4096 + 3072)), pf, o0);
          o1 = mfma(cat(trd(vr + j * 4096 + 1536), trd(vr + j * 4096 + 3584)), pf, o1);
        }
        den = xrow(sel3, o1.x);
      }
    }
    const unsigned ob = W_O + layer * 2097152 + t * 131072;
    {
      const float rd = __builtin_amdgcn_rcpf(den);
      o0 *= rd; o1 *= rd;
      v4u v = {pk(o0.x, o0.y), pk(o0.z, o0.w), pk(o1.x, o1.y), pk(o1.z, o1.w)};
      const v4u hs = imma(hwt, v, kc);
      v.z = g3 ? (hs.y << 13) + hs.x : v.z; v.w = g3 ? (hs.w << 13) + hs.z : v.w;
      __builtin_amdgcn_raw_buffer_store_b128(v, rs_ws, vl16, ob + (w * 16 + h) * 1024, SC1);
      keep(v);
    }
    __builtin_amdgcn_s_setprio(0);
    STAMP(8 + layer * 12);
    if (first) {
      const v4u xr = swap16(xres);
      res0 = v4f{asf(xr.x << 16), asf(xr.x & 0xffff0000u), asf(xr.y << 16), asf(xr.y & 0xffff0000u)};
      res1 = v4f{asf(xr.z << 16), asf(xr.z & 0xffff0000u), asf(xr.w << 16), asf(xr.w & 0xffff0000u)};
    }
    unsigned op = ob + (2 * (h >> 2) + (lead ? 0 : 1)) * 16384 + (w & 3) * 1024;
    asm volatile("" : "+s"(op));
    v4u of[4];
    if (isA && !(first && KVE && lead)) swap_kv(0, first && KVE ? 4 : 2);
    if (lead == isA && w == (isA ? 7 : 3)) *(LAS int*)(csg + 768) = lateness(oh, h, isA ? 165 : 125, key0, srdW, vrow, soh, budget);
    wg_bar();
    const int olate = __builtin_amdgcn_readfirstlane(*(LAS int*)(csg + 768));
    if (!isA && more) swap_kv(0, 2);
    pause(olate + (isA ? T_OA : T_OB) + (more ? 0 : 6));
#pragma unroll
    for (int i = 0; i < 4; ++i) of[i] = ld16(rs_ws, vl16, op + i * 4096);
    if (more && !first) swap_kv(2, 4);
    const unsigned qd = 7 * (w & 3) + (vl16 >> 8), qa = L_SCR + (lead ? 0 : 14336) + (qd >> 1) * 256 + (qd & 1) * 8 + (vl16 & 255);
    const lptr lo = lbase(qa), hi = lbase(g3 ? L_SCR + 32768 + vl16 : qa + 512);
    STAMP(9 + layer * 12);
    auto stage_o = [&]() INL {
      auto put = [&]() INL {
        v4u hs = zu;
        unsigned e0 = 0u - 4u * key0, e1 = 0u - 4u * key1;
#pragma unroll
        for (int i = 0; i < 4; ++i) {
          hs = imma(hwt, of[i], hs); SB();
          *(LAS v2u*)(lo + i * 3584) = v2u{of[i].x, of[i].y};
          *(LAS v2u*)(hi + i * 3584) = v2u{of[i].z, of[i].w};
          e0 += of[i].z; e1 += of[i].w; PIN(e0); PIN(e1);
          SB();
        }
        return row3_differs(hs, e0, e1);
      };
      if (__builtin_expect(put(), 0)) {
        FAIL(2);
        do reload4(of[0], of[1], of[2], of[3], srdW, vl16, op, op + 4096, op + 8192, op + 12288);
        while (put() && spend(budget));
      }
    };
    stage_o();
    wg_bar();
    if (first && !KVE) swap_kv(2, 4);
    const unsigned sx = W_HINT + (8 + layer) * 32768 + t * 2048;
    if (lead && w == 7) {
      if (more) {
        hint_put(8 + layer);
        __builtin_amdgcn_s_sleep(P_X);
        *(LAS int*)(csg + 832) = lateness(hint_get(vrow, sx), h, 70, key0, srdW, vrow, sx, budget);
      }
    }
    int w2 = w;
    asm volatile("" : "+s"(w2));
    if (!lead || w2 != 7) {
      const unsigned won = wos + (isA ? 28 * 14336 : 0);
      v4u bq[8];
#pragma unroll
      for (int i = 0; i < 4; ++i) { bq[2 * i] = lrd(sl16 + i * 1024); SB(); bq[2 * i + 1] = lrd(sl16 + 14336 + i * 1024); SB(); }
#pragma unroll
      for (int s = 0; s < 14; ++s) {
        const int i = (2 * s) & 7;
        if (!(s & 1)) { if (s < 12) LGKNOWN(4); else LGKNOWN(0); SB(); }
        res0 = mfma(wof[s], bq[i], res0); SB(); if (s + 4 < 14) { bq[i] = lrd(sl16 + (s + 4) * 1024); SB(); }
        res1 = mfma(wof[s], bq[i + 1], res1); SB(); if (s + 4 < 14) { bq[i + 1] = lrd(sl16 + 14336 + (s + 4) * 1024); SB(); }
        if (more && s >= 1 && (s - 1) % WOS == 0 && (s - 1) / WOS < 6) { const int n = (s - 1) / WOS; wof[n] = ld16(rs_ws, vl16 + (n & 3) * 1024, won + (n >> 2) * 4096); }
      }
      const v4u xo = swap16(v4u{pk(res0.x, res0.y), pk(res0.z, res0.w), pk(res1.x, res1.y), pk(res1.z, res1.w)});
      if (more) {
        __builtin_amdgcn_raw_buffer_store_b128(xo, rs_ws, pubx, W_X + (layer + 1) * TILEBUF + tp, SC1);
        keep(xo);
        auto r = __builtin_amdgcn_permlane32_swap(xo.x + xo.z, xo.y + xo.w, false, false);
        const unsigned r0 = r[0], r1 = r[1];
        *(LAS v2u*)(lbase(L_SCR + 28672 + w * 512 + (vl16 >> 1))) = v2u{r0 + r1, asu(sumsq8(xo))};
      } else {
        const unsigned lo = lane_id();
        __builtin_amdgcn_raw_buffer_store_b128(xo, __builtin_amdgcn_make_buffer_rsrc(p.out, 0, SEQ * DM * 2, 0x00020000),
                                               (lo & 31) * (DM * 2) + (2 * ft + (lo >> 5)) * 16, (t * 128 + (h >> 2) * 32) * (DM * 2), 0);
        keep(xo);
      }
    }
    STAMP(10 + layer * 12);
    if (more) {
      if (first) VMWAIT(7);
      wg_bar();
      const int late = __builtin_amdgcn_readfirstlane(*(LAS int*)(csg + 832));
      if (lead && w2 == 7) {
        float q = 0.f;
        unsigned a = 0u;
        const lptr sp = lbase(L_SCR + 28672 + (vl16 >> 1));
#pragma unroll
        for (int i = 0; i < 7; ++i) { const v2u v = *(LAS v2u*)(sp + i * 512); a += v.x; q += asf(v.y); }
        auto r = __builtin_amdgcn_permlane32_swap(asu(q), asu(q), false, false);
        auto ra = __builtin_amdgcn_permlane32_swap(a, a, false, false);
        const unsigned r0 = r[0], r1 = r[1], a0 = ra[0], a1 = ra[1], qs = asu(asf(r0) + asf(r1));
        const v4u rec = {qs, key0 - qs - a0, key1 - a1, 0u};
        __builtin_amdgcn_raw_buffer_store_b128(rec, rs_ws, vl16 < 512 ? (vl16 >> 8) * 1024 + (vl16 & 255) : OOB,
                                               W_ST + (layer + 1) * 131072 + (t * 8 + 2 * (h >> 2)) * 1024 + (h & 3) * 256, SC1);
        keep(rec);
        if (isA) hint_put(2 * layer + 2, max(late, 0));
        xwait = late + T_X - 12;
      } else {
        xwait = late + T_X;
      }
    }
  };
  auto finish = [&]() INL {
  };
  if (w_ >= 4) {
    run_layer(Layer<0>{}, Layer<1>{}); run_layer(Layer<1>{}, Layer<1>{}); run_layer(Layer<2>{}, Layer<1>{}); run_layer(Layer<3>{}, Layer<1>{});
    if (w_ == 7 && b == 0) __builtin_amdgcn_raw_buffer_store_b32(epoch, rs_ws, 0, W_KEY, 0);
#pragma unroll
    for (int s = 0; s < 14; ++s) asm volatile("" : "=v"(xf[s]));
    asm volatile("" : "=v"(k0), "=v"(k1), "=v"(v0), "=v"(v1), "=v"(ck0), "=v"(ck1), "=v"(cv0), "=v"(cv1), "=v"(var), "=v"(wphi[0]), "=v"(wphi[1]), "=v"(vres0));
  }
  int wz = w_;
  asm volatile("" : "+s"(wz));
  if (wz < 4) {
    run_layer(Layer<0>{}, Layer<0>{}); run_layer(Layer<1>{}, Layer<0>{}); run_layer(Layer<2>{}, Layer<0>{}); run_layer(Layer<3>{}, Layer<0>{});
  }
  finish();
}
}

extern "C" void kernel_launch(void* const* d_in, const int* in_sizes, int n_in, void* d_out, int out_size, void* d_ws, size_t ws_size,
                              hipStream_t stream) {
  (void)in_sizes; (void)n_in; (void)out_size; (void)ws_size;
  (void)hipFuncSetAttribute((const void*)fused_layers, hipFuncAttributeMaxDynamicSharedMemorySize, (int)L_TOTAL);
  Params p;
  p.x = d_in[0]; p.wA = d_in[1]; p.wphi = d_in[2]; p.woA = d_in[3]; p.wB = d_in[5]; p.woB = d_in[6]; p.out = d_out; p.ws = d_ws;
  hipLaunchKernelGGL(fused_layers, dim3(256), dim3(512), L_TOTAL, stream, p);
}
```

```cpp
#include <hip/hip_runtime.h>
#include <stdint.h>

#define LAS __attribute__((address_space(3)))
#define DEV __device__ __forceinline__
typedef unsigned v4u __attribute__((ext_vector_type(4)));
typedef unsigned v2u __attribute__((ext_vector_type(2)));
typedef float v4f __attribute__((ext_vector_type(4)));
typedef float v2f __attribute__((ext_vector_type(2)));
typedef int v4i __attribute__((ext_vector_type(4)));
typedef short v4s __attribute__((ext_vector_type(4)));
typedef __bf16 v8bf __attribute__((ext_vector_type(8)));
typedef __bf16 v2bf __attribute__((ext_vector_type(2)));
typedef __amdgpu_buffer_rsrc_t rsrc_t;

#define TPX 4
#define DLY_T 3
#define WO1 6
#define POLL_OB 1
#define T_KV 4
#define T_H 32
#define T_OA 10
#define T_OB 12
#define P_X 11
#define T_X 14
#define T_XL 0
#define T_OT 20
#define PRI0 1
#define RAMP 0
#define WOS 1
#define T_HL 4
#define P_F 440
#define BOFF 4
#define CODEB 60000
#define NW0 2
#define KVE 1
#define PBACK 99
#define ILV 1
#define WIN 5

namespace {
constexpr int SEQ = 2048, DM = 448;
constexpr unsigned L_WK = 0, L_WV = 28672, L_WQ = 57344, L_WQB = 86016, L_SCR = 114688, L_TOTAL = 163840;
constexpr unsigned L_CS = L_SCR + 23 * 2048;
constexpr unsigned L_KVF = L_SCR + 40960;
constexpr unsigned TILEBUF = 16 * 8 * 14 * 1024;
constexpr unsigned W_KEY = 0, W_HINT = 4096, W_FLAG = W_HINT + 11 * 32768, W_ST = W_HINT + 12 * 32768, W_KVP = W_ST + 4 * 131072, W_O = W_KVP + 2 * 1048576,
                   W_HALO = W_O + 4 * 2097152, W_X = W_HALO + 2 * 4194304, W_PT = W_X + 4 * TILEBUF, W_WO = W_PT + 2 * 16 * 86016,
                   W_CS = W_WO + 2 * 28 * 14336, W_END = W_CS + 16 * 56 * 128;
constexpr unsigned W_DBG = 64u << 20;
constexpr int SC1 = 16;
constexpr unsigned OOB = 0x40000000u;

DEV v4f mfma(v4u a, v4u b, v4f c) {
  return __builtin_amdgcn_mfma_f32_16x16x32_bf16(__builtin_bit_cast(v8bf, a), __builtin_bit_cast(v8bf, b), c, 0, 0, 0);
}
DEV unsigned pk(float a, float b) { v2f f = {a, b}; v2bf r = __builtin_convertvector(f, v2bf); return __builtin_bit_cast(unsigned, r); }
typedef LAS char* lptr;
DEV lptr lbase(unsigned v) { asm volatile("" : "+v"(v)); return (lptr)(size_t)v; }
DEV v2u trd(lptr a) { v4s r = __builtin_amdgcn_ds_read_tr16_b64_v4i16((LAS v4s*)a); return __builtin_bit_cast(v2u, r); }
DEV v4u cat(v2u a, v2u b) { return v4u{a.x, a.y, b.x, b.y}; }
DEV v4u lrd(lptr a) { return *(LAS v4u*)a; }
DEV void lwr(lptr a, v4u v) { *(LAS v4u*)a = v; }
#define VMWAIT(n) asm volatile("s_waitcnt vmcnt(" #n ")" ::: "memory")
DEV float asf(unsigned u) { return __builtin_bit_cast(float, u); }
DEV unsigned asu(float f) { return __builtin_bit_cast(unsigned, f); }
DEV void wg_bar() { asm volatile("s_waitcnt lgkmcnt(0)\n\ts_barrier" ::: "memory"); }
DEV void drain() { asm volatile("s_waitcnt vmcnt(0)" ::: "memory"); }
DEV void fence() { asm volatile("" ::: "memory"); }
DEV float ex2(float x) { return __builtin_amdgcn_exp2f(x); }

DEV unsigned lane_id() { unsigned r; asm volatile("v_mbcnt_lo_u32_b32 %0, -1, 0\n\tv_mbcnt_hi_u32_b32 %0, -1, %0" : "=v"(r)); __builtin_assume(r < 64); return r; }
DEV float mx(float a, float b) { return __builtin_elementwise_maximum(a, b); }
DEV float mn(float a, float b) { return __builtin_elementwise_minimum(a, b); }
DEV float mx3(float a, float b, float c) { return mx(mx(a, b), c); }
DEV float mn3(float a, float b, float c) { return mn(mn(a, b), c); }
DEV float xrow(float sel, float v) { const v4f z = {0.f, 0.f, 0.f, 0.f}; const v4f r = __builtin_amdgcn_mfma_f32_16x16x4f32(sel, v, z, 0, 0, 0); return r.x; }
DEV v4f xrow4(float sel, float v) { const v4f z = {0.f, 0.f, 0.f, 0.f}; return __builtin_amdgcn_mfma_f32_16x16x4f32(sel, v, z, 0, 0, 0); }
DEV v4u swap16(v4u v) {
  auto r = __builtin_amdgcn_permlane16_swap(v.x, v.z, false, false);
  auto q = __builtin_amdgcn_permlane16_swap(v.y, v.w, false, false);
  const unsigned r0 = r[0], r1 = r[1], q0 = q[0], q1 = q[1];
  return v4u{r0, q0, r1, q1};
}
DEV float sumsq8(v4u v) {
  const v4f lo = __builtin_bit_cast(v4f, v << 16), hi = __builtin_bit_cast(v4f, v & 0xffff0000u), q = lo * lo + hi * hi;
  return (q.x + q.y) + (q.z + q.w);
}
DEV void xmax2(float& a, float& b) {
  auto r = __builtin_amdgcn_permlane32_swap(asu(a), asu(b), false, false);
  unsigned r0 = r[0], r1 = r[1];
  float z = mx(asf(r0), asf(r1));
  auto q = __builtin_amdgcn_permlane16_swap(asu(z), asu(z), false, false);
  unsigned q0 = q[0], q1 = q[1];
  z = mx(asf(q0), asf(q1));
  auto p = __builtin_amdgcn_permlane32_swap(asu(z), asu(z), false, false);
  unsigned p0 = p[0], p1 = p[1];
  a = asf(p0); b = asf(p1);
}
DEV float xmax1(float a) {
  auto r = __builtin_amdgcn_permlane32_swap(asu(a), asu(a), false, false);
  unsigned r0 = r[0], r1 = r[1];
  float z = mx(asf(r0), asf(r1));
  auto q = __builtin_amdgcn_permlane16_swap(asu(z), asu(z), false, false);
  unsigned q0 = q[0], q1 = q[1];
  return mx(asf(q0), asf(q1));
}
DEV void dma2(unsigned ldsdst, unsigned va, unsigned vb, v4u srd, unsigned sa, unsigned sb) {
  asm volatile("s_mov_b32 m0, %0\n\ts_nop 3\n\tbuffer_load_dwordx4 %1, %3, %4 offen lds\n\tbuffer_load_dwordx4 %2, %3, %5 offen offset:1024 lds"
               :: "s"(ldsdst), "v"(va), "v"(vb), "s"(srd), "s"(sa), "s"(sb) : "memory");
}
DEV void keep(v4u v) { asm volatile("s_nop 1" :: "v"(v) : "memory"); }
DEV void keep(v4u v, v4u u) { asm volatile("s_nop 1" :: "v"(v), "v"(u) : "memory"); }
DEV void dma2s(unsigned ldsdst, unsigned v, v4u srd, unsigned so) {
  asm volatile("s_mov_b32 m0, %0\n\ts_nop 3\n\tbuffer_load_dwordx4 %1, %2, %3 offen sc1 lds\n\tbuffer_load_dwordx4 %1, %2, %3 offen offset:1024 sc1 lds"
               :: "s"(ldsdst), "v"(v), "s"(srd), "s"(so) : "memory");
}
DEV v4u ld16(rsrc_t rs, unsigned v, unsigned so) {
  const v4u r = __builtin_amdgcn_raw_buffer_load_b128(rs, v, so, SC1);
  __builtin_amdgcn_sched_barrier(0);
  return r;
}
DEV v4u mk_srd(const void* p, unsigned bytes) {
  unsigned long long a = (unsigned long long)p;
  return v4u{(unsigned)a, (unsigned)(a >> 32) & 0xffffu, bytes, 0x00020000u};
}
DEV v4u imma(v4u a, v4u b, v4u c) {
  const v4i r = __builtin_amdgcn_mfma_i32_16x16x64_i8(__builtin_bit_cast(v4i, a), __builtin_bit_cast(v4i, b), __builtin_bit_cast(v4i, c), 0, 0, 0);
  return __builtin_bit_cast(v4u, r);
}
DEV v4u weights(unsigned lo) {
  const unsigned a = (lo + 1) * 0x9e3779b1u, b = (a ^ (a >> 15)) * 0x85ebca77u, c = (b ^ (b >> 13)) * 0xc2b2ae3du, d = (c ^ (c >> 16)) * 0x27d4eb2fu;
  return v4u{(a ^ (a >> 16)) | 0x01010101u, (b ^ (b >> 16)) | 0x01010101u, (c ^ (c >> 16)) | 0x01010101u, (d ^ (d >> 16)) | 0x01010101u};
}
DEV void keys(unsigned e, unsigned& k0, unsigned& k1) { k0 = e * 0x9e3779b1u + 0x7f4a7c15u; k1 = e * 0x85ebca77u + 0x165667b1u; }
DEV unsigned xsum2u(unsigned a, unsigned b) {
  auto r = __builtin_amdgcn_permlane32_swap(a, b, false, false);
  const unsigned r0 = r[0], r1 = r[1], z = r0 + r1;
  auto q = __builtin_amdgcn_permlane16_swap(z, z, false, false);
  const unsigned q0 = q[0], q1 = q[1];
  return q0 + q1;
}
DEV void reload4(v4u& a, v4u& b, v4u& c, v4u& d, v4u srd, unsigned voff, unsigned sa, unsigned sb, unsigned sc, unsigned sd) {
  asm volatile("s_sleep 2\n\tbuffer_load_dwordx4 %0, %4, %5, %6 offen sc1\n\tbuffer_load_dwordx4 %1, %4, %5, %7 offen sc1\n\t"
               "buffer_load_dwordx4 %2, %4, %5, %8 offen sc1\n\tbuffer_load_dwordx4 %3, %4, %5, %9 offen sc1\n\ts_waitcnt vmcnt(0)"
               : "+v"(a), "+v"(b), "+v"(c), "+v"(d) : "v"(voff), "s"(srd), "s"(sa), "s"(sb), "s"(sc), "s"(sd) : "memory");
}
DEV bool spend(int& budget) { budget = max(budget - 1, 0); return budget != 0; }
DEV bool nonzero(unsigned v, unsigned long long lanes = ~0ull) { return (__builtin_amdgcn_uicmp(v, 0u, 33) & lanes) != 0ull; }
DEV bool row3_differs(v4u hs, unsigned e0, unsigned e1) {
  const unsigned long long m = __builtin_amdgcn_uicmp((hs.y << 13) + hs.x, e0, 33) | __builtin_amdgcn_uicmp((hs.w << 13) + hs.z, e1, 33);
  unsigned hi = (unsigned)(m >> 32);
  asm volatile("" : "+s"(hi));
  return (hi >> 16) != 0u;
}
DEV bool retry(bool bad, int& budget) {
  if (__builtin_expect(!__any(bad), 1)) return false;
  budget = max(budget - 1, 0);
  return __any(budget != 0);
}

DEV unsigned clock32() { unsigned long long t; asm volatile("s_memrealtime %0\n\ts_waitcnt lgkmcnt(0)" : "=s"(t) :: "memory"); return (unsigned)t; }
DEV int lateness(v2u v, int own, int seg, unsigned key, v4u srd, unsigned voff, unsigned soff, int& budget) {
  asm volatile("" : "+v"(v) :: "memory");
  int over = 0;
  if (__builtin_expect(nonzero(v.y - key, 0xffffull), 0)) {
    do {
      asm volatile("s_sleep %4\n\tbuffer_load_dwordx2 %0, %1, %2, %3 offen sc1\n\ts_waitcnt vmcnt(0)" : "+v"(v) : "v"(voff), "s"(srd), "s"(soff), "n"(BOFF) : "memory");
    } while (nonzero(v.y - key, 0xffffull) && spend(budget));
    const unsigned tv = v.x;
    over = max((int)(clock32() - __builtin_amdgcn_readlane(tv, own)) - seg, 0);
  }
  const unsigned tv = v.x;
  int d = (int)(tv - __builtin_amdgcn_readlane(tv, own));
  d = max(d, __builtin_amdgcn_update_dpp(0, d, 0x128, 0xf, 0xf, false));
  d = max(d, __builtin_amdgcn_update_dpp(0, d, 0x124, 0xf, 0xf, false));
  d = max(d, __builtin_amdgcn_update_dpp(0, d, 0x122, 0xf, 0xf, false));
  d = max(d, __builtin_amdgcn_update_dpp(0, d, 0x121, 0xf, 0xf, false));
  return min(__builtin_amdgcn_readfirstlane(d), 1000) - over;
}
DEV void pause(int ticks) {
  asm volatile("s_cmp_lt_i32 %0, 1\n\ts_cbranch_scc1 2f\n1:\n\ts_sleep 1\n\ts_sub_i32 %0, %0, 4\n\ts_cmp_gt_i32 %0, 0\n\ts_cbranch_scc1 1b\n2:" : "+s"(ticks) :: "memory", "scc");
}

template <class F>
DEV void feature_map(v4u vf, const v4u (&wphi)[2], v4u& plus, v4u& minus, F mid) {
  const v4f z4 = {0.f, 0.f, 0.f, 0.f};
  const v4f p0 = mfma(wphi[0], vf, z4), p1 = mfma(wphi[1], vf, z4);
  float hi = mx3(mx3(mx3(p0.x, p0.y, p0.z), p0.w, p1.x), p1.y, p1.z);
  float lo = -mn3(mn3(mn3(p0.x, p0.y, p0.z), p0.w, p1.x), p1.y, p1.z);
  xmax2(hi, lo);
  mid();
  const v2f p01 = {p0.x, p0.y}, p23 = {p0.z, p0.w}, p45 = {p1.x, p1.y}, h2 = {hi, hi}, l2 = {lo, lo};
  v2f a01 = p01 - h2, a23 = p23 - h2, a45 = p45 - h2, b01 = p01 + l2, b23 = p23 + l2, b45 = p45 + l2;
  asm volatile("" : "+v"(a01), "+v"(a23), "+v"(a45), "+v"(b01), "+v"(b23), "+v"(b45));
  const v2f e01 = {ex2(a01.x), ex2(a01.y)}, e23 = {ex2(a23.x), ex2(a23.y)}, e45 = {ex2(a45.x), ex2(a45.y)};
  const float e6 = ex2(p1.z - hi);
  const v2f se = (e01 + e23) + e45;
  v4f te = xrow4(1.f, (se.x + e6) + se.y);
  __builtin_amdgcn_sched_barrier(0);
  const v2f f01 = {ex2(-b01.x), ex2(-b01.y)}, f23 = {ex2(-b23.x), ex2(-b23.y)}, f45 = {ex2(-b45.x), ex2(-b45.y)};
  const float f6 = ex2(-(p1.z + lo));
  const v2f sf = (f01 + f23) + f45;
  v4f tf = xrow4(1.f, (sf.x + f6) + sf.y);
  __builtin_amdgcn_sched_barrier(0);
  const float re = __builtin_amdgcn_rcpf(te.x);
  asm volatile("" :: "v"(te));
  const v2f x01 = e01 * re, x23 = e23 * re, x45 = e45 * re;
  plus = v4u{pk(x01.x, x01.y), pk(x23.x, x23.y), pk(x45.x, x45.y), pk(e6 * re, 0.f)};
  __builtin_amdgcn_sched_barrier(0);
  const float rf = __builtin_amdgcn_rcpf(tf.x);
  asm volatile("" :: "v"(tf));
  const v2f y01 = f01 * rf, y23 = f23 * rf, y45 = f45 * rf;
  minus = v4u{pk(y01.x, y01.y), pk(y23.x, y23.y), pk(y45.x, y45.y), pk(f6 * rf, 0.f)};
}

DEV v4u pexp(v4f a, v4f b) {
  return v4u{pk(ex2(a.x), ex2(a.y)), pk(ex2(a.z), ex2(a.w)), pk(ex2(b.x), ex2(b.y)), pk(ex2(b.z), ex2(b.w))};
}
#define VMKNOWN(n) __builtin_amdgcn_s_waitcnt(((n) & 15) | (((n) >> 4) << 14) | 0xf70)
#define LGKNOWN(n) __builtin_amdgcn_s_waitcnt(0xc07f | ((n) << 8))
#define PIN(x) asm volatile("" : "+v"(x))
#define SB() __builtin_amdgcn_sched_barrier(0)
#define INL __attribute__((always_inline))

#define STAMP(i) do {} while (0)

template <int N> struct Layer { static constexpr int value = N; };
struct Params {
  const void* x; const void* wA; const void* wphi; const void* woA; const void* wB; const void* woB; void* out; void* ws;
};

__global__ void __launch_bounds__(512, 2) fused_layers(Params) {
  typedef unsigned long long v8p __attribute__((ext_vector_type(8)));
  v8p args;
  asm volatile("s_load_dwordx16 %0, %1, 0x0" : "=s"(args) : "s"(__builtin_amdgcn_kernarg_segment_ptr()));
  unsigned long long born;
  asm volatile("s_memrealtime %0" : "=s"(born));
  const int b = blockIdx.x, xg = b & 7, xi = b >> 3;
  constexpr int HPX = 32 / TPX, NGT = 16 / TPX;
  const int t = (xg % NGT) * TPX + xi / HPX;
  const int h = (xg / NGT) * HPX + xi % HPX;
  const int wid = __builtin_amdgcn_readfirstlane(threadIdx.x >> 6) & 7;
  const int w_ = wid < 4 ? 7 - wid : wid - 4;
  const int l = threadIdx.x & 63, c = l & 15, g = l >> 4;
  const bool g3 = g == 3;
  STAMP(0);
  constexpr unsigned L_XS = 73728, L_JOB = L_WQ, L_CSP = L_XS + 81920;
  const unsigned chunk16 = ((l & 3) ^ ((l >> 5) << 1)) * 16, chunk56 = chunk16 == 48 ? 40 : chunk16, vqkv = (l >> 2) * (3 * DM * 2) + chunk56;
  const int kvp_ = (w_ >> 1) & 1, kvs = w_ & 1;
  const unsigned kvat = kvp_ * L_WV + kvs * 2048, kvso = kvs * (32 * 3 * DM * 2) + (kvp_ + 1) * DM * 2 + 56 * h;
  const unsigned xsb = L_XS + w_ * 10240, xso = (t * 128 + w_ * 16) * (DM * 2);
  const unsigned vx0 = (l >> 3) * (DM * 2) + ((l & 7) ^ (l >> 4)) * 16, vx1 = ((l >> 3) + 8) * (DM * 2) + ((l & 7) ^ (4 + (l >> 4))) * 16 - 1024;
  const bool job = t < 14 && (w_ < 4 || (w_ < 6 && h < 14));
  const unsigned jat = L_JOB + w_ * 2048;
  asm volatile("" :: "v"(vqkv), "v"(vx0), "v"(vx1), "s"(kvat), "s"(kvso), "s"(xsb), "s"(xso), "s"(jat));
  asm volatile("s_waitcnt lgkmcnt(0)" : "+s"(args), "+s"(born));
  const Params p = {(const void*)args[0], (const void*)args[1], (const void*)args[2], (const void*)args[3], (const void*)args[4], (const void*)args[5], (void*)args[6], (void*)args[7]};
  const v4u srdA = mk_srd(p.wA, DM * 3 * DM * 2), srdX = mk_srd(p.x, SEQ * DM * 2);
  auto kvblock = [&](int j) INL { const unsigned so = kvso + j * (64 * 3 * DM * 2); dma2(kvat + j * 4096, vqkv, vqkv, srdA, so, so + 16 * 3 * DM * 2 - 1024); };
  auto xline = [&](int j, int up) INL {
    const unsigned so = xso + up * (64 * DM * 2) + j * 128;
    dma2(xsb + up * 40960 + (j % 5) * 2048, vx0, vx1, srdX, so, so);
  };
  if (w_ < 4) {
    __builtin_amdgcn_s_setprio(PRI0);
    const unsigned so = t * (32 * 3 * DM * 2) + (w_ == 3 ? 0 : w_ * DM * 2) + 56 * (h ^ 1);
    if (job) dma2(jat, vqkv, vqkv, mk_srd(w_ ? p.wB : p.wA, DM * 3 * DM * 2), so, so + 16 * 3 * DM * 2 - 1024);
    xline(0, 0); xline(0, 1);
  } else {
    if (job) {
      const unsigned v = (l >> 2) * (DM * 2) + chunk16, so = t * (32 * DM * 2) + 64 * h;
      dma2(jat, v, v, mk_srd(w_ == 5 ? p.woB : p.woA, DM * DM * 2), so, so + 16 * DM * 2 - 1024);
    } else if (w_ == 7) {
      const unsigned e = (l >> 2) & 7, g0 = l >> 5, d0 = e < 4 ? 4 * g0 + e : 12 + 3 * g0 + e, d1 = d0 + (e < 4 ? 8 : 6);
      dma2(jat, e < 7 ? d0 * 56 + chunk56 + 1024 : OOB, e < 7 ? d1 * 56 + chunk56 : OOB,
           mk_srd((const char*)p.wphi - 1024, 16 * 28 * 28 * 2 + 1024), h * 28 * 28 * 2, h * 28 * 28 * 2);
    }
    kvblock(0); kvblock(1);
  }
  unsigned codepf = 0;
  unsigned long long pc;
  asm volatile("s_getpc_b64 %0" : "=s"(pc));
  if (w_ == 6) {
    const unsigned off = (l * 32 + xi) * 128;
    asm volatile("s_nop 4\n\tbuffer_load_dword %0, %1, %2, 0 offen" : "=v"(codepf) : "v"(off), "s"(mk_srd((const void*)pc, CODEB)) : "memory");
  }
  unsigned epoch, key0, key1, keyk;
  asm volatile("s_load_dword %0, %1, 0x0" : "=s"(epoch) : "s"(p.ws));
  const rsrc_t rs_ws = __builtin_amdgcn_make_buffer_rsrc(p.ws, 0, W_END, 0x00020000);
  const v4u srdW = mk_srd(p.ws, W_END);
  unsigned vl16 = l * 16;
  asm volatile("" : "+v"(vl16));
  const int ft = 7 * (h & 3) + min(w_, 6), nx = 2 * ft + (g >> 1);
  const unsigned pubx = w_ == 7 ? OOB : (g & 1) * 14336 + (nx >> 2) * 1024 + ((nx & 3) * 16 + c) * 16;
  const unsigned tw = (t * 8 + w_) * 14336, tp = (t * 8 + 2 * (h >> 2)) * 14336;
  const unsigned ptA = W_PT + h * 86016 + w_ * 2048;
  int budget = 1 << 16;
  v4u wphi[2];
  v4u park[8];
  v4u xf[14];
  v4f k0, k1, v0, v1, ck0, ck1, cv0, cv1;
  float var;
  v4f res0, res1;
  auto hint_put = [&](int slot, int ahead = 0) INL {
    __builtin_amdgcn_raw_buffer_store_b64(v2u{clock32() + ahead, key0}, rs_ws, vl16 < 256 ? vl16 >> 1 : OOB, W_HINT + slot * 32768 + (t * 16 + h) * 128, SC1);
  };
  auto hint_get = [&](unsigned v, unsigned so) INL { SB(); const v2u r = __builtin_amdgcn_raw_buffer_load_b64(rs_ws, v, so, SC1); SB(); return r; };
  const unsigned q4 = (l & 15) >> 2, p4 = l & 3;
  const lptr l16 = (lptr)(size_t)vl16, lq16 = lbase(L_WQ + l * 16);
  const lptr stw = lbase(L_SCR + c * 32 + ((g ^ (c >> 2)) * 8));
  const lptr str = lbase(L_SCR + (4 * g + q4) * 32 + ((p4 ^ g) * 8));
  const lptr sl16 = lbase(L_SCR + l * 16), csg = lbase(L_CS + g * 16);
  const unsigned vcol = l < 16 ? l * 2048 : OOB, vrow = l < 16 ? l * 128 : OOB;
  const float sel3 = g3 ? 1.f : 0.f;
  v4u hwt = weights(l);
  if (g3) { hwt.z = 0u; hwt.w = 0u; }
  const v4u zu = {0u, 0u, 0u, 0u};

  v4u wof[14];
  const unsigned wos = W_WO + ft * 14336;
  auto kv_gemm = [&](const int s0, const int s1, auto step) INL {
    const v4f z4 = {0.f, 0.f, 0.f, 0.f};
    if (s0 == 0) { k0 = z4; k1 = z4; v0 = z4; v1 = z4; }
    v4u a[8];
#pragma unroll
    for (int i = 0; i < 2; ++i) { const int o = (s0 + i) * 2048; a[4 * i] = lrd(l16 + o); a[4 * i + 1] = lrd(l16 + o + 1024); a[4 * i + 2] = lrd(l16 + L_WV + o); a[4 * i + 3] = lrd(l16 + L_WV + o + 1024); SB(); }
#pragma unroll
    for (int s = s0; s < s1; ++s) {
      const int i = 4 * (s & 1), o = (s + 2) * 2048;
      if (s < s1 - 1) LGKNOWN(4); else LGKNOWN(0);
      SB();
      k1 = mfma(a[i + 1], xf[s], k1); SB();
      step(s);
      if (s + 2 < s1) { a[i + 1] = lrd(l16 + o + 1024); SB(); }
      k0 = mfma(a[i], xf[s], k0); SB(); if (s + 2 < s1) { a[i] = lrd(l16 + o); SB(); }
      v0 = mfma(a[i + 2], xf[s], v0); SB(); if (s + 2 < s1) { a[i + 2] = lrd(l16 + L_WV + o); SB(); }
      v1 = mfma(a[i + 3], xf[s], v1); SB(); if (s + 2 < s1) { a[i + 3] = lrd(l16 + L_WV + o + 1024); SB(); }
    }
  };
  v4u hwk = weights(l);
  hwk.w &= 0xffffu;
  asm volatile("" : "+v"(hwk.z), "+v"(hwk.w), "+v"(hwt));
  v4u fl0, fl1;
  auto flags_wait = [&](v2u f, unsigned v, unsigned so, unsigned long long lanes) INL {
    while (nonzero((f.x ^ key0) | (f.y ^ key1), lanes) && spend(budget))
      asm volatile("s_sleep 3\n\tbuffer_load_dwordx2 %0, %1, %2, %3 offen sc1\n\ts_waitcnt vmcnt(0)" : "+v"(f) : "v"(v), "s"(srdW), "s"(so) : "memory");
  };
  unsigned vres0;
  {
    unsigned a0 = (8 * g + (c >> 2)) * 64 + ((((l & 3) >> 1) ^ ((g & 1) << 1)) * 16) + (l & 1) * 8;
    unsigned a1 = (a0 ^ 32) ^ ((l & 2) << 2);
    unsigned sl3 = c >= 12 ? 4 * c - 45 : c + ((c * 11) >> 5);
    const unsigned one = 0x3f803f80u;
    v4u one8 = {one, one, one, one};
    const v4f z4 = {0.f, 0.f, 0.f, 0.f};
    auto turn = [&](lptr r0, lptr r1, bool pad, v4u& f0, v4u& f1) INL {
      f0 = cat(trd(r0), trd(r0 + 256)); f1 = cat(trd(r1), trd(r1 + 256));
      f1 = v4u{pad ? one : f1.x, pad ? one : f1.y, pad ? one : f1.z, pad ? one : f1.w};
    };
    const unsigned xa = xsb + c * 128 + (g ^ (c >> 1)) * 16;
    const lptr xa0 = lbase(xa), xa1 = lbase(xa ^ 64), cp = lbase(L_CSP + g * 16);
    unsigned vres = (t * 128 + (2 * (h >> 2) + (g & 1)) * 16 + c) * (DM * 2) + nx * 16;
    unsigned pubx_ = pubx, vcol_ = vcol, vrow_ = vrow;
    float dg0 = c == 4 * g ? 1.f : 0.f, dg1 = c == 4 * g + 1 ? 1.f : 0.f, dg2 = c == 4 * g + 2 ? 1.f : 0.f, dg3 = c == 4 * g + 3 ? 1.f : 0.f;
    asm volatile("" : "+v"(vres), "+v"(a0), "+v"(a1), "+v"(sl3), "+v"(one8), "+v"(dg0), "+v"(dg1), "+v"(dg2), "+v"(dg3) : "v"(pubx_), "v"(vcol_), "v"(vrow_), "v"(sel3));
    {
      const bool spread = w_ == 7 || (w_ < 4 && w_ != 2);
      const unsigned slot = spread ? sl3 : c;
      const lptr jr0 = lbase(jat + a0), jr1 = lbase(jat + ((w_ & 6) == 4 ? a0 ^ 32 : a1));
      unsigned vj1 = ((w_ & 6) == 4 ? 14336 : 1024) + (vl16 & 768) + slot * 16, vcs = g == 0 ? c * 4 : g == 1 ? 64 + slot * 4 : OOB;
      unsigned dst = !job ? W_END : w_ < 4 ? W_PT + ((w_ ? 16 : 0) + (h ^ 1)) * 86016 + ((w_ ? w_ - 1 : 2) * 14 + t) * 2048 : W_WO + ((w_ - 4) * 28 + 2 * h) * 14336 + t * 1024;
      unsigned dcs = (job && w_ < 4) ? W_CS + ((h ^ 1) * 56 + w_ * 14 + t) * 128 : W_END;
      asm volatile("" : "+v"(vj1), "+v"(vcs));
      asm volatile("" : "+s"(dst), "+s"(dcs));
      STAMP(51);
      if (w_ < 4) { xline(1, 0); xline(1, 1); } else { kvblock(2); kvblock(3); }
      asm volatile("s_waitcnt lgkmcnt(0)" : "+s"(epoch) :: "memory");
      epoch += 1u;
      keys(epoch, key0, key1);
      keyk = ((vl16 & 512) ? key1 : key0) >> ((vl16 & 256) ? 14 : 0);
      asm volatile("" : "+v"(keyk));
      VMWAIT(8);
      STAMP(47);
      v4u f0, f1;
      turn(jr0, jr1, spread && c >= 12, f0, f1);
      if (w_ == 7) { lwr(lq16 + 7 * 2048, f0); lwr(lbase(jat + vj1), f1); }
      __builtin_amdgcn_raw_buffer_store_b128(f0, rs_ws, vl16, dst, SC1);
      __builtin_amdgcn_raw_buffer_store_b128(f1, rs_ws, vj1, dst, SC1);
      keep(f0, f1);
      const v4f c0 = mfma(one8, f0, z4), c1 = mfma(one8, f1, z4);
      __builtin_amdgcn_raw_buffer_store_b32(asu(g ? c1.x : c0.x), rs_ws, vcs, dcs, SC1);
      fence();
      STAMP(52);
    }
    const unsigned kslot = kvp_ ? c : sl3;
    const lptr kr0 = lbase(kvat + a0), kr1 = lbase(kvat + a1), kw0 = lbase(kvat + vl16), kw1 = lbase(kvat + 1024 + (vl16 & 768) + kslot * 16);
    const lptr cw0 = lbase(L_CSP + (w_ & 3) * 128 + c * 4), cw1 = lbase(L_CSP + (w_ & 3) * 128 + 64 + kslot * 4);
    const bool kpad = !kvp_ && c >= 12;
    v4f cs0 = z4, cs1 = z4;
    auto kvturn = [&](int j) INL {
      v4u f0, f1;
      turn(kr0 + j * 4096, kr1 + j * 4096, kpad, f0, f1);
      lwr(kw0 + j * 4096, f0); lwr(kw1 + j * 4096, f1);
      cs0 = mfma(one8, f0, cs0); cs1 = mfma(one8, f1, cs1);
    };
    if (w_ >= 4) {
      VMWAIT(7); kvturn(0); kvturn(1);
      VMWAIT(3); kvturn(2); kvturn(3);
      kvblock(4); xline(4, 0); kvblock(5); kvblock(6);
      VMWAIT(8);
    } else {
      xline(2, 0); xline(2, 1); xline(3, 0); xline(3, 1);
      __builtin_amdgcn_s_setprio(0);
      xline(4, 0);
      VMWAIT(2);
    }
    asm volatile("" :: "v"(codepf));
    STAMP(48);
    wg_bar();
    STAMP(49);
#pragma unroll
    for (int j = 0; j < 2; ++j) { xf[2 * j] = lrd(xa0 + j * 2048); xf[2 * j + 1] = lrd(xa1 + j * 2048); }
    if (w_ == 6) __builtin_amdgcn_raw_buffer_store_b64(v2u{key0, key1}, rs_ws, vl16 < 256 ? vl16 >> 1 : OOB, W_FLAG + (t * 16 + h) * 128, SC1);
    v4f gram = z4, pa, pb;
    kv_gemm(0, 8, [&](int s) INL {
      gram = mfma(xf[s], xf[s], gram);
      if (s < 2) { xf[4 + 2 * s] = lrd(xa0 + (2 + s) * 2048); xf[5 + 2 * s] = lrd(xa1 + (2 + s) * 2048); }
      if (s == 1) xline(5, 0);
      if (s == 3) xline(6, 0);
      if (s == 5) { wphi[0] = lrd(lq16 + 7 * 2048); wphi[1] = lrd(lq16 + 7 * 2048 + 1024); }
      SB();
    });
    STAMP(50);
    v2u fc;
    const unsigned vfl = w_ == 7 ? (vl16 & 240) * 128 : (vl16 & 240) * 128 + (vl16 >> 8) * 128, sfl = W_FLAG + (w_ == 7 ? h ^ 1 : (7 * (h & 3)) >> 1) * 128;
    const bool now = w_ >= 6 && (int)(clock32() - (unsigned)born) >= P_F;
    if (now) asm volatile("s_nop 4\n\tbuffer_load_dwordx2 %0, %1, %2, %3 offen sc1\n\ts_waitcnt vmcnt(1)" : "=&v"(fc) : "v"(vfl), "s"(srdW), "s"(sfl) : "memory");
    else drain();
    if (w_ >= 4) { kvturn(4); kvturn(5); kvturn(6); *(LAS float*)(cw0) = cs0.x; *(LAS float*)(cw1) = cs1.x; }
#pragma unroll
    for (int j = 4; j < 7; ++j) { xf[2 * j] = lrd(xa0 + (j % 5) * 2048); xf[2 * j + 1] = lrd(xa1 + (j % 5) * 2048); }
    if (w_ >= 6) {
      asm volatile("s_waitcnt vmcnt(0)" : "+v"(fc) :: "memory");
      if (!now) { pause(P_F - 8 - (int)(clock32() - (unsigned)born)); fc = v2u{~key0, 0u}; }
      flags_wait(fc, vfl, sfl, ~0ull);
    }
    wg_bar();
    STAMP(54);
    kv_gemm(8, 14, [&](int s) INL {
      if (s < 11) { gram = mfma(xf[2 * s - 8], xf[2 * s - 8], gram); gram = mfma(xf[2 * s - 7], xf[2 * s - 7], gram); }
      if (s == 8) {
        const unsigned so = w_ >= 4 ? W_CS + (h * 56 + 14 * (w_ & 3)) * 128 : W_END;
        fl0 = ld16(rs_ws, vl16, so); fl1 = ld16(rs_ws, vl16 < 768 ? vl16 + 1024 : OOB, so);
        dma2s(L_WQ + w_ * 2048, vl16, srdW, ptA + L_WQ);
      }
      if (s == 9 && w_ < 6) dma2s(L_WQ + 16384 + w_ * 2048, vl16, srdW, ptA + L_WQ + 16384);
      if (s >= 10) {
#pragma unroll
        for (int n = NW0 * (s - 10); n < NW0 * (s - 9); ++n) wof[n] = ld16(rs_ws, w_ == 7 ? OOB : vl16, wos + n * 1024);
      }
      if (s >= 9 && s < 13) { const v4f u = pa + pb; if (s == 9) ck0 = u; else if (s == 10) ck1 = u; else if (s == 11) cv0 = u; else cv1 = u; }
      if (s < 12) { const int o = ((s - 8) >> 1) * 256 + ((s - 8) & 1) * 64; pa = *(LAS v4f*)(cp + o); pb = *(LAS v4f*)(cp + o + 128); }
      if (s == 12) var = xrow(1.f / DM, (gram.x * dg0 + gram.y * dg1) + (gram.z * dg2 + gram.w * dg3));
      SB();
    });
    if (w_ == 6) { *(LAS v4f*)(csg) = ck0; *(LAS v4f*)(csg + 64) = ck1; *(LAS v4f*)(csg + 128) = cv0; *(LAS v4f*)(csg + 192) = cv1; }
    vres0 = vres;
  }
  STAMP(56);
  int xwait;
#define FAIL(k)
  v4u kc = {key0, 0u, key1, 0u};
  PIN(kc);
  auto run_layer = [&](auto kind, auto role) INL {
    constexpr int layer = decltype(kind)::value, rep = layer >> 1;
    constexpr bool isA = !(layer & 1), first = layer == 0, more = layer < 3, lead = decltype(role)::value;
    int w = w_;
    asm volatile("" : "+s"(w));
    __builtin_assume(lead ? (w >= 4 && w < 8) : (w >= 0 && w < 4));
    const v4f z4 = {0.f, 0.f, 0.f, 0.f};
    const unsigned wob = (lead && w == 7) ? W_END - 12288 : wos + (isA ? 0 : 28 * 14336), wov = (lead && w == 7) ? OOB : vl16;
    auto load_wo = [&](int s0, int s1) INL {
#pragma unroll
      for (int s = s0; s < s1; ++s) wof[s] = ld16(rs_ws, wov + (s & 3) * 1024, wob + (s >> 2) * 4096);
    };
    v2u hh;
    unsigned vh;
    const unsigned shh = W_HINT + 2 * layer * 32768 + (t - 2) * 2048 + h * 128;
    if (!isA && lead) { const unsigned lo = lane_id(); vh = lo >= 16 ? OOB : (lo < 2 && (int)lo < t) ? (1 - lo) * 2048 : 4096; }
    const lptr cs = csg + (isA ? 0 : 384);
    float rstd, nmr;
    auto stats = [&]() INL {
      const float mean = k1.w * (1.f / DM);
      rstd = __builtin_amdgcn_rsqf(mx(fmaf(-mean, mean, var), 0.f) + 1e-5f); nmr = -mean * rstd;
      PIN(rstd); PIN(nmr);
    };
    unsigned hp = t < 15 ? W_HALO + rep * 4194304 + h * 262144 + (t * 8 + w) * 2048 : W_END;
    if (!isA) asm volatile("" : "+s"(hp));
    if (!first) {
      unsigned xs[4], sts = W_ST + layer * 131072 + (t * 8 + w) * 1024;
#pragma unroll
      for (int i = 0; i < 4; ++i) { xs[i] = W_X + layer * TILEBUF + tw + i * 4096; asm volatile("" : "+s"(xs[i])); }
      asm volatile("" : "+s"(sts));
      if (!isA && lead) PIN(vh);
      pause(xwait);
      auto ldx = [&](int s) INL { xf[s] = ld16(rs_ws, vl16 + (s & 3) * 1024, xs[s >> 2]); };
      for (;;) {
#pragma unroll
        for (int s = 0; s < WIN; ++s) ldx(s);
        STAMP(2 + layer * 12);
        v4u st;
        unsigned c0 = 0u - key0, c1 = 0u - key1;
        kv_gemm(0, 14, [&](int s) INL {
          const int n0 = WIN + s + (s < RAMP ? s : RAMP), n1 = WIN + s + 1 + (s + 1 < RAMP ? s + 1 : RAMP);
#pragma unroll
          for (int n = n0; n < n1; ++n) {
            if (n < 14) ldx(n);
            if (n == 14) st = ld16(rs_ws, vl16, sts);
            if (n == 15 && !isA && lead) hh = hint_get(vh, w == 4 ? shh : W_END);
          }
          c0 += xf[s].x + xf[s].z; c1 += xf[s].y + xf[s].w; PIN(c0); PIN(c1);
          if (s == 13) { var = xrow(1.f / DM, asf(st.x)); c0 += st.x + st.y; c1 += st.z; PIN(c0); PIN(c1); }
          if (s == 12) { ck0 = *(LAS v4f*)(cs); ck1 = *(LAS v4f*)(cs + 64); cv0 = *(LAS v4f*)(cs + 128); cv1 = *(LAS v4f*)(cs + 192); }
          SB();
        });
        c0 = xsum2u(c0, c1);
        stats();
        if (__builtin_expect(!nonzero(c0), 1) || !spend(budget)) break;
        FAIL(0);
        __builtin_amdgcn_s_sleep(4);
      }
    }
    STAMP(3 + layer * 12);
    if (first) stats();
    auto ln7 = [&](v4f a0, v4f a1, v4f c0, v4f c1, float sc, float sh, float pad) INL {
      a0 = a0 * sc + c0 * sh;
      const v2f u = v2f{a1.x, a1.y} * sc + v2f{c1.x, c1.y} * sh;
      return v4u{pk(a0.x, a0.y), pk(a0.z, a0.w), pk(u.x, u.y), pk(fmaf(a1.z, sc, c1.z * sh), pad)};
    };
    auto q_gemm = [&](float sc, float pad) INL {
      v4f q0 = z4, q1 = z4, c0, c1;
      const lptr lq = lq16 + (isA ? 0 : L_WQB - L_WQ);
      v4u a[8];
#pragma unroll
      for (int i = 0; i < 8; ++i) { a[i] = lrd(lq + i * 1024); SB(); }
#pragma unroll
      for (int s = 0; s < 14; ++s) {
        const int i = (2 * s) & 7;
        if (!(s & 1)) { if (s < 12) LGKNOWN(4); else LGKNOWN(0); SB(); }
        q0 = mfma(a[i], xf[s], q0); SB(); if (s + 4 < 14) { a[i] = lrd(lq + (s + 4) * 2048); SB(); }
        q1 = mfma(a[i + 1], xf[s], q1); SB(); if (s + 4 < 14) { a[i + 1] = lrd(lq + (s + 4) * 2048 + 1024); SB(); }
        if (s == 12) { c0 = *(LAS v4f*)(cs + 256); c1 = *(LAS v4f*)(cs + 320); SB(); }
      }
      v4u r = ln7(q0, q1, c0, c1, rstd * sc, nmr * sc, pad);
      asm volatile("" : "+v"(r) :: "memory");
      return r;
    };
    constexpr float L2E = 1.4426950408889634f;
    const v4u kf = isA ? ln7(k0, k1, ck0, ck1, rstd * L2E, nmr * L2E, 0.f) : ln7(k0, k1, ck0, ck1, rstd, nmr, 0.f);
    auto v_pack = [&]() INL {
      const v4f a0 = v0 * rstd + cv0 * nmr, a1 = v1 * rstd + cv1 * nmr;
      return v4u{pk(a0.x, a0.y), pk(a0.z, a0.w), g3 ? 0x3f80u : pk(a1.x, a1.y), g3 ? 0u : pk(a1.z, a1.w)};
    };
    auto swap_kv = [&](int j0, int j1) INL {
#pragma unroll
      for (int j = j0; j < j1 && j < (lead ? 3 : 4); ++j) {
        const lptr a = lbase(w * 2048 + j * 16384 + vl16);
        v4u t0, t1;
        if (layer < 2) { t0 = lrd(a); t1 = lrd(a + 1024); }
        if (!first) { lwr(a, park[2 * j]); lwr(a + 1024, park[2 * j + 1]); }
        if (layer < 2) { park[2 * j] = t0; park[2 * j + 1] = t1; }
      }
      if (first) {
        asm volatile("s_waitcnt lgkmcnt(0)" ::: "memory");
#pragma unroll
        for (int j = j0; j < j1 && j < (lead ? 3 : 4); ++j) dma2s(w * 2048 + j * 16384, vl16, srdW, ptA + 16 * 86016 + j * 16384);
      }
    };
    v4u xres;
    v4f o0, o1;
    float den;
    const unsigned soh = W_HINT + (2 * layer + 1) * 32768 + t * 2048;
    v2u oh;

    if constexpr (isA) {
      if (lead && w == 7) hint_put(2 * layer);
      if (!first) { if (!lead) load_wo(6, 10); }
      else if (lead) {
        v4f cq = __builtin_bit_cast(v4f, fl0) + __builtin_bit_cast(v4f, fl1);
#pragma unroll
        for (int i = 0; i < 4; ++i) {
          float u = cq[i];
          u += asf(__builtin_amdgcn_update_dpp(0, asu(u), 0x128, 0xf, 0xf, false));
          cq[i] = xrow(1.f, u);
        }
        *(LAS v4f*)(lbase(L_CS + 256 + (w & 3) * 128 + (vl16 & 127))) = cq;
      }
      if (first) load_wo(4 * NW0, 4 * NW0 + 3);
      const v4u vf = v_pack();
      v4u pp, pm, qf;
      feature_map(kf, wphi, pp, pm, [&]() INL { SB(); if (first) load_wo(4 * NW0 + 3, 4 * NW0 + 6); else if (!lead) load_wo(10, 14); });
      {
        const lptr tb = stw + w * 3072;
        *(LAS v2u*)(tb) = v2u{pp.x, pp.y}; *(LAS v2u*)(tb + 512) = v2u{pp.z, pp.w};
        *(LAS v2u*)(tb + 1024) = v2u{pm.x, pm.y}; *(LAS v2u*)(tb + 1536) = v2u{pm.z, pm.w};
        *(LAS v2u*)(tb + 2048) = v2u{vf.x, vf.y}; *(LAS v2u*)(tb + 2560) = v2u{vf.z, vf.w};
      }
      if (first) VMWAIT(6);
      v4u hwk_ = {hwt.x, hwt.y, hwk.z, hwk.w};
      unsigned kvp = W_KVP + rep * 1048576 + h * 65536 + (w & 3) * 1024 + (lead ? t * 4096 : 0);
      const lptr pa = lbase((size_t)str + (w & 2) * 512), pb = lbase((size_t)str + 2048 + (w & 1) * 512);
      asm volatile("" : "+v"(hwk_));
      asm volatile("" : "+s"(kvp));
      wg_bar();
      STAMP(4 + layer * 12);
      auto residual = [&]() INL {
        xres = __builtin_amdgcn_raw_buffer_load_b128(__builtin_amdgcn_make_buffer_rsrc((void*)p.x, 0, SEQ * DM * 2, 0x00020000), vres0, 0, 0); SB();
      };
      if (lead) {
        __builtin_amdgcn_s_setprio(2);
        v4u bv[4], x0[4], x1[4];
#pragma unroll
        for (int kk = 0; kk < 4; ++kk) {
          bv[kk] = cat(trd(pb + kk * 6144), trd(pb + kk * 6144 + 3072));
          x0[kk] = cat(trd(pa + kk * 6144), trd(pa + kk * 6144 + 3072));
          x1[kk] = cat(trd(pa + kk * 6144 + 512), trd(pa + kk * 6144 + 3072 + 512));
          SB();
        }
        v4f d0 = z4, d1 = z4;
#pragma unroll
        for (int kk = 0; kk < 4; ++kk) { d0 = mfma(x0[kk], bv[kk], d0); d1 = mfma(x1[kk], bv[kk], d1); }
        v4u dv = {pk(d0.x, d0.y), pk(d0.z, d0.w), pk(d1.x, d1.y), pk(d1.z, 0.f)};
        const v4u hs = imma(hwk_, dv, zu);
        dv.w |= ((hs.x + keyk) & 0x3fffu) << 16;
        __builtin_amdgcn_raw_buffer_store_b128(dv, rs_ws, vl16, kvp, SC1);
        keep(dv);
        __builtin_amdgcn_s_setprio(0);
        STAMP(5 + layer * 12);
        if (first) {
          load_wo(4 * NW0 + 6, 14); residual();
#pragma unroll
          for (int j = 0; j < 4; ++j) if (j < 3 || w < 6) dma2s(L_WQB + (w & 3) * 2048 + j * 8192, vl16, srdW, ptA - 8192 + 16 * 86016 + L_WQ + j * 8192);
        } else load_wo(6, 14);
        qf = q_gemm(L2E, 0.f);
        feature_map(qf, wphi, pp, pm, []() {});
        PIN(pp); PIN(pm);
        if (first && KVE) swap_kv(0, 4);
      } else {
        const unsigned sh = W_HINT + 2 * layer * 32768 + h * 128;
        __builtin_amdgcn_s_setprio(1);
        __builtin_amdgcn_s_sleep(DLY_T);
        const v2u hv = hint_get(vcol, sh);
        if (first) { load_wo(4 * NW0 + 6, 14); residual(); }
        qf = q_gemm(L2E, 0.f);
        pause(lateness(hv, t, 150, key0, srdW, vcol, sh, budget) + T_KV);
        if (w == 0) hint_put(2 * layer + 1);
        fence();
        STAMP(6 + layer * 12);
        v4u kp[16];
#pragma unroll
        for (int i = 0; i < 8; ++i) kp[i] = ld16(rs_ws, vl16, kvp + i * 4096);
        feature_map(qf, wphi, pp, pm, [&]() INL {
          SB();
#pragma unroll
          for (int i = 8; i < 16; ++i) kp[i] = ld16(rs_ws, vl16, kvp + i * 4096);
        });
        PIN(pp); PIN(pm);
        const unsigned lo = lane_id();
        const unsigned bit = ((lo >> 4) == ((lo >> 2) & 3)) ? ((lo & 1) ? 0x3f800000u : 0x3f80u) : 0u;
        const unsigned u0 = (lo & 2) ? 0u : bit, u1 = (lo & 2) ? bit : 0u;
        const v4u sa = {u0, u1, 0u, 0u}, sb = {0u, 0u, u0, u1};
        const lptr kd = lbase((size_t)sl16 + (L_KVF - L_SCR) + w * 1024);
        auto add_up = [&](bool once) INL {
          v4f lo4 = z4, hi4 = z4;
          v4u hs = zu;
          unsigned ck = 0u - 16u * keyk;
          SB();
#pragma unroll
          for (int i = 0; i < 16; ++i) {
            hs = imma(hwk_, kp[i], hs); lo4 = mfma(sa, kp[i], lo4); hi4 = mfma(sb, kp[i], hi4);
            ck += kp[i].w >> 16; PIN(ck);
            SB();
          }
          const bool bad = nonzero((hs.x - ck) & 0x3fffu);
          SB();
          lwr(kd, v4u{pk(lo4.x, lo4.y), pk(lo4.z, lo4.w), pk(hi4.x, hi4.y), pk(hi4.z, 0.f)});
          return bad;
        };
        if (__builtin_expect(add_up(true), 0)) {
          FAIL(1);
          do {
#pragma unroll
            for (int i = 0; i < 16; i += 4) reload4(kp[i], kp[i + 1], kp[i + 2], kp[i + 3], srdW, vl16, kvp + i * 4096, kvp + (i + 1) * 4096, kvp + (i + 2) * 4096, kvp + (i + 3) * 4096);
          } while (add_up(false) && spend(budget));
        }
      }
      __builtin_amdgcn_s_setprio(0);
      wg_bar();
      STAMP(7 + layer * 12);
      v4u kv[4];
      kv[0] = lrd(sl16 + (L_KVF - L_SCR) + 1024); SB(); kv[1] = lrd(sl16 + (L_KVF - L_SCR) + 3072); SB();
      kv[2] = lrd(sl16 + (L_KVF - L_SCR)); SB(); kv[3] = lrd(sl16 + (L_KVF - L_SCR) + 2048);
      SB();
      if (lead) oh = hint_get(vrow, w == 7 ? soh : W_END);
      o1 = mfma(kv[0], pp, z4); o1 = mfma(kv[1], pm, o1); SB();
      o0 = mfma(kv[2], pp, z4); o0 = mfma(kv[3], pm, o0); SB();
      { const v4f e6 = {1e-6f, 1e-6f, 1e-6f, 1e-6f}, r = __builtin_amdgcn_mfma_f32_16x16x4f32(sel3, o1.x, e6, 0, 0, 0); den = r.x; }
    } else {
      const unsigned hb = W_HALO + rep * 4194304 + h * 262144;
      __builtin_amdgcn_raw_buffer_store_b128(kf, rs_ws, vl16, hp, SC1); SB();
      const v4u hsk = imma(hwt, kf, v4u{kf.z + key0, 0u, kf.w + key1, 0u}); SB();
      v4u vf = v_pack();
      const v4u hs_v = imma(hwt, vf, hsk); SB();
      const lptr kw = sl16 + w * 2048, vw = stw + w * 2048, vr = str + w * 2048;
      *(LAS v2u*)(vw + 32768 + 1024) = v2u{vf.x, vf.y}; *(LAS v2u*)(vw + 32768 + 1536) = v2u{vf.z, vf.w};
      if (!lead || w < 7) lwr(kw + 32768, kf);
      SB();
      vf.z = g3 ? (hs_v.y << 13) + hs_v.x : vf.z; vf.w = g3 ? (hs_v.w << 13) + hs_v.z : vf.w;
      __builtin_amdgcn_raw_buffer_store_b128(vf, rs_ws, vl16 + 1024, hp, SC1); keep(vf);
      STAMP(4 + layer * 12);
      v4u hk[2], hv[2];
      unsigned hs[2];
#pragma unroll
      for (int i = 0; i < 2; ++i) hs[i] = t - 2 + i >= 0 ? hb + ((t - 2 + i) * 8 + w) * 2048 : W_END;
      int hlate;
      auto fetch = [&](int margin) INL {
        pause(__builtin_amdgcn_readfirstlane(hlate) + margin);
#pragma unroll
        for (int i = 0; i < 2; ++i) { hk[i] = ld16(rs_ws, vl16, hs[i]); hv[i] = ld16(rs_ws, vl16 + 1024, hs[i]); }
      };
      if (lead && w == 4) *(LAS int*)(csg + 896) = lateness(hh, 2, 200, key0, srdW, vh, shh, budget);
      if (lead) load_wo(6, 10);
      wg_bar();
      hlate = *(LAS int*)(csg + 896);
      if (!lead) { fetch(T_H); __builtin_amdgcn_s_setprio(1); }
      const v4u qf = q_gemm(0.18898223650461357f * L2E, 1.f);
      if (lead) { fetch(T_HL); load_wo(10, 14); } else load_wo(6, 10);
      const float NEG = -3.0e38f;
      v4f sd = mfma(kf, qf, z4);
#pragma unroll
      for (int r = 0; r < 4; ++r) sd[r] = (4 * g + r <= c) ? sd[r] : NEG;
      float m = xmax1(mx(mx3(sd.x, sd.y, sd.z), sd.w));
      auto diag = [&]() INL {
        const v2u a0 = trd(vr + 32768 + 1024), a1 = trd(vr + 32768 + 1536);
        const v4f e = sd - m;
        const v2u pd = {pk(ex2(e.x), ex2(e.y)), pk(ex2(e.z), ex2(e.w))};
        typedef short v4h __attribute__((ext_vector_type(4)));
        o0 = __builtin_amdgcn_mfma_f32_16x16x16bf16_1k(__builtin_bit_cast(v4h, a0), __builtin_bit_cast(v4h, pd), z4, 0, 0, 0);
        o1 = __builtin_amdgcn_mfma_f32_16x16x16bf16_1k(__builtin_bit_cast(v4h, a1), __builtin_bit_cast(v4h, pd), z4, 0, 0, 0);
      };
      diag();
      auto take_halo = [&]() INL {
        STAMP(6 + layer * 12);
        const unsigned nv = min(t, 2);
        auto stage = [&]() INL {
          v4u hs_ = zu;
          unsigned e0 = 0u - nv * key0, e1 = 0u - nv * key1;
#pragma unroll
          for (int i = 0; i < 2; ++i) {
            const unsigned none = t - 2 + i < 0 ? 0xfe000000u : 0u;
            hs_ = imma(hwt, hk[i], hs_); SB();
            hs_ = imma(hwt, hv[i], hs_); SB();
            e0 += hv[i].z - hk[i].z; e1 += hv[i].w - hk[i].w; PIN(e0); PIN(e1);
            lwr(kw + i * 16384, v4u{hk[i].x, hk[i].y, hk[i].z, hk[i].w | none});
            *(LAS v2u*)(vw + i * 16384 + 1024) = v2u{hv[i].x, hv[i].y};
            *(LAS v2u*)(vw + i * 16384 + 1536) = v2u{g3 ? 0x3f80u : hv[i].z, g3 ? 0u : hv[i].w};
            SB();
          }
          return row3_differs(hs_, e0, e1);
        };
        if (__builtin_expect(stage(), 0)) {
          FAIL(1);
          do reload4(hk[0], hk[1], hv[0], hv[1], srdW, vl16, hs[0], hs[1], min(hs[0] + 1024, W_END), min(hs[1] + 1024, W_END));
          while (stage() && spend(budget));
        }
        wg_bar();
        STAMP(7 + layer * 12);
      };
      {
        const v4f cm = {-m, -m, -m, -m};
        v4f sa, sb; v4u a0, a1, b0, b1, pq;
#pragma unroll
        for (int j = 7; j >= 0; --j) {
          if (j >= 4 && (j < 6) == lead && (w >> 1) == 7 - j) take_halo();
          if (j == (lead ? 4 : 6)) VMKNOWN(4);
          if (!lead && j == 6 && w == 3) hint_put(2 * layer + 1);
          if (!lead && j <= 5 && j >= 4) load_wo(10 + 2 * (5 - j), 12 + 2 * (5 - j));
          if (!lead && j == PBACK) __builtin_amdgcn_s_setprio(0);
          if (!lead && j == POLL_OB) oh = hint_get(vrow, w == 3 ? soh : W_END);
          const v4u ka = lrd(kw + j * 4096), kb = lrd(kw + j * 4096 + 2048);
          const v4u n0 = cat(trd(vr + j * 4096 + 1024), trd(vr + j * 4096 + 3072)), n1 = cat(trd(vr + j * 4096 + 1536), trd(vr + j * 4096 + 3584));
          SB();
          if (j < 7) {
            const float e0 = ex2(sa.x), e1 = ex2(sa.y); SB();
            if (j < 6) { o0 = mfma(b0, pq, o0); SB(); }
            const float e2 = ex2(sa.z), e3 = ex2(sa.w); SB();
            if (j < 6) { o1 = mfma(b1, pq, o1); SB(); }
            const float e4 = ex2(sb.x), e5 = ex2(sb.y); SB();
            sa = mfma(ka, qf, cm); SB();
            const float e6 = ex2(sb.z), e7 = ex2(sb.w); SB();
            sb = mfma(kb, qf, cm); SB();
            pq = v4u{pk(e0, e1), pk(e2, e3), pk(e4, e5), pk(e6, e7)};
            b0 = a0; b1 = a1;
          } else {
            sa = mfma(ka, qf, cm); sb = mfma(kb, qf, cm);
          }
          a0 = n0; a1 = n1;
          SB();
        }
        o0 = mfma(b0, pq, o0); o1 = mfma(b1, pq, o1); SB();
#pragma unroll
        for (int r = 0; r < 4; ++r) sa[r] = (4 * g + r > c) ? sa[r] : NEG;
        const v4u pf = pexp(sa, sb);
        o1 = mfma(a1, pf, o1); o0 = mfma(a0, pf, o0);
      }
      den = xrow(sel3, o1.x);
      if (__builtin_expect(__any(!(den < 1.0e18f)), 0)) {
        float mt = m;
#pragma unroll 1
        for (int k = 0; k < 16; ++k) {
          v4f s = mfma(lrd(kw + k * 2048), qf, z4);
#pragma unroll
          for (int r = 0; r < 4; ++r) s[r] = (k > 0 || 4 * g + r > c) ? s[r] : NEG;
          mt = mx3(mt, mx(s.x, s.y), mx(s.z, s.w));
        }
        m = xmax1(mt);
        diag();
        const v4f cm = {-m, -m, -m, -m};
#pragma unroll 1
        for (int j = 7; j >= 0; --j) {
          v4f sa = mfma(lrd(kw + j * 4096), qf, cm);
          const v4f sb = mfma(lrd(kw + j * 4096 + 2048), qf, cm);
#pragma unroll
          for (int r = 0; r < 4; ++r) sa[r] = (j > 0 || 4 * g + r > c) ? sa[r] : NEG;
          const v4u pf = pexp(sa, sb);
          o0 = mfma(cat(trd(vr + j * 4096 + 1024), trd(vr + j * 4096 + 3072)), pf, o0);
          o1 = mfma(cat(trd(vr + j * 4096 + 1536), trd(vr + j * 4096 + 3584)), pf, o1);
        }
        den = xrow(sel3, o1.x);
      }
    }
    const unsigned ob = W_O + layer * 2097152 + t * 131072;
    {
      const float rd = __builtin_amdgcn_rcpf(den);
      o0 *= rd; o1 *= rd;
      v4u v = {pk(o0.x, o0.y), pk(o0.z, o0.w), pk(o1.x, o1.y), pk(o1.z, o1.w)};
      const v4u hs = imma(hwt, v, kc);
      v.z = g3 ? (hs.y << 13) + hs.x : v.z; v.w = g3 ? (hs.w << 13) + hs.z : v.w;
      __builtin_amdgcn_raw_buffer_store_b128(v, rs_ws, vl16, ob + (w * 16 + h) * 1024, SC1);
      keep(v);
    }
    __builtin_amdgcn_s_setprio(0);
    STAMP(8 + layer * 12);
    if (first) {
      const v4u xr = swap16(xres);
      res0 = v4f{asf(xr.x << 16), asf(xr.x & 0xffff0000u), asf(xr.y << 16), asf(xr.y & 0xffff0000u)};
      res1 = v4f{asf(xr.z << 16), asf(xr.z & 0xffff0000u), asf(xr.w << 16), asf(xr.w & 0xffff0000u)};
    }
    unsigned op = ob + (2 * (h >> 2) + (lead ? 0 : 1)) * 16384 + (w & 3) * 1024;
    asm volatile("" : "+s"(op));
    v4u of[4];
    if (isA && !(first && KVE && lead)) swap_kv(0, first && KVE ? 4 : 2);
    if (lead == isA && w == (isA ? 7 : 3)) *(LAS int*)(csg + 768) = lateness(oh, h, isA ? 165 : 125, key0, srdW, vrow, soh, budget);
    wg_bar();
    const int olate = __builtin_amdgcn_readfirstlane(*(LAS int*)(csg + 768));
    if (!isA && more) swap_kv(0, 2);
    pause(olate + (isA ? T_OA : T_OB) + (more ? 0 : 6));
#pragma unroll
    for (int i = 0; i < 4; ++i) of[i] = ld16(rs_ws, vl16, op + i * 4096);
    if (more && !first) swap_kv(2, 4);
    const unsigned qd = 7 * (w & 3) + (vl16 >> 8), qa = L_SCR + (lead ? 0 : 14336) + (qd >> 1) * 256 + (qd & 1) * 8 + (vl16 & 255);
    const lptr lo = lbase(qa), hi = lbase(g3 ? L_SCR + 32768 + vl16 : qa + 512);
    STAMP(9 + layer * 12);
    auto stage_o = [&]() INL {
      auto put = [&]() INL {
        v4u hs = zu;
        unsigned e0 = 0u - 4u * key0, e1 = 0u - 4u * key1;
#pragma unroll
        for (int i = 0; i < 4; ++i) {
          hs = imma(hwt, of[i], hs); SB();
          *(LAS v2u*)(lo + i * 3584) = v2u{of[i].x, of[i].y};
          *(LAS v2u*)(hi + i * 3584) = v2u{of[i].z, of[i].w};
          e0 += of[i].z; e1 += of[i].w; PIN(e0); PIN(e1);
          SB();
        }
        return row3_differs(hs, e0, e1);
      };
      if (__builtin_expect(put(), 0)) {
        FAIL(2);
        do reload4(of[0], of[1], of[2], of[3], srdW, vl16, op, op + 4096, op + 8192, op + 12288);
        while (put() && spend(budget));
      }
    };
    stage_o();
    wg_bar();
    if (first && !KVE) swap_kv(2, 4);
    const unsigned sx = W_HINT + (8 + layer) * 32768 + t * 2048;
    if (lead && w == 7) {
      if (more) {
        hint_put(8 + layer);
        unsigned again;
        asm volatile("s_nop 4\n\tbuffer_load_dword %0, %1, %2, 0 offen" : "=v"(again) : "v"(vl16 * 256 + xi * 128), "s"(mk_srd((const void*)pc, CODEB)) : "memory");
        __builtin_amdgcn_s_sleep(P_X);
        *(LAS int*)(csg + 832) = lateness(hint_get(vrow, sx), h, 70, key0, srdW, vrow, sx, budget);
        asm volatile("" :: "v"(again));
      }
    }
    int w2 = w;
    asm volatile("" : "+s"(w2));
    if (!lead || w2 != 7) {
      const unsigned won = wos + (isA ? 28 * 14336 : 0);
      v4u bq[8];
#pragma unroll
      for (int i = 0; i < 4; ++i) { bq[2 * i] = lrd(sl16 + i * 1024); SB(); bq[2 * i + 1] = lrd(sl16 + 14336 + i * 1024); SB(); }
#pragma unroll
      for (int s = 0; s < 14; ++s) {
        const int i = (2 * s) & 7;
        if (!(s & 1)) { if (s < 12) LGKNOWN(4); else LGKNOWN(0); SB(); }
        res0 = mfma(wof[s], bq[i], res0); SB(); if (s + 4 < 14) { bq[i] = lrd(sl16 + (s + 4) * 1024); SB(); }
        res1 = mfma(wof[s], bq[i + 1], res1); SB(); if (s + 4 < 14) { bq[i + 1] = lrd(sl16 + 14336 + (s + 4) * 1024); SB(); }
        if (more && s >= 1 && (s - 1) % WOS == 0 && (s - 1) / WOS < 6) { const int n = (s - 1) / WOS; wof[n] = ld16(rs_ws, vl16 + (n & 3) * 1024, won + (n >> 2) * 4096); }
      }
      const v4u xo = swap16(v4u{pk(res0.x, res0.y), pk(res0.z, res0.w), pk(res1.x, res1.y), pk(res1.z, res1.w)});
      if (more) {
        __builtin_amdgcn_raw_buffer_store_b128(xo, rs_ws, pubx, W_X + (layer + 1) * TILEBUF + tp, SC1);
        keep(xo);
        auto r = __builtin_amdgcn_permlane32_swap(xo.x + xo.z, xo.y + xo.w, false, false);
        const unsigned r0 = r[0], r1 = r[1];
        *(LAS v2u*)(lbase(L_SCR + 28672 + w * 512 + (vl16 >> 1))) = v2u{r0 + r1, asu(sumsq8(xo))};
      } else {
        const unsigned lo = lane_id();
        __builtin_amdgcn_raw_buffer_store_b128(xo, __builtin_amdgcn_make_buffer_rsrc(p.out, 0, SEQ * DM * 2, 0x00020000),
                                               (lo & 31) * (DM * 2) + (2 * ft + (lo >> 5)) * 16, (t * 128 + (h >> 2) * 32) * (DM * 2), 0);
        keep(xo);
      }
    }
    STAMP(10 + layer * 12);
    if (more) {
      if (first) VMWAIT(7);
      wg_bar();
      const int late = __builtin_amdgcn_readfirstlane(*(LAS int*)(csg + 832));
      if (lead && w2 == 7) {
        float q = 0.f;
        unsigned a = 0u;
        const lptr sp = lbase(L_SCR + 28672 + (vl16 >> 1));
#pragma unroll
        for (int i = 0; i < 7; ++i) { const v2u v = *(LAS v2u*)(sp + i * 512); a += v.x; q += asf(v.y); }
        auto r = __builtin_amdgcn_permlane32_swap(asu(q), asu(q), false, false);
        auto ra = __builtin_amdgcn_permlane32_swap(a, a, false, false);
        const unsigned r0 = r[0], r1 = r[1], a0 = ra[0], a1 = ra[1], qs = asu(asf(r0) + asf(r1));
        const v4u rec = {qs, key0 - qs - a0, key1 - a1, 0u};
        __builtin_amdgcn_raw_buffer_store_b128(rec, rs_ws, vl16 < 512 ? (vl16 >> 8) * 1024 + (vl16 & 255) : OOB,
                                               W_ST + (layer + 1) * 131072 + (t * 8 + 2 * (h >> 2)) * 1024 + (h & 3) * 256, SC1);
        keep(rec);
        if (isA) hint_put(2 * layer + 2, max(late, 0));
        xwait = late + T_X - 12;
      } else {
        xwait = late + T_X;
      }
    }
  };
  auto finish = [&]() INL {
  };
  if (w_ >= 4) {
    run_layer(Layer<0>{}, Layer<1>{}); run_layer(Layer<1>{}, Layer<1>{}); run_layer(Layer<2>{}, Layer<1>{}); run_layer(Layer<3>{}, Layer<1>{});
    if (w_ == 7 && b == 0) __builtin_amdgcn_raw_buffer_store_b32(epoch, rs_ws, 0, W_KEY, 0);
#pragma unroll
    for (int s = 0; s < 14; ++s) asm volatile("" : "=v"(xf[s]));
    asm volatile("" : "=v"(k0), "=v"(k1), "=v"(v0), "=v"(v1), "=v"(ck0), "=v"(ck1), "=v"(cv0), "=v"(cv1), "=v"(var), "=v"(wphi[0]), "=v"(wphi[1]), "=v"(vres0));
  }
  int wz = w_;
  asm volatile("" : "+s"(wz));
  if (wz < 4) {
    run_layer(Layer<0>{}, Layer<0>{}); run_layer(Layer<1>{}, Layer<0>{}); run_layer(Layer<2>{}, Layer<0>{}); run_layer(Layer<3>{}, Layer<0>{});
  }
  finish();
}
}

extern "C" void kernel_launch(void* const* d_in, const int* in_sizes, int n_in, void* d_out, int out_size, void* d_ws, size_t ws_size,
                              hipStream_t stream) {
  (void)in_sizes; (void)n_in; (void)out_size; (void)ws_size;
  (void)hipFuncSetAttribute((const void*)fused_layers, hipFuncAttributeMaxDynamicSharedMemorySize, (int)L_TOTAL);
  Params p;
  p.x = d_in[0]; p.wA = d_in[1]; p.wphi = d_in[2]; p.woA = d_in[3]; p.wB = d_in[5]; p.woB = d_in[6]; p.out = d_out; p.ws = d_ws;
  hipLaunchKernelGGL(fused_layers, dim3(256), dim3(512), L_TOTAL, stream, p);
}
```
